```python
import torch

def _softmax(x, axis=-1):
    return torch.softmax(x, dim=axis)
def _gelu(x):
    return torch.nn.functional.gelu(x, approximate="tanh")
def _silu(x):
    return torch.nn.functional.silu(x)
def _sigmoid(x):
    return torch.sigmoid(x)
def _layernorm(x, axis=-1, eps=1e-5):
    m = x.mean(dim=axis, keepdim=True)
    v = x.var(dim=axis, keepdim=True, unbiased=False)
    return (x - m) / torch.sqrt(v + eps)
def _scatter_add(tgt, idx, src, axis=0):
    return tgt.clone().index_add_(axis, idx, src)
def _segment_sum(src, seg, n):
    out = torch.zeros((n,) + src.shape[1:], dtype=src.dtype)
    return out.index_add_(0, seg, src)
def _pad(x, pw):
    flat = tuple(v for pair in reversed(pw) for v in pair)
    return torch.nn.functional.pad(x, flat)

kernel_name = 'gather_scatter__molecular_angular_gemnet__graph'

def setup_inputs():

    _w = (lambda a: (lambda t: t.to(torch.bfloat16) if t.is_floating_point() else t)(torch.as_tensor(a)))
    N, deg, De, L = 10000, 64, 4, 1
    E = N * deg
    T = E * deg
    NR = 8
    dist = torch.rand(E).to(float)
    angle = (torch.rand(T) * torch.pi).to(float)
    centers = torch.linspace(0.0, 1.0, NR).to(float)
    ang_freq = torch.arange(1, L + 1).to(float)
    return {
        "m": _w((lambda *s: torch.randn(*s, dtype=torch.bfloat16) * 0.05)(E, De)),
        "idx_in": _w((lambda lo, hi, *s: torch.randint(lo, hi, s))(0, E, T).to(torch.int64)),
        "idx_out": _w((lambda lo, hi, *s: torch.randint(lo, hi, s))(0, E, T).to(torch.int64)),
        "dist": _w(dist),
        "angle": _w(angle),
        "centers": _w(centers),
        "ang_freq": _w(ang_freq),
        "w_rbf": _w((lambda *s: torch.randn(*s, dtype=torch.bfloat16) * 0.2)(NR, De)),
        "w_sbf": _w((lambda *s: torch.randn(*s, dtype=torch.bfloat16) * 0.2)(NR * L, De)),
        "w_down": _w((lambda *s: torch.randn(*s, dtype=torch.bfloat16) * 0.2)(De, De)),
        "w_up": _w((lambda *s: torch.randn(*s, dtype=torch.bfloat16) * 0.2)(De, De)),
    }

def reference(m, idx_in, idx_out, dist, angle, centers, ang_freq, w_rbf, w_sbf, w_down, w_up):
    E, De = m.shape[0], m.shape[1]
    NR = centers.shape[0]
    L = ang_freq.shape[0]
    rbf = torch.exp(-10.0 * (dist[:, None] - centers[None, :]) ** 2)
    cbf = torch.cos(ang_freq[None, :] * angle[:, None])
    rbf_t = rbf[idx_out]
    sbf = torch.reshape(rbf_t[:, :, None] * cbf[:, None, :], (-1, NR * L))
    x = (m[idx_in] @ w_down) * (sbf @ w_sbf)
    agg = _segment_sum(x, idx_out, E)
    return m + (agg @ w_up) * (rbf @ w_rbf)
```

```cpp
#include <hip/hip_runtime.h>
#include <hip/hip_bf16.h>
#include <stdint.h>

typedef int v4i __attribute__((ext_vector_type(4)));
typedef unsigned v4u __attribute__((ext_vector_type(4)));
typedef unsigned v2u __attribute__((ext_vector_type(2)));
typedef int v4i_rs __attribute__((ext_vector_type(4)));
typedef __attribute__((address_space(3))) unsigned lds_u32;
typedef __attribute__((address_space(3))) v4u lds_v4u;

static __device__ __forceinline__ float bf2f(unsigned short v) { return __uint_as_float(((unsigned)v) << 16); }
static __device__ __forceinline__ unsigned f2bf_bits(float f) {
    unsigned u = __float_as_uint(f);
    u += 0x7FFF + ((u >> 16) & 1);
    return u >> 16;
}
static __device__ __forceinline__ float lo_bf(unsigned u) { return __uint_as_float(u << 16); }
static __device__ __forceinline__ float hi_bf(unsigned u) { return __uint_as_float(u & 0xFFFF0000u); }
static __device__ __forceinline__ v4i_rs mk_rsrc(const void* p, unsigned bytes) {
    unsigned long long a = (unsigned long long)p;
    v4i_rs r = {(int)(unsigned)a, (int)(unsigned)((a >> 32) & 0xFFFF), (int)bytes, 0x00020000};
    return r;
}
static __device__ __forceinline__ lds_u32* ldsp(unsigned byte) { return (lds_u32*)(size_t)byte; }

#define P1_THREADS 1024
#define PD 1
#define UNR ((PD & 1) ? 2 * PD : PD)
#define P1_GRID 256
#define NWS 256
#define CH (P1_THREADS * 4)
#define NT 1024
#define LMAX 20480
#define P2_THREADS 1024
#define OOB 0x80000000u
#define OBASE 20480
#define IDXB 4096
#define ST_AUX 16
#define LB 4096
#define LS 132
#define TINC 683
#define WL_OFF (LB + NT * LS)
#define MS_OFF (WL_OFF + 16 * 512)
#define BIN_LDS (MS_OFF + 128 + 320)
#define NSTAT 14
#define WM_CTR 1024
#define WM_BOX (WM_CTR + 128)
#define WM_LOC (WM_BOX + 256)
#define ORD 0
#define NCH ((unsigned)NR * P1_GRID)
#define CORD(c) (c)
#define NTH "nt"
#define OUT_AUX 16
#define PR_AUX 18
#define TCH 7
#define CZERO ((512u << 1) | (512u << 11) | (512u << 22))
#define FL_SIZE (2 * P1_GRID * 2)

struct Grp { v4i io; v4i ii; v2u an; };
static __device__ __forceinline__ void load_grp(Grp& r, v4i_rs ro, v4i_rs ri, v4i_rs ra, unsigned c, unsigned vo16, unsigned vo8) {
    asm volatile("buffer_load_dwordx4 %0, %1, %2, %3 offen nt" : "=v"(r.io) : "v"(vo16), "s"(ro), "s"(c * (CH * 4)));
    asm volatile("buffer_load_dwordx4 %0, %1, %2, %3 offen nt" : "=v"(r.ii) : "v"(vo16), "s"(ri), "s"(c * (CH * 4)));
    asm volatile("buffer_load_dwordx2 %0, %1, %2, %3 offen nt" : "=v"(r.an) : "v"(vo8), "s"(ra), "s"(c * (CH * 2)));
}
#define TELLC ((rd & 6) == 0)
#define WAIT_GRP(n, g) asm volatile("s_waitcnt vmcnt(%3)" : "+v"((g).io), "+v"((g).ii), "+v"((g).an) : "n"(n))

static __device__ __forceinline__ unsigned wave_scan_max(unsigned x) {
    x = max(x, (unsigned)__builtin_amdgcn_update_dpp(0, (int)x, 0x111, 0xF, 0xF, false));
    x = max(x, (unsigned)__builtin_amdgcn_update_dpp(0, (int)x, 0x112, 0xF, 0xF, false));
    x = max(x, (unsigned)__builtin_amdgcn_update_dpp(0, (int)x, 0x114, 0xF, 0xF, false));
    x = max(x, (unsigned)__builtin_amdgcn_update_dpp(0, (int)x, 0x118, 0xF, 0xF, false));
    x = max(x, (unsigned)__builtin_amdgcn_update_dpp(0, (int)x, 0x142, 0xA, 0xF, false));
    x = max(x, (unsigned)__builtin_amdgcn_update_dpp(0, (int)x, 0x143, 0xC, 0xF, false));
    return x;
}
static __device__ __forceinline__ unsigned row_scan_add(unsigned x) {
    x += (unsigned)__builtin_amdgcn_update_dpp(0, (int)x, 0x111, 0xF, 0xF, false);
    x += (unsigned)__builtin_amdgcn_update_dpp(0, (int)x, 0x112, 0xF, 0xF, false);
    x += (unsigned)__builtin_amdgcn_update_dpp(0, (int)x, 0x114, 0xF, 0xF, false);
    x += (unsigned)__builtin_amdgcn_update_dpp(0, (int)x, 0x118, 0xF, 0xF, false);
    return x;
}
static __device__ __forceinline__ unsigned wave_scan_add(unsigned x) {
    x = row_scan_add(x);
    x += (unsigned)__builtin_amdgcn_update_dpp(0, (int)x, 0x142, 0xA, 0xF, false);
    x += (unsigned)__builtin_amdgcn_update_dpp(0, (int)x, 0x143, 0xC, 0xF, false);
    return x;
}

static __device__ __forceinline__ float wave_scan_addf(float x) {
    x += __int_as_float(__builtin_amdgcn_update_dpp(0, __float_as_int(x), 0x111, 0xF, 0xF, false));
    x += __int_as_float(__builtin_amdgcn_update_dpp(0, __float_as_int(x), 0x112, 0xF, 0xF, false));
    x += __int_as_float(__builtin_amdgcn_update_dpp(0, __float_as_int(x), 0x114, 0xF, 0xF, false));
    x += __int_as_float(__builtin_amdgcn_update_dpp(0, __float_as_int(x), 0x118, 0xF, 0xF, false));
    x += __int_as_float(__builtin_amdgcn_update_dpp(0, __float_as_int(x), 0x142, 0xA, 0xF, false));
    x += __int_as_float(__builtin_amdgcn_update_dpp(0, __float_as_int(x), 0x143, 0xC, 0xF, false));
    return x;
}

struct Lay { unsigned K0, KX, dumu, fcap; };
#define PLANE (NWS * NT)
#define SWZ(t, w) ((t) ^ (((w) ^ ((w) >> 4) ^ ((w) << 4)) & 1023))
static __device__ __forceinline__ unsigned blk_line(unsigned w) { return (((w & 3) | ((w >> 3) << 2)) << 11) | ((w & 4) << 4); }
static __device__ __forceinline__ unsigned tile_line(unsigned t, unsigned w) { const unsigned x = SWZ(t, w); return (x & 63) | ((x >> 6) << 7); }
static __device__ __forceinline__ unsigned frag_unit(unsigned w, const Lay& ly) { return PLANE * 8 - (w + 1) * ly.fcap; }
static __device__ __forceinline__ char* rec_base(const char* p) { return (char*)p + ((int)(-(unsigned)(size_t)p & 0x1FFFFF) - PLANE * 128); }

static __device__ __forceinline__ void conv_rec(unsigned io, unsigned ii, float ang, float frev, unsigned& A, unsigned& t4, unsigned& cq, unsigned& seen) {
    unsigned x, y;
    asm("v_and_b32 %0, 31, %4\n v_and_b32 %1, 0xf80, %3\n v_lshl_or_b32 %2, %0, 2, %1" : "=&v"(x), "=&v"(y), "=v"(t4) : "v"(io), "v"(ii));
    asm("v_lshrrev_b32 %0, 5, %3\n v_lshrrev_b32 %1, 3, %4\n v_bfi_b32 %0, %5, %3, %0\n v_add_u32 %1, %6, %1\n v_lshl_add_u32 %2, %0, 17, %1"
        : "=&v"(x), "=&v"(y), "=v"(A) : "v"(io), "v"(ii), "s"(127u), "n"(IDXB << 17));
    float c = __builtin_amdgcn_cosf(__builtin_amdgcn_fractf(frev * ang));
    const unsigned cb = __float_as_uint(__builtin_fmaf(c, 511.0f, 12583424.0f));
    seen |= cb;
    cq = cb & 0x3FFu;
}
#define PADV 1
static __device__ __forceinline__ unsigned pad_rec(unsigned q) { return PADV == 0 ? 0x7FFFu << 17 : PADV == 1 ? (0x7FFFu - (q & 511)) << 17 : (IDXB + (q & 511)) << 17; }

static __device__ __forceinline__ void prep_md(const v2u* mv, unsigned dst, const unsigned short* w_down, unsigned* mdp, unsigned plane, unsigned* mx, float* sq) {
    float wd[16];
#pragma unroll
    for (int i = 0; i < 8; i++) { unsigned u = ((const __attribute__((address_space(4))) unsigned*)w_down)[i]; wd[2 * i] = lo_bf(u); wd[2 * i + 1] = hi_bf(u); }
    unsigned o[2][4];
#pragma unroll
    for (int j = 0; j < 4; j++) {
        const float a[4] = {lo_bf(mv[j].x), hi_bf(mv[j].x), lo_bf(mv[j].y), hi_bf(mv[j].y)};
        float s[4];
#pragma unroll
        for (int d = 0; d < 4; d++) {
            s[d] = 0.f;
#pragma unroll
            for (int k = 0; k < 4; k++) s[d] += a[k] * wd[k * 4 + d];
            sq[d] += s[d] * s[d];
            mx[d] = max(mx[d], __float_as_uint(s[d]) & 0x7FFFFFFFu);
        }
#pragma unroll
        for (int p = 0; p < 2; p++) {
            const unsigned lo = f2bf_bits(s[2 * p]) & 0xFFFF;
            const unsigned sb = __float_as_uint(s[2 * p + 1]);
            const unsigned h = (unsigned)max((int)((sb & 0x7FFFFFFFu) - lo + 0x8000u), 0) & 0x7FFF0000u;
            o[p][j] = lo | h | (sb & 0x80000000u);
        }
    }
#pragma unroll
    for (int p = 0; p < 2; p++) {
        v4u v = {o[p][0], o[p][1], o[p][2], o[p][3]};
        *(v4u*)(mdp + (size_t)p * plane + dst) = v;
    }
}

static __device__ __forceinline__ unsigned touch_code(int tid) {
    unsigned alo, ahi, blo, r = 0;
    asm volatile("s_getpc_b64 s[96:97]\n s_add_u32 %0, s96, k_bin@rel32@lo+4\n s_addc_u32 %1, s97, k_bin@rel32@hi+12\n"
                 "s_getpc_b64 s[96:97]\n s_add_u32 %2, s96, k_naive@rel32@lo+4\n" : "=&s"(alo), "=&s"(ahi), "=&s"(blo) :: "scc", "s96", "s97");
    const unsigned size = blo - alo < 65536u ? blo - alo : 0u;
    const v4i_rs rc = {(int)alo, (int)(ahi & 0xFFFF), (int)size, 0x00020000};
    if (tid < 512) asm volatile("buffer_load_dword %0, %1, %2, 0 offen" : "=v"(r) : "v"(tid * 128), "s"(rc));
    return r;
}

struct Held { unsigned a[4], t[4], A[4], cd[4]; unsigned long long m[4]; };
struct Conv { unsigned A[4], t4[4], cq[4]; };
#define HB(k) "s_mov_b64 exec, %[m" #k "]\n ds_write_b32 %[a" #k "], %[A" #k "]\n ds_or_b32 %[t" #k "], %[c" #k "]\n"
static __device__ __forceinline__ void held_out(const Held& H) {
    asm volatile(HB(0) HB(1) HB(2) HB(3) "s_mov_b64 exec, -1\n"
                 :: [m0] "s"(H.m[0]), [m1] "s"(H.m[1]), [m2] "s"(H.m[2]), [m3] "s"(H.m[3]), [a0] "v"(H.a[0]), [a1] "v"(H.a[1]), [a2] "v"(H.a[2]), [a3] "v"(H.a[3]),
                   [t0] "v"(H.t[0]), [t1] "v"(H.t[1]), [t2] "v"(H.t[2]), [t3] "v"(H.t[3]),
                   [A0] "v"(H.A[0]), [A1] "v"(H.A[1]), [A2] "v"(H.A[2]), [A3] "v"(H.A[3]), [c0] "v"(H.cd[0]), [c1] "v"(H.cd[1]), [c2] "v"(H.cd[2]), [c3] "v"(H.cd[3])
                 : "memory");
}
static __device__ __forceinline__ void tickets(unsigned* v, const Conv& c, unsigned inc) {
    asm volatile("ds_add_rtn_u32 %0, %4, %8\n ds_add_rtn_u32 %1, %5, %8\n ds_add_rtn_u32 %2, %6, %8\n ds_add_rtn_u32 %3, %7, %8\n"
                 : "=&v"(v[0]), "=&v"(v[1]), "=&v"(v[2]), "=&v"(v[3]) : "v"(c.t4[0]), "v"(c.t4[1]), "v"(c.t4[2]), "v"(c.t4[3]), "v"(inc) : "memory");
}
static __device__ __forceinline__ void tickets_masked(unsigned* v, const Conv& c, unsigned inc, const unsigned long long* vm) {
#pragma unroll
    for (int k = 0; k < 4; k++) {
        v[k] = 0;
        asm volatile("s_mov_b64 exec, %3\n ds_add_rtn_u32 %0, %1, %2\n s_mov_b64 exec, -1" : "+v"(v[k]) : "v"(c.t4[k]), "v"(inc), "s"(vm[k]) : "memory");
    }
}
static __device__ __forceinline__ void place(Held& H, unsigned* v, Conv& c, const unsigned long long* vm, unsigned lb) {
    unsigned bt[4];
#pragma unroll
    for (int k = 0; k < 4; k++) asm("v_mad_u32_u24 %0, %1, %2, %3" : "=v"(bt[k]) : "v"(c.t4[k]), "n"(LS / 4), "v"(lb));
    asm volatile("s_waitcnt lgkmcnt(0)" : "+v"(v[0]), "+v"(v[1]), "+v"(v[2]), "+v"(v[3]));
#pragma unroll
    for (int k = 0; k < 4; k++) {
        H.m[k] = __ballot(v[k] >= 16384u);
        unsigned sh;
        asm("v_lshrrev_b32 %0, 7, %3\n v_lshrrev_b32 %2, 6, %3\n v_and_b32 %0, 0x7c, %0\n v_or_b32 %1, 12, %0\n v_add_u32 %0, %0, %4\n v_add_u32 %1, %1, %4"
            : "=&v"(H.a[k]), "=&v"(H.t[k]), "=&v"(sh) : "v"(v[k]), "v"(bt[k]));
        H.A[k] = c.A[k];
        asm("v_lshlrev_b32 %0, %1, %2" : "=v"(H.cd[k]) : "v"(sh), "v"(c.cq[k]));
        asm volatile("s_andn2_b64 exec, %[w], %[m]\n ds_write_b32 %[a], %[A]\n ds_or_b32 %[t], %[c]\n s_mov_b64 exec, -1\n"
                     :: [w] "s"(vm[k]), [m] "s"(H.m[k]), [a] "v"(H.a[k]), [A] "v"(H.A[k]), [t] "v"(H.t[k]), [c] "v"(H.cd[k]) : "memory", "scc");
    }
}
struct Own { v2u e; unsigned nl, fm, ovf; };
static __device__ __forceinline__ v4u read_unit(unsigned la) {
    const lds_u32* p = ldsp(la);
    v4u v = {p[0], p[1], p[2], p[3]};
    return v;
}
typedef __attribute__((address_space(3))) v2u lds_v2u;
static __device__ __forceinline__ unsigned long long low_lanes(unsigned groups) { return groups >= 8 ? ~0ull : (1ull << (groups * 8)) - 1; }
static __device__ __forceinline__ void flush_pass(v2u e, v4u v, unsigned long long act, unsigned k16, __amdgpu_buffer_rsrc_t rr) {
    unsigned vo;
    asm volatile("s_mov_b64 exec, %1\n ds_write_b32 %2, %3 offset:12\n s_mov_b64 exec, -1\n v_cndmask_b32 %0, %4, %5, %1"
                 : "=v"(vo) : "s"(act), "v"(e.x), "v"(0u), "v"(OOB), "v"(e.y + k16) : "memory");
    __builtin_amdgcn_raw_buffer_store_b128(v, rr, vo, 0, ST_AUX);
}
static __device__ __forceinline__ void flush(Own& o, unsigned cnt, int tid, const Lay& ly, __amdgpu_buffer_rsrc_t rr) {
    const bool full = cnt >= 16384u;
    const unsigned long long bal = __ballot(full);
    const unsigned nfull = __popcll(bal);
    const unsigned wl = WL_OFF + (tid >> 6) * 512;
    if (full) {
        const unsigned pos = __builtin_amdgcn_mbcnt_hi((unsigned)(bal >> 32), __builtin_amdgcn_mbcnt_lo((unsigned)bal, 0));
        *(lds_v2u*)(size_t)(wl + pos * 8) = o.e;
        cnt -= 24 * TINC;
        *ldsp(tid * 4) = cnt;
        o.ovf |= cnt;
        o.nl++;
        o.fm >>= 1;
        o.e.y = min((o.e.y + PLANE * 128) ^ (o.fm & 8192u), OOB);
    }
    const unsigned g = (tid >> 3) & 7, k16 = (tid & 7) * 16;
    v2u e0 = *(lds_v2u*)(size_t)(wl + g * 8), e1 = *(lds_v2u*)(size_t)(wl + g * 8 + 64);
    e0.x += k16; e1.x += k16;
    const v4u v0 = read_unit(e0.x), v1 = read_unit(e1.x);
    asm volatile("" ::: "memory");
    flush_pass(e0, v0, low_lanes(nfull), k16, rr);
    flush_pass(e1, v1, nfull > 8 ? low_lanes(nfull - 8) : 0ull, k16, rr);
    for (unsigned p = 16; p < nfull; p += 8) {
        v2u e = *(lds_v2u*)(size_t)(wl + (p + g) * 8);
        e.x += k16;
        const v4u v = read_unit(e.x);
        asm volatile("" ::: "memory");
        flush_pass(e, v, low_lanes(nfull - p), k16, rr);
    }
    asm volatile("s_waitcnt lgkmcnt(0)" ::: "memory");
}

extern "C" __global__ __launch_bounds__(P1_THREADS) void k_bin(const int* __restrict__ idx_in, const int* __restrict__ idx_out,
                                                    const unsigned short* __restrict__ angle, const unsigned short* __restrict__ ang_freq,
                                                    const unsigned short* __restrict__ m, const unsigned short* __restrict__ w_down,
                                                    unsigned* __restrict__ mdp, int E, int T,
                                                    char* __restrict__ rec_end, unsigned* __restrict__ tab, unsigned* __restrict__ flags,
                                                    int NR, int per, Lay ly, unsigned* __restrict__ wgmode) {
    __shared__ __attribute__((aligned(16))) unsigned L[BIN_LDS / 4];
    const int tid = threadIdx.x;
    const unsigned run = blockIdx.x;
    char* const rec = rec_base(rec_end);
    float freq;
    const v4i_rs ro = mk_rsrc(idx_out, (unsigned)T * 4), ri = mk_rsrc(idx_in, (unsigned)T * 4), ra = mk_rsrc(angle, (unsigned)T * 2);
    const __amdgpu_buffer_rsrc_t rr = __builtin_amdgcn_make_buffer_rsrc((void*)rec, (short)0,
        (int)(ly.dumu * 16),
        0x00020000);
    const unsigned vo16 = tid * 16, vo8 = tid * 8;
    const unsigned shard = (run >> 3) & 3;
    const bool leader = run == shard * 8;
    const v4i_rs rw = mk_rsrc(wgmode + WM_CTR, (128 + 256 + 2048) * 4);
    const int wave0 = __builtin_amdgcn_readfirstlane(tid >> 6) == 0;
    const unsigned ctr_o = shard * 128, box_o = (128 + shard * 64 + ((run & 7) | ((run >> 5) << 3))) * 4;
#define PLO(j) (((j) & 1) * 8192 + ((j) & 2) * 2048 + ((j) >> 2) * 16384 + ((j) >> 1) * 1280)
    const bool prober = ((run ^ (run >> 2)) & 8) == 0;
    const unsigned loc_o = (tid & 63) - 1u < 8u ? (128 + 256 + (run >> 4) * 128 + ((tid & 63) - 1) * 16 + (run & 15)) * 4 : OOB;
    const unsigned ppg = (((run & 7) + 1) * PLANE + (run >> 4) * 16384) * 128;
    if (wave0) {
        const v4u z = {0, 0, 0, 0};
        asm volatile("buffer_store_dwordx4 %0, %1, %2, 0 offen sc0 sc1" :: "v"(z), "v"(prober ? ppg + PLO(tid >> 3) + (tid & 7) * 16 : OOB), "s"(mk_rsrc(rec, ly.dumu * 16)) : "memory");
        asm volatile("buffer_store_dword %0, %1, %2, 0 offen sc1" :: "v"(0u), "v"(tid == 0 ? (leader ? ctr_o : box_o) : loc_o), "s"(rw) : "memory");
    }
    unsigned tc0 = touch_code(tid);
    unsigned fbits;
    asm volatile("buffer_load_ushort %0, off, %1, 0" : "=v"(fbits) : "s"(mk_rsrc(ang_freq, 2)));
    v2u mv[4];
    const unsigned gp = run * 80 + (tid >> 5) * 4;
    {
        const v4i_rs rm = mk_rsrc(m, (unsigned)E * 8);
        const unsigned o = tid < 640 ? (((gp & ~255u) | ((gp >> 2) & 63)) * 32 + (tid & 31)) * 8 : OOB;
#pragma unroll
        for (int j = 0; j < 4; j++) asm volatile("buffer_load_dwordx2 %0, %1, %2, %3 offen " NTH : "=v"(mv[j]) : "v"(o), "s"(rm), "s"(j * 64 * 32 * 8));
    }
    Grp tg; bool tv[4];
    const unsigned tbase = (unsigned)NR * (P1_GRID * CH) + run * per;
    {
        const bool in = tid * 4 < per;
#pragma unroll
        for (int k = 0; k < 4; k++) tv[k] = in && tbase + tid * 4 + k < (unsigned)T;
        const unsigned o16 = in ? vo16 : OOB, o8 = in ? vo8 : OOB;
        asm volatile("buffer_load_dwordx4 %0, %1, %2, %3 offen nt" : "=v"(tg.io) : "v"(o16), "s"(ro), "s"(tbase * 4));
        asm volatile("buffer_load_dwordx4 %0, %1, %2, %3 offen nt" : "=v"(tg.ii) : "v"(o16), "s"(ri), "s"(tbase * 4));
        asm volatile("buffer_load_dwordx2 %0, %1, %2, %3 offen nt" : "=v"(tg.an) : "v"(o8), "s"(ra), "s"(tbase * 2));
    }
    const unsigned NFULL = (unsigned)NR * P1_GRID;
    Grp g0, cur[1];
    load_grp(g0, ro, ri, ra, NR > 0 ? CORD(run) : 0x3FFFFu, vo16, vo8);
    load_grp(cur[0], ro, ri, ra, NR > 1 ? CORD(run + P1_GRID) : 0x3FFFFu, vo16, vo8);
    Own own;
    own.e.x = LB + tid * LS; own.nl = 0;
    own.ovf = (unsigned)(size_t)L << 14;
    own.fm = 0; own.e.y = OOB;
    *ldsp(tid * 4) = 64;
#pragma unroll
    for (int i = 0; i < 8; i++) *ldsp(LB + tid * LS + i * 16 + 12) = 0;
    { const v2u z = {0, 0}; *(lds_v2u*)(size_t)(WL_OFF + tid * 8) = z; }
    if (tid < 32) *ldsp(MS_OFF + tid * 4) = 0;
    if (run == 0 && tid < 64) wgmode[(tid >> 1) * 32 + (tid & 1)] = 0;
    if (run == 1 && tid < 64) {
        v4u v = {pad_rec(tid), pad_rec(tid + 64), pad_rec(tid + 128), CZERO};
        *(v4u*)(rec + ((size_t)ly.dumu + tid) * 16) = v;
    }
    unsigned ans = 2;
    const unsigned tell_o = (tid & 63) < 16 ? (128 + 256 + (run >> 4) * 128 + (run & 7) * 16 + (tid & 63)) * 4 : OOB;
    if (wave0 && prober) {
        asm volatile("s_waitcnt vmcnt(16) lgkmcnt(0)" ::: "memory");
        if ((run & 7) < ly.KX) {
            unsigned long long t[7];
            unsigned x;
#define PRB1(i, j) "s_memtime %" #i "\n s_load_dword %7, %8, %" #j "\n s_waitcnt lgkmcnt(0)\n"
            asm volatile(PRB1(0, 9) PRB1(1, 10) PRB1(2, 11) PRB1(3, 12) PRB1(4, 13) PRB1(5, 14) "s_memtime %6\n s_waitcnt lgkmcnt(0)"
                         : "=&s"(t[0]), "=&s"(t[1]), "=&s"(t[2]), "=&s"(t[3]), "=&s"(t[4]), "=&s"(t[5]), "=&s"(t[6]), "=&s"(x)
                         : "s"(rec + ppg), "n"(PLO(0)), "n"(PLO(1)), "n"(PLO(2)), "n"(PLO(3)), "n"(PLO(4)), "n"(PLO(5)) : "memory");
            unsigned d[6];
#pragma unroll
            for (int i = 0; i < 6; i++) d[i] = (unsigned)t[i + 1] - (unsigned)t[i];
            ans = 2 | (((min(min(d[0], d[2]), d[4]) > min(min(d[1], d[3]), d[5]) ? 1 : 0) ^ (run >> 2)) & 1);
        }
        asm volatile("buffer_store_dword %0, %1, %2, 0 offen sc1" :: "v"(ans), "v"(tell_o), "s"(rw) : "memory");
    }
    Conv cv[2];
    unsigned seen = 0;
    auto convert = [&](Grp& gp, Conv& o) {
#pragma unroll
        for (int k = 0; k < 4; k++) {
            unsigned io = k == 0 ? gp.io.x : k == 1 ? gp.io.y : k == 2 ? gp.io.z : gp.io.w;
            unsigned ii = k == 0 ? gp.ii.x : k == 1 ? gp.ii.y : k == 2 ? gp.ii.z : gp.ii.w;
            unsigned aw = (k & 2) ? gp.an.y : gp.an.x;
            float ang = (k & 1) ? hi_bf(aw) : lo_bf(aw);
            conv_rec(io, ii, ang, freq, o.A[k], o.t4[k], o.cq[k], seen);
        }
#pragma unroll
        for (int k = 0; k < 4; k++) asm volatile("" : "+v"(o.A[k]), "+v"(o.t4[k]), "+v"(o.cq[k]));
    };
    const unsigned tinc = TINC, lb7 = LB;
    Held H;
    {
        asm volatile("s_waitcnt vmcnt(9)" : "+v"(mv[0]), "+v"(mv[1]), "+v"(mv[2]), "+v"(mv[3]));
        if (tid < 640) {
            unsigned mx[4] = {0, 0, 0, 0};
            float sq[4] = {0.f, 0.f, 0.f, 0.f};
            prep_md(mv, (tid & 31) * LMAX + gp, w_down, mdp, 32 * LMAX, mx, sq);
#pragma unroll
            for (int d = 0; d < 4; d++) {
                const unsigned v = wave_scan_max(mx[d]);
                const float s = wave_scan_addf(sq[d]);
                if ((tid & 63) == 63) { *ldsp(MS_OFF + 128 + (tid >> 6) * 32 + d * 4) = v; *ldsp(MS_OFF + 128 + (tid >> 6) * 32 + 16 + d * 4) = __float_as_uint(s); }
            }
        }
        asm volatile("s_waitcnt vmcnt(3)" : "+v"(tg.io), "+v"(tg.ii), "+v"(tg.an), "+v"(fbits),
                     "+v"(g0.io), "+v"(g0.ii), "+v"(g0.an));
        asm volatile("" :: "v"(tc0));
        freq = lo_bf(fbits) * 0.15915494309189535f;
        if (T & 1) {
            const unsigned rel = (unsigned)T - 1 - (tbase + tid * 4);
            if (tv[0] && rel < 4) { const unsigned av = angle[T - 1]; if (rel == 0) tg.an.x = av; else tg.an.y = av; }
        }
        __syncthreads();
        unsigned long long vm[4];
#pragma unroll
        for (int k = 0; k < 4; k++) vm[k] = __ballot(tv[k]);
        if (vm[0]) {
            unsigned v[4];
            convert(tg, cv[1]);
            tickets_masked(v, cv[1], tinc, vm);
            place(H, v, cv[1], vm, lb7);
            if ((H.m[0] & vm[0]) | (H.m[1] & vm[1]) | (H.m[2] & vm[2]) | (H.m[3] & vm[3])) own.ovf |= 16384u;
        }
        convert(g0, cv[0]);
#pragma unroll
        for (int k = 0; k < 4; k++) { H.m[k] = 0; H.a[k] = 0; H.t[k] = 0; H.A[k] = 0; H.cd[k] = 0; }
        asm volatile("s_waitcnt vmcnt(0)" ::: "memory");
    }
    const unsigned long long vm[4] = {~0ull, ~0ull, ~0ull, ~0ull};
    unsigned ca = run + P1_GRID, cb = run + 2 * P1_GRID;
    unsigned got = 0;
    unsigned dl = 0;
    int st = leader ? 1 : 0;
    if (NR == 0) { asm volatile("s_waitcnt lgkmcnt(0)" ::: "memory"); __syncthreads(); }
    for (int rd = 0; NR > 0; rd += 2) {
        bool done = false;
#pragma unroll
        for (int r = 0; r < 2; r++) {
            unsigned v[4];
            tickets(v, cv[r & 1], tinc);
            held_out(H);
            asm volatile("s_waitcnt vmcnt(2)" : "+v"(cur[0].io), "+v"(cur[0].ii), "+v"(cur[0].an), "+v"(got));
            convert(cur[0], cv[(r + 1) & 1]);
            if (wave0) {
                const unsigned g = __builtin_amdgcn_readfirstlane(got);
                unsigned nx = cb + P1_GRID;
                if (rd + r + 3 >= NSTAT) nx = st == 2 ? NSTAT * P1_GRID + g * 4 + shard : NFULL;
                if (st == 0 && g != 0) st = 1;
                if (st == 0 && rd + r + 4 == NSTAT) {
                    for (int i = 0; i < 128 && st == 0; i++) {
                        unsigned x = 0;
                        asm volatile("buffer_atomic_add %0, %1, %2, 0 offen sc0\n s_waitcnt vmcnt(0)" : "+v"(x) : "v"(tid == 0 ? box_o : OOB), "s"(rw) : "memory");
                        if (__builtin_amdgcn_readfirstlane(x) != 0) st = 1;
                    }
                }
                if (st == 1 && rd + r + 4 >= NSTAT) st = 2;
                nx = cb < NFULL ? min(nx, NFULL) : NFULL;
                if ((tid & 63) == 0) *ldsp(MS_OFF + 104) = nx;
                bool ask = !dl;
                if (ask && (__ballot(got != 0) & 510) == 510) { const unsigned lc = (unsigned)__ballot(got & 1) | 1; if (tid == 0) *ldsp(MS_OFF + 108) = lc; ask = false; }
                got = st == 2 && tid == 0 ? 1u : 0u;
                const bool tk = st != 1 && nx < NFULL;
                if (tk || ask)
                    asm volatile("buffer_atomic_add %0, %1, %2, 0 offen sc0" : "+v"(got) : "v"(tid == 0 ? (tk ? (st == 2 ? ctr_o : box_o) : OOB) : ask ? loc_o : OOB), "s"(rw) : "memory");
                if (prober && r == 0 && TELLC) asm volatile("buffer_store_dword %0, %1, %2, 0 offen sc1" :: "v"(ans), "v"(tell_o), "s"(rw) : "memory");
                if (leader) asm volatile("buffer_store_dword %0, %1, %2, 0 offen sc1" :: "v"(1u), "v"((128 + shard * 64 + tid) * 4), "s"(rw) : "memory");
            }
            load_grp(cur[0], ro, ri, ra, cb < NFULL ? CORD(cb) : 0x3FFFFu, vo16, vo8);
            place(H, v, cv[r & 1], vm, lb7);
            asm volatile("s_waitcnt lgkmcnt(0)" ::: "memory");
            __syncthreads();
            const unsigned cnt = *ldsp(tid * 4);
            const unsigned nx = *ldsp(MS_OFF + 104);
            if (!dl) {
                unsigned lc = __builtin_amdgcn_readfirstlane(*ldsp(MS_OFF + 108));
                if (!lc && __ballot(cnt >= 16384u)) {
                    for (int i = 0; i < 4096 && !lc; i++) {
                        unsigned x = 0;
                        asm volatile("buffer_atomic_add %0, %1, %2, 0 offen sc0\n s_waitcnt vmcnt(0)" : "+v"(x) : "v"(loc_o), "s"(rw) : "memory");
                        if ((__ballot(x != 0) & 510) == 510) lc = (unsigned)__ballot(x & 1) | 1;
                    }
                    if (!lc) { own.ovf |= 16384u; dl = 256; }
                }
                if (lc) {
                    dl = (((lc >> 1) ^ lc ^ 1) & 255) | 256;
                    own.fm = (dl & 255) << 13;
                    own.e.y = ((PLANE + blk_line(run) + tile_line(tid, run)) * 128) ^ (own.fm & 8192u);
                }
            }
            flush(own, cnt, tid, ly, rr);
            __syncthreads();
            if (ca >= NFULL) { done = true; break; }
            ca = cb;
            cb = ca < NFULL ? __builtin_amdgcn_readfirstlane(nx) : NFULL;
        }
        if (done) break;
    }
    asm volatile("" : "+v"(got));
    unsigned tc1 = touch_code(tid);
    held_out(H);
    asm volatile("s_waitcnt lgkmcnt(0)" ::: "memory");
    {
        v2u fl;
        if (tid < 64) {
            const unsigned sa = MS_OFF + 128 + (tid & 15) * 32 + (tid >> 4) * 4;
            unsigned mx = (tid & 15) < 10 ? *ldsp(sa) : 0u;
            float sq = (tid & 15) < 10 ? __uint_as_float(*ldsp(sa + 16)) : 0.f;
#pragma unroll
            for (int i = 0; i < 4; i++) {
                mx = max(mx, (unsigned)(i == 0 ? __builtin_amdgcn_update_dpp(0, (int)mx, 0x111, 0xF, 0xF, false) : i == 1 ? __builtin_amdgcn_update_dpp(0, (int)mx, 0x112, 0xF, 0xF, false)
                                        : i == 2 ? __builtin_amdgcn_update_dpp(0, (int)mx, 0x114, 0xF, 0xF, false) : __builtin_amdgcn_update_dpp(0, (int)mx, 0x118, 0xF, 0xF, false)));
                const int si = __float_as_int(sq);
                sq += __int_as_float(i == 0 ? __builtin_amdgcn_update_dpp(0, si, 0x111, 0xF, 0xF, false) : i == 1 ? __builtin_amdgcn_update_dpp(0, si, 0x112, 0xF, 0xF, false)
                                     : i == 2 ? __builtin_amdgcn_update_dpp(0, si, 0x114, 0xF, 0xF, false) : __builtin_amdgcn_update_dpp(0, si, 0x118, 0xF, 0xF, false));
            }
            mx = min(mx ? (mx >> 16) + 2 : 0u, 0x7F80u);
            fl.x = (mx << 16) | (unsigned)__builtin_amdgcn_update_dpp(0, (int)mx, 0x142, 0xA, 0xF, false);
            fl.y = (__float_as_uint(sq) & 0xFFFF0000u) | ((unsigned)__builtin_amdgcn_update_dpp(0, __float_as_int(sq), 0x142, 0xA, 0xF, false) >> 16);
        }
        *ldsp(WL_OFF + tid * 4) = min(own.nl, 31u) | ((dl & 255) << 9);
        if (own.ovf >= 16384u || own.nl > ly.KX || (seen & 0x30000000u)) *ldsp(MS_OFF + 96) = 1;
        const unsigned tt = ((tid & 31) << 5) | (tid >> 5);
        const unsigned d = *ldsp(tt * 4) - 64;
        unsigned P = (d * 3) >> 11;
        if (P >= 24) { *ldsp(MS_OFF + 96) = 1; P = 0; }
        const unsigned nu = (P + 2) / 3;
        const unsigned inc = wave_scan_add(nu);
        const unsigned tot = ((unsigned)__builtin_amdgcn_readlane((int)inc, 63) + 7) & ~7u;
        if ((tid & 63) == 0) *ldsp(MS_OFF + (tid >> 6) * 4) = tot;
        __syncthreads();
        const unsigned fb = __builtin_amdgcn_readlane(row_scan_add((tid & 63) < (tid >> 6) ? *ldsp(MS_OFF + (tid & 15) * 4) : 0), 15);
        if (fb + tot > ly.fcap) *ldsp(MS_OFF + 96) = 1;
        *ldsp(WL_OFF + tt * 4) |= (nu << 5) | ((fb + inc - nu) << 18);
        *ldsp(tid * 4) = ((inc - nu) << 8) | P;
        __syncthreads();
        __hip_atomic_store(tab + run * NT + tid, *ldsp(WL_OFF + tid * 4), __ATOMIC_RELAXED, __HIP_MEMORY_SCOPE_AGENT);
        if (tid < 64) {
            if (*ldsp(MS_OFF + 96)) fl.x = 0x7F807F80u;
            if ((tid & 31) == 31) __hip_atomic_store((unsigned long long*)(flags + ((tid >> 5) * P1_GRID + run) * 2), ((unsigned long long)fl.y << 32) | fl.x, __ATOMIC_RELAXED, __HIP_MEMORY_SCOPE_AGENT);
        }
        const unsigned k = tid & 7;
        const unsigned pa = (run & 15) * 24 + k * 3;
        const __amdgpu_buffer_rsrc_t rf = __builtin_amdgcn_make_buffer_rsrc((void*)(rec + (size_t)frag_unit(run, ly) * 16), (short)0, ly.fcap * 16, 0x00020000);
        v4u fv[8]; unsigned fd[8];
        const unsigned sg = LB + (tid >> 6) * 8192;
#pragma unroll
        for (int it = 0; it < 8; it++) {
            const unsigned u = (tid & ~63) + it * 8 + ((tid >> 3) & 7), t = ((u & 31) << 5) | (u >> 5);
            const unsigned w = *ldsp(u * 4), Pt = w & 255;
            v4u v = read_unit(LB + t * LS + k * 16);
            if (k * 3 + 1 >= Pt) { v.y = pad_rec(pa + 1); v.w |= 512u << 11; }
            if (k * 3 + 2 >= Pt) { v.z = pad_rec(pa + 2); v.w |= 512u << 22; }
            fv[it] = v; fd[it] = k * 3 < Pt ? sg + ((w >> 8) + k) * 16 : 0xFFFF0u;
        }
        __syncthreads();
#pragma unroll
        for (int it = 0; it < 8; it++) *(lds_v4u*)(size_t)fd[it] = fv[it];
        for (unsigned p = 0; p < tot; p += 64) {
            const v4u v = *(lds_v4u*)(size_t)(sg + (p + (tid & 63)) * 16);
            __builtin_amdgcn_raw_buffer_store_b128(v, rf, p + (tid & 63) < tot ? (fb + p + (tid & 63)) * 16 : OOB, 0, 16);
        }
        asm volatile("s_waitcnt vmcnt(0)" :: "v"(tc1));
    }
#pragma unroll
    for (int r = 0; r < PD; r++) WAIT_GRP(0, cur[r]);
}

struct AccArgs {
    const char* rec; const unsigned* tab; const unsigned* flags; const unsigned* mdp; float* part; unsigned* praw; unsigned* wgmode;
    const int* idx_in; const int* idx_out; const unsigned short* angle; const unsigned short* ang_freq; const unsigned short* m; const unsigned short* w_down;
    const unsigned short* dist; const unsigned short* centers; const unsigned short* w_rbf; const unsigned short* w_sbf; const unsigned short* w_up;
    unsigned short* out;
    int EP; unsigned O; int T; float c4; int E; int unused;
    Lay ly;
};
typedef char AccArgs_has_no_padding[sizeof(AccArgs) == 19 * 8 + 10 * 4 ? 1 : -1];

typedef const __attribute__((address_space(4))) unsigned* cptr;
static __device__ __forceinline__ void edge_gh(float d, const unsigned short* centers, const unsigned short* w_sbf, const unsigned short* w_rbf, float* g, float* h) {
#pragma unroll
    for (int k = 0; k < 4; k++) g[k] = h[k] = 0.f;
#pragma unroll
    for (int r = 0; r < 8; r++) {
        const unsigned cu = ((cptr)centers)[r >> 1];
        const float df = d - ((r & 1) ? hi_bf(cu) : lo_bf(cu));
        const float rb = __expf(-10.f * df * df);
#pragma unroll
        for (int k = 0; k < 2; k++) {
            const unsigned u = ((cptr)w_sbf)[r * 2 + k], v = ((cptr)w_rbf)[r * 2 + k];
            g[2 * k] += rb * lo_bf(u); g[2 * k + 1] += rb * hi_bf(u); h[2 * k] += rb * lo_bf(v); h[2 * k + 1] += rb * hi_bf(v);
        }
    }
}
static __device__ __forceinline__ v2u edge_out(const float* agg, const float* g, const float* h, unsigned m0, unsigned m1, const unsigned* wu) {
    float a[4];
#pragma unroll
    for (int k = 0; k < 4; k++) a[k] = agg[k] * g[k];
    const float mm[4] = {lo_bf(m0), hi_bf(m0), lo_bf(m1), hi_bf(m1)};
    float o[4];
#pragma unroll
    for (int dd = 0; dd < 4; dd++) {
        float s = 0.f;
#pragma unroll
        for (int k = 0; k < 4; k++) { const unsigned u = wu[k * 2 + (dd >> 1)]; s += a[k] * ((dd & 1) ? hi_bf(u) : lo_bf(u)); }
        o[dd] = mm[dd] + s * h[dd];
    }
    v2u r;
    asm("v_cvt_pk_bf16_f32 %0, %1, %2" : "=v"(r.x) : "v"(o[0]), "v"(o[1]));
    asm("v_cvt_pk_bf16_f32 %0, %1, %2" : "=v"(r.y) : "v"(o[2]), "v"(o[3]));
    return r;
}

typedef const __attribute__((address_space(4))) AccArgs* kaptr;
static __device__ __forceinline__ void acc_fallback(unsigned* lds, unsigned mode, const __attribute__((address_space(4))) AccArgs& a_, int tid) {
    const char* rec = rec_base(a_.rec); const unsigned* flags = a_.flags; const unsigned* mdp = a_.mdp;
    const int EP = a_.EP, T = a_.T; const Lay ly = {a_.ly.K0, a_.ly.KX, a_.ly.dumu, a_.ly.fcap};
    const int lane = tid & 63;
    const int xcd = blockIdx.x & 7, jj = blockIdx.x >> 3;
    const int qt = xcd >> 1, cp = jj & 1, b = (xcd & 1) * 16 + (jj >> 1);
    const unsigned O = a_.O;
    float* pout0 = a_.part + (size_t)(qt * 4 + cp * 2) * EP + (size_t)b * O;
    const unsigned G = tid >> 3, k = tid & 7;
    {
        unsigned* praw = a_.praw + (size_t)(qt * 2 + cp) * EP + (size_t)b * O;
        for (unsigned i = tid; i < O; i += P2_THREADS) praw[i] = 0;
    }
    if (mode == 1) {
        unsigned* acc = lds + OBASE;
        const float Xm = 2139095040.0f / (float)(NWS * (ly.KX * 24 + 24)) - 2.0f;
#pragma unroll 1
        for (int c = 0; c < 2; c++) {
            __syncthreads();
            if (tid == 0) lds[0] = 0;
            __syncthreads();
            if (tid < NWS) { unsigned v = wave_scan_max((flags[(cp * P1_GRID + tid) * 2] >> (16 * c)) & 0xFFFF); if (lane == 63) atomicMax(&lds[0], v); }
            __syncthreads();
            unsigned mbits = __builtin_amdgcn_readfirstlane(lds[0]);
            if (mbits == 0) mbits = 0x3f80;
            const float M = __uint_as_float(mbits << 16);
            const float S = Xm / (511.0f * M), inv = M / Xm;
            float racc[20];
#pragma unroll
            for (int r = 0; r < 20; r++) racc[r] = 0.f;
#pragma unroll 1
            for (int s = 0; s < 8; s++) {
                __syncthreads();
                const unsigned* src = mdp + (size_t)cp * (32 * LMAX) + (size_t)(qt * 8 + s) * LMAX;
                for (unsigned i = tid; i < LMAX; i += P2_THREADS) {
                    unsigned w = src[i];
                    lds[(i & ~255u) | ((i & 3) << 6) | ((i >> 2) & 63)] = __float_as_uint((c ? __uint_as_float(w) : lo_bf(w)) * S);
                }
                for (int i = tid; i < OBASE; i += P2_THREADS) acc[i] = 0;
                __syncthreads();
                const unsigned t = b * 32 + qt * 8 + s;
                for (unsigned r = G; r < NWS; r += 128) {
                    const unsigned tw = a_.tab[r * NT + t], nl = tw & 31, nu = (tw >> 5) & 15;
                    for (unsigned l = 0; l <= nl; l++) {
                        unsigned u;
                        if (l < nl) u = (((l + 1) * PLANE + blk_line(r) + tile_line(t, r)) ^ ((__popc((tw >> 9) & 511 & ((2u << l) - 1)) & 1) << 6)) * 8 + k;
                        else { if (k >= nu) break; u = frag_unit(r, ly) + (tw >> 18) + k; }
                        v4u q = *(const v4u*)(rec + (size_t)u * 16);
                        unsigned a[3] = {q.x, q.y, q.z};
#pragma unroll
                        for (int j = 0; j < 3; j++) {
                            const int cq = (int)((q.w >> (j == 0 ? 1 : j == 1 ? 11 : 22)) & 1023) - 512;
                            const int x = __float2int_rn(__uint_as_float(lds[(a[j] >> 2) & 0x7FFF]) * (float)cq);
                            if ((a[j] >> 17) - IDXB < OBASE) atomicAdd(&acc[(a[j] >> 17) - IDXB], (unsigned)x);
                        }
                    }
                }
                __syncthreads();
#pragma unroll
                for (int r = 0; r < 20; r++) racc[r] += (float)(int)acc[r * P2_THREADS + tid] * inv;
            }
#pragma unroll
            for (int r = 0; r < 20; r++) if ((unsigned)(r * P2_THREADS + tid) < O) pout0[(size_t)c * EP + r * P2_THREADS + tid] = racc[r];
        }
    }
    if (mode == 2) {
        float* facc = (float*)lds;
        const float freq = bf2f(a_.ang_freq[0]);
        float wd[2][4];
#pragma unroll
        for (int kk = 0; kk < 4; kk++) { wd[0][kk] = bf2f(a_.w_down[kk * 4 + cp * 2]); wd[1][kk] = bf2f(a_.w_down[kk * 4 + cp * 2 + 1]); }
        __syncthreads();
        for (int i = tid; i < 2 * OBASE; i += P2_THREADS) facc[i] = 0.f;
        __syncthreads();
        for (int t = tid; t < T; t += P2_THREADS) {
            const unsigned io = a_.idx_out[t], ii = a_.idx_in[t];
            if (((io >> 7) & 31) == (unsigned)b && ((ii >> 3) & 3) == (unsigned)qt) {
                const v2u mv = ((const v2u*)a_.m)[ii];
                const float a[4] = {lo_bf(mv.x), hi_bf(mv.x), lo_bf(mv.y), hi_bf(mv.y)};
                const float cs = __cosf(freq * bf2f(a_.angle[t]));
                const unsigned ol = (io & 127u) | ((io >> 5) & ~127u);
#pragma unroll
                for (int c = 0; c < 2; c++)
                    atomicAdd(&facc[c * OBASE + ol], (a[0] * wd[c][0] + a[1] * wd[c][1] + a[2] * wd[c][2] + a[3] * wd[c][3]) * cs);
            }
        }
        __syncthreads();
        for (unsigned idx = tid; idx < O; idx += P2_THREADS) { pout0[idx] = facc[idx]; pout0[(size_t)EP + idx] = facc[OBASE + idx]; }
    }
}

#define SWAPX 2
#define WXOR ((wave & 1) << 2)
#define RAUX ""
#define J0 (wave >> 1)
#define IDXMASK "0x8fffffff"
#define STR2(x) #x
#define STR(x) STR2(x)
#define PLAN_IT 160

extern "C" __global__ __launch_bounds__(P2_THREADS) void k_acc(AccArgs a_) {
    const Lay ly = a_.ly;
    __shared__ __attribute__((aligned(16))) unsigned lds[2 * OBASE];
    const int tid = threadIdx.x;
    const int wave = __builtin_amdgcn_readfirstlane(tid >> 6), lane = tid & 63;
    const int xcd = blockIdx.x & 7, jj = blockIdx.x >> 3;
    const int qt = xcd >> 1, cp = jj & 1, b = (xcd & 1) * 16 + (jj >> 1);
    const unsigned O = a_.O;
    unsigned stv = 0;
    unsigned tc2 = touch_code(tid);
    unsigned* praw = a_.praw + (size_t)(qt * 2 + cp) * a_.EP + (size_t)b * O;
    const unsigned* mds = a_.mdp + (size_t)cp * (32 * LMAX) + (size_t)(qt * 8) * LMAX;
    unsigned mode = (unsigned)(size_t)lds ? 2u : 0u;
    unsigned exs = 0, bad = 0;
    v4u mm[2], dd;
    const int q = qt * 2 + cp;
    unsigned t16 = tid * 16;
    if (mode == 0) {
        const unsigned w = wave * 16 + ((lane & 15) ^ (cp ? SWAPX : 0) ^ (WXOR)), t0 = b * 32 + qt * 8 + (lane >> 4) * 2;
        v4i_rs rs = mk_rsrc(rec_base(a_.rec), ly.dumu + 64);
        rs.y |= 16 << 16;
        const v4i_rs rm = mk_rsrc(mds, 8 * LMAX * 4);
        const v4i_rs rp0 = mk_rsrc(praw, O * 4);
        const v4i_rs rt = mk_rsrc(a_.tab, (NT * NWS + FL_SIZE) * 4);
        const v4i_rs rmm = mk_rsrc(a_.m, a_.E * 8), rd = mk_rsrc(a_.dist, a_.E * 2);
        asm volatile(
            "s_setprio 3\n"
            "buffer_load_dwordx2 v[8:9], %[tof], %[rt], 0 offen\n"
            "v_and_b32 v10, 1023, %[tid16]\n"
            "v_bfrev_b32 v12, 1\n"
            "v_add_u32 v11, %[pb], v10\n"
            "v_bfrev_b32 v13, 1\n"
            "v_bfrev_b32 v14, 1\n"
            "v_bfrev_b32 v15, 1\n"
            "ds_write_b128 v11, v[12:15] offset:0\n"
            "ds_write_b128 v11, v[12:15] offset:1024\n"
            "ds_write_b128 v11, v[12:15] offset:2048\n"
            "ds_write_b128 v11, v[12:15] offset:3072\n"
            "ds_write_b128 v11, v[12:15] offset:4096\n"
            "v_bfrev_b32 v80, 1\n"
            "v_mov_b32 v84, 0xb9000000\n"
            "v_mov_b32 v88, 0\n"
            "v_add_u32 v89, 81920, %[tid16]\n"
            "v_add_u32 v90, 65536, v89\n"
            "v_lshrrev_b32 v16, 4, v10\n"
            "v_add_u32 v83, %[dumu], v16\n"
            "v_and_b32 v17, 7, v16\n"
            "v_lshl_or_b32 v82, v17, 28, v17\n"
            "v_lshrrev_b32 v18, 3, v16\n"
            "v_lshl_add_u32 v17, v17, 3, v18\n"
            "v_lshlrev_b32 v17, 2, v17\n"
            "v_add_u32 v17, %[pb], v17\n"
            "s_getpc_b64 s[70:71]\n"
            "Lpc%=:\n"
            "s_add_u32 s72, s70, Lchg2%=-Lpc%=\n s_addc_u32 s73, s71, 0\n"
            "s_add_u32 s66, s70, Lchg3%=-Lpc%=\n s_addc_u32 s67, s71, 0\n"
            "s_add_u32 s70, s70, Lchg1%=-Lpc%=\n s_addc_u32 s71, s71, 0\n"
            "s_waitcnt vmcnt(0)\n"
            "buffer_load_dwordx4 v[92:95], v10, %[rt], %[fof] offen\n"
            "buffer_load_dwordx4 v[96:99], v10, %[rt], %[fof] offen offset:1024\n"
            "s_mov_b32 s94, 0\n"
            "buffer_load_dwordx4 v[40:43], %[tid16], %[rm], s94 offen\n"
            "s_add_u32 s96, s94, 16384\n"
            "buffer_load_dwordx4 v[44:47], %[tid16], %[rm], s96 offen\n"
            "s_add_u32 s96, s94, 32768\n"
            "buffer_load_dwordx4 v[48:51], %[tid16], %[rm], s96 offen\n"
            "s_add_u32 s96, s94, 49152\n"
            "buffer_load_dwordx4 v[52:55], %[tid16], %[rm], s96 offen\n"
            "s_add_u32 s96, s94, 65536\n"
            "buffer_load_dwordx4 v[56:59], %[tid16], %[rm], s96 offen\n"
            "v_and_b32 v20, 31, v8\n v_and_b32 v21, 31, v9\n v_bfe_u32 v22, v8, 5, 4\n v_bfe_u32 v23, v9, 5, 4\n"
            "v_min_u32 v24, 1, v22\n v_min_u32 v25, 1, v23\n v_add_u32 v24, v24, v20\n v_add_u32 v25, v25, v21\n"
            "v_lshl_or_b32 v24, v25, 16, v24\n"
            "v_and_b32 v19, 0x3fe00, v8\n v_lshrrev_b32 v8, 18, v8\n v_lshrrev_b32 v9, 18, v9\n"
            "v_add_u32_dpp v25, v24, v24 row_shr:1 row_mask:0xf bank_mask:0xf bound_ctrl:1\n"
            "v_add_u32 v8, %[fb], v8\n v_add_u32 v9, %[fb], v9\n"
            "v_add_u32_dpp v25, v25, v25 row_shr:2 row_mask:0xf bank_mask:0xf bound_ctrl:1\n"
            "v_sub_u32 v26, 8, v22\n v_sub_u32 v27, 8, v23\n"
            "v_add_u32_dpp v25, v25, v25 row_shr:4 row_mask:0xf bank_mask:0xf bound_ctrl:1\n"
            "v_lshl_or_b32 v8, v26, 28, v8\n v_lshl_or_b32 v9, v27, 28, v9\n"
            "v_add_u32_dpp v25, v25, v25 row_shr:8 row_mask:0xf bank_mask:0xf bound_ctrl:1\n"
            "s_nop 1\n"
            "v_readlane_b32 s84, v25, 15\n"
            "v_readlane_b32 s85, v25, 31\n"
            "v_readlane_b32 s86, v25, 47\n"
            "v_readlane_b32 s87, v25, 63\n"
            "v_sub_u32 v25, v25, v24\n"
            "s_mov_b32 s96, 0\n s_mov_b64 s[76:77], 0\n"
            "s_add_u32 s84, s84, 0x70007\n"
            "s_bfe_u32 s88, s84, 0xd0003\n s_lshr_b32 s89, s84, 19\n"
            "s_max_u32 s88, s88, 8\n s_max_u32 s89, s89, 8\n"
            "s_add_u32 s90, s96, s88\n"
            "s_pack_ll_b32_b16 s91, s96, s90\n"
            "s_add_u32 s96, s90, s89\n"
            "s_lshl_b32 s89, s89, 8\n s_or_b32 s88, s88, s89\n s_and_b32 s88, s88, 0xffff\n"
            "s_or_b32 s76, s76, s88\n"
            "s_lshl_b32 s91, s91, 3\n"
            "v_mov_b32 v26, s91\n"
            "s_add_u32 s85, s85, 0x70007\n"
            "s_bfe_u32 s88, s85, 0xd0003\n s_lshr_b32 s89, s85, 19\n"
            "s_max_u32 s88, s88, 8\n s_max_u32 s89, s89, 8\n"
            "s_add_u32 s90, s96, s88\n"
            "s_pack_ll_b32_b16 s91, s96, s90\n"
            "s_add_u32 s96, s90, s89\n"
            "s_lshl_b32 s89, s89, 8\n s_or_b32 s88, s88, s89\n s_and_b32 s88, s88, 0xffff\n"
            "s_lshl_b32 s88, s88, 16\n"
            "s_or_b32 s76, s76, s88\n"
            "s_lshl_b32 s91, s91, 3\n"
            "s_mov_b32 exec_lo, 0xffff0000\n s_mov_b32 exec_hi, 0xffffffff\n"
            "v_mov_b32 v26, s91\n"
            "s_add_u32 s86, s86, 0x70007\n"
            "s_bfe_u32 s88, s86, 0xd0003\n s_lshr_b32 s89, s86, 19\n"
            "s_max_u32 s88, s88, 8\n s_max_u32 s89, s89, 8\n"
            "s_add_u32 s90, s96, s88\n"
            "s_pack_ll_b32_b16 s91, s96, s90\n"
            "s_add_u32 s96, s90, s89\n"
            "s_lshl_b32 s89, s89, 8\n s_or_b32 s88, s88, s89\n s_and_b32 s88, s88, 0xffff\n"
            "s_or_b32 s77, s77, s88\n"
            "s_lshl_b32 s91, s91, 3\n"
            "s_mov_b32 exec_lo, 0x0\n s_mov_b32 exec_hi, 0xffffffff\n"
            "v_mov_b32 v26, s91\n"
            "s_add_u32 s87, s87, 0x70007\n"
            "s_bfe_u32 s88, s87, 0xd0003\n s_lshr_b32 s89, s87, 19\n"
            "s_max_u32 s88, s88, 8\n s_max_u32 s89, s89, 8\n"
            "s_add_u32 s90, s96, s88\n"
            "s_pack_ll_b32_b16 s91, s96, s90\n"
            "s_add_u32 s96, s90, s89\n"
            "s_lshl_b32 s89, s89, 8\n s_or_b32 s88, s88, s89\n s_and_b32 s88, s88, 0xffff\n"
            "s_lshl_b32 s88, s88, 16\n"
            "s_or_b32 s77, s77, s88\n"
            "s_lshl_b32 s91, s91, 3\n"
            "s_mov_b32 exec_lo, 0x0\n s_mov_b32 exec_hi, 0xffff0000\n"
            "v_mov_b32 v26, s91\n"
            "s_mov_b64 exec, -1\n"
            "s_mov_b32 %[bad], 0\n"
            "s_cmp_gt_u32 s96, " STR(PLAN_IT) "\n"
            "s_cbranch_scc0 1f\n"
            "s_mov_b32 %[bad], 1\n s_mov_b32 s76, 0x08080808\n s_mov_b32 s77, 0x08080808\n"
            "s_branch Lnoplan%=\n"
            "1:\n"
            "v_add_u32 v25, v25, v26\n"
            "v_and_b32 v26, 0xffff, v25\n v_lshrrev_b32 v27, 16, v25\n"
            "v_lshlrev_b32 v26, 2, v26\n v_lshlrev_b32 v27, 2, v27\n"
            "v_add_u32 v26, %[pb], v26\n v_add_u32 v27, %[pb], v27\n"
            "v_lshl_add_u32 v28, v20, 2, v26\n v_lshl_add_u32 v29, v21, 2, v27\n"
            "v_cmp_ne_u32 vcc, 0, v22\n v_cmp_ne_u32 s[88:89], 0, v23\n"
            "v_mov_b32 v18, %[xb]\n"
            "s_mov_b64 exec, vcc\n ds_write_b32 v28, v8\n s_mov_b64 exec, s[88:89]\n ds_write_b32 v29, v9\n s_mov_b64 exec, -1\n"
            "v_min_u32 v24, v20, v21\n"
            "s_mov_b32 s90, 0\n"
            "v_cmp_gt_u32 vcc, %[k0], v24\n"
            "s_cmp_lg_u64 vcc, 0\n s_cbranch_scc1 2f\n"
            "4:\n"
            "s_add_u32 s96, s90, 2\n s_cmp_gt_u32 s96, %[k0]\n"
            "s_cbranch_scc1 2f\n"
            "v_and_b32 v30, 512, v19\n v_add_u32 v18, 2097152, v18\n v_lshrrev_b32 v19, 1, v19\n v_xor_b32 v18, v18, v30\n v_mov_b32 v30, v18\n v_xor_b32 v31, 8, v18\n"
            "v_and_b32 v28, 512, v19\n v_add_u32 v18, 2097152, v18\n v_lshrrev_b32 v19, 1, v19\n v_xor_b32 v18, v18, v28\n v_mov_b32 v28, v18\n v_xor_b32 v29, 8, v18\n"
            "ds_write2_b32 v26, v30, v28 offset1:1\n ds_write2_b32 v27, v31, v29 offset1:1\n"
            "v_add_u32 v26, 8, v26\n v_add_u32 v27, 8, v27\n"
            "s_mov_b32 s90, s96\n"
            "s_branch 4b\n"
            "2:\n"
            "v_cmp_lt_u32 vcc, s90, v20\n v_cmp_lt_u32 s[88:89], s90, v21\n"
            "v_and_b32 v30, 512, v19\n v_add_u32 v18, 2097152, v18\n v_lshrrev_b32 v19, 1, v19\n v_xor_b32 v18, v18, v30\n v_mov_b32 v30, v18\n v_xor_b32 v31, 8, v18\n"
            "s_add_u32 s90, s90, 1\n"
            "s_or_b64 s[84:85], vcc, s[88:89]\n"
            "s_cbranch_scc0 Lnoplan%=\n"
            "s_mov_b64 exec, vcc\n ds_write_b32 v26, v30\n s_mov_b64 exec, s[88:89]\n ds_write_b32 v27, v31\n s_mov_b64 exec, -1\n"
            "v_add_u32 v26, 4, v26\n v_add_u32 v27, 4, v27\n"
            "s_branch 2b\n"
            "Lnoplan%=:\n"
            "ds_read_b32 v60, v17 offset:0\n"
            "ds_read_b32 v61, v17 offset:256\n"
            "ds_read_b32 v62, v17 offset:512\n"
            "ds_read_b32 v63, v17 offset:768\n"
            "ds_read_b32 v64, v17 offset:1024\n"
            "ds_read_b32 v65, v17 offset:1280\n"
            "ds_read_b32 v66, v17 offset:1536\n"
            "ds_read_b32 v67, v17 offset:1792\n"
            "ds_read_b32 v68, v17 offset:2048\n"
            "ds_read_b32 v69, v17 offset:2304\n"
            "ds_read_b32 v70, v17 offset:2560\n"
            "ds_read_b32 v71, v17 offset:2816\n"
            "ds_read_b32 v72, v17 offset:3072\n"
            "ds_read_b32 v73, v17 offset:3328\n"
            "ds_read_b32 v74, v17 offset:3584\n"
            "ds_read_b32 v75, v17 offset:3840\n"
            "ds_read_b32 v76, v17 offset:4096\n"
            "ds_read_b32 v77, v17 offset:4352\n"
            "ds_read_b32 v78, v17 offset:4608\n"
            "ds_read_b32 v79, v17 offset:4864\n"
            "v_mov_b32 v100, 0\n"
            "v_mov_b32 v101, 0\n"
            "v_mov_b32 v102, 0\n"
            "v_mov_b32 v103, 0\n"
            "v_mov_b32 v107, v11\n"
            "s_mov_b32 s82, 0\n"
            "s_waitcnt lgkmcnt(15)\n"
            "v_mov_b32 v81, v60\n"
            "s_nop 1\n"
            "v_add_u32_dpp v111, v81, v82 row_newbcast:0 row_mask:0xf bank_mask:0x3\n"
            "v_add_u32_dpp v111, v81, v82 row_newbcast:8 row_mask:0xf bank_mask:0xc\n"
            "v_and_b32 v111, " IDXMASK ", v111\n"
            "v_min_u32 v111, v111, v83\n"
            "buffer_load_dwordx4 v[8:11], v111, %[rs], 0 idxen " RAUX "\n"
            "v_add_u32_dpp v111, v81, v82 row_newbcast:1 row_mask:0xf bank_mask:0x3\n"
            "v_add_u32_dpp v111, v81, v82 row_newbcast:9 row_mask:0xf bank_mask:0xc\n"
            "v_and_b32 v111, " IDXMASK ", v111\n"
            "v_min_u32 v111, v111, v83\n"
            "buffer_load_dwordx4 v[12:15], v111, %[rs], 0 idxen " RAUX "\n"
            "v_add_u32_dpp v111, v81, v82 row_newbcast:2 row_mask:0xf bank_mask:0x3\n"
            "v_add_u32_dpp v111, v81, v82 row_newbcast:10 row_mask:0xf bank_mask:0xc\n"
            "v_and_b32 v111, " IDXMASK ", v111\n"
            "v_min_u32 v111, v111, v83\n"
            "buffer_load_dwordx4 v[16:19], v111, %[rs], 0 idxen " RAUX "\n"
            "v_add_u32_dpp v111, v81, v82 row_newbcast:3 row_mask:0xf bank_mask:0x3\n"
            "v_add_u32_dpp v111, v81, v82 row_newbcast:11 row_mask:0xf bank_mask:0xc\n"
            "v_and_b32 v111, " IDXMASK ", v111\n"
            "v_min_u32 v111, v111, v83\n"
            "buffer_load_dwordx4 v[20:23], v111, %[rs], 0 idxen " RAUX "\n"
            "v_add_u32_dpp v111, v81, v82 row_newbcast:4 row_mask:0xf bank_mask:0x3\n"
            "v_add_u32_dpp v111, v81, v82 row_newbcast:12 row_mask:0xf bank_mask:0xc\n"
            "v_and_b32 v111, " IDXMASK ", v111\n"
            "v_min_u32 v111, v111, v83\n"
            "buffer_load_dwordx4 v[24:27], v111, %[rs], 0 idxen " RAUX "\n"
            "v_add_u32_dpp v111, v81, v82 row_newbcast:5 row_mask:0xf bank_mask:0x3\n"
            "v_add_u32_dpp v111, v81, v82 row_newbcast:13 row_mask:0xf bank_mask:0xc\n"
            "v_and_b32 v111, " IDXMASK ", v111\n"
            "v_min_u32 v111, v111, v83\n"
            "buffer_load_dwordx4 v[28:31], v111, %[rs], 0 idxen " RAUX "\n"
            "v_add_u32_dpp v111, v81, v82 row_newbcast:6 row_mask:0xf bank_mask:0x3\n"
            "v_add_u32_dpp v111, v81, v82 row_newbcast:14 row_mask:0xf bank_mask:0xc\n"
            "v_and_b32 v111, " IDXMASK ", v111\n"
            "v_min_u32 v111, v111, v83\n"
            "buffer_load_dwordx4 v[32:35], v111, %[rs], 0 idxen " RAUX "\n"
            "v_add_u32_dpp v111, v81, v82 row_newbcast:7 row_mask:0xf bank_mask:0x3\n"
            "v_add_u32_dpp v111, v81, v82 row_newbcast:15 row_mask:0xf bank_mask:0xc\n"
            "v_and_b32 v111, " IDXMASK ", v111\n"
            "v_min_u32 v111, v111, v83\n"
            "buffer_load_dwordx4 v[36:39], v111, %[rs], 0 idxen " RAUX "\n"
            "s_min_u32 s82, s82, 19\n"
            "s_add_u32 s82, s82, 1\n"
            "s_set_gpr_idx_on s82, gpr_idx(SRC0)\n"
            "s_nop 0\n"
            "v_mov_b32 v81, v60\n"
            "s_set_gpr_idx_off\n"
            "s_setprio 0\n"
            "ds_write_b128 v107, v[100:103] offset:0\n"
            "ds_write_b128 v107, v[100:103] offset:1024\n"
            "ds_write_b128 v107, v[100:103] offset:2048\n"
            "ds_write_b128 v107, v[100:103] offset:3072\n"
            "ds_write_b128 v107, v[100:103] offset:4096\n"
            "s_waitcnt vmcnt(13)\n"
            "v_pk_max_u16 v92, v92, v94\n v_pk_max_u16 v96, v96, v98\n"
            "v_lshlrev_b32 v104, 16, v93\n v_lshlrev_b32 v105, 16, v95\n v_lshlrev_b32 v106, 16, v97\n v_lshlrev_b32 v108, 16, v99\n"
            "v_pk_max_u16 v92, v92, v96\n"
            "v_add_f32 v104, v104, v105\n v_add_f32 v106, v106, v108\n v_add_f32 v93, v93, v95\n v_add_f32 v97, v97, v99\n"
            "v_and_b32 v94, 0xffff, v92\n v_lshrrev_b32 v92, 16, v92\n"
            "v_add_f32 v104, v104, v106\n v_add_f32 v93, v93, v97\n"
            "s_nop 0\n"
            "v_max_u32_dpp v94, v94, v94 row_shr:1 row_mask:0xf bank_mask:0xf bound_ctrl:1\n"
            "v_max_u32_dpp v92, v92, v92 row_shr:1 row_mask:0xf bank_mask:0xf bound_ctrl:1\n"
            "v_add_f32_dpp v104, v104, v104 row_shr:1 row_mask:0xf bank_mask:0xf bound_ctrl:1\n"
            "v_add_f32_dpp v93, v93, v93 row_shr:1 row_mask:0xf bank_mask:0xf bound_ctrl:1\n"
            "v_max_u32_dpp v94, v94, v94 row_shr:2 row_mask:0xf bank_mask:0xf bound_ctrl:1\n"
            "v_max_u32_dpp v92, v92, v92 row_shr:2 row_mask:0xf bank_mask:0xf bound_ctrl:1\n"
            "v_add_f32_dpp v104, v104, v104 row_shr:2 row_mask:0xf bank_mask:0xf bound_ctrl:1\n"
            "v_add_f32_dpp v93, v93, v93 row_shr:2 row_mask:0xf bank_mask:0xf bound_ctrl:1\n"
            "v_max_u32_dpp v94, v94, v94 row_shr:4 row_mask:0xf bank_mask:0xf bound_ctrl:1\n"
            "v_max_u32_dpp v92, v92, v92 row_shr:4 row_mask:0xf bank_mask:0xf bound_ctrl:1\n"
            "v_add_f32_dpp v104, v104, v104 row_shr:4 row_mask:0xf bank_mask:0xf bound_ctrl:1\n"
            "v_add_f32_dpp v93, v93, v93 row_shr:4 row_mask:0xf bank_mask:0xf bound_ctrl:1\n"
            "v_max_u32_dpp v94, v94, v94 row_shr:8 row_mask:0xf bank_mask:0xf bound_ctrl:1\n"
            "v_max_u32_dpp v92, v92, v92 row_shr:8 row_mask:0xf bank_mask:0xf bound_ctrl:1\n"
            "v_add_f32_dpp v104, v104, v104 row_shr:8 row_mask:0xf bank_mask:0xf bound_ctrl:1\n"
            "v_add_f32_dpp v93, v93, v93 row_shr:8 row_mask:0xf bank_mask:0xf bound_ctrl:1\n"
            "v_max_u32_dpp v94, v94, v94 row_bcast:15 row_mask:0xa bank_mask:0xf\n"
            "v_max_u32_dpp v92, v92, v92 row_bcast:15 row_mask:0xa bank_mask:0xf\n"
            "v_add_f32_dpp v104, v104, v104 row_bcast:15 row_mask:0xa bank_mask:0xf\n"
            "v_add_f32_dpp v93, v93, v93 row_bcast:15 row_mask:0xa bank_mask:0xf\n"
            "v_max_u32_dpp v94, v94, v94 row_bcast:31 row_mask:0xc bank_mask:0xf\n"
            "v_max_u32_dpp v92, v92, v92 row_bcast:31 row_mask:0xc bank_mask:0xf\n"
            "v_add_f32_dpp v104, v104, v104 row_bcast:31 row_mask:0xc bank_mask:0xf\n"
            "v_add_f32_dpp v93, v93, v93 row_bcast:31 row_mask:0xc bank_mask:0xf\n"
            "s_nop 1\n"
            "v_readlane_b32 s84, v94, 63\n v_readlane_b32 s85, v92, 63\n v_readlane_b32 s86, v104, 63\n v_readlane_b32 s87, v93, 63\n"
            "s_mov_b32 %[mode], 0\n"
            "s_max_u32 s96, s84, s85\n s_cmp_ge_u32 s96, 0x7f00\n s_cselect_b32 %[mode], 2, 0\n"
            "s_cmp_eq_u32 s84, 0\n s_cselect_b32 s84, 0x3f80, s84\n s_cselect_b32 s86, 0x7f000000, s86\n"
            "s_cmp_eq_u32 s85, 0\n s_cselect_b32 s85, 0x3f80, s85\n s_cselect_b32 s87, 0x7f000000, s87\n"
            "s_min_u32 s96, s84, s85\n s_cmp_lt_u32 s96, 0x800\n s_cselect_b32 %[mode], 2, %[mode]\n"
            "s_cmp_lg_u32 %[mode], 0\n"
            "s_cbranch_scc1 Lend%=\n"
            "s_lshl_b32 s84, s84, 16\n s_lshl_b32 s85, s85, 16\n"
            "v_mov_b32 v104, s84\n v_mov_b32 v105, s85\n"
            "v_mul_f32 v106, 0x3e82d0e5, v104\n v_mul_f32 v108, 0x3e82d0e5, v105\n"
            "v_mul_f32 v104, v104, v104\n v_mul_f32 v105, v105, v105\n"
            "v_add_u32 v106, 0x7fffff, v106\n v_add_u32 v108, 0x7fffff, v108\n"
            "v_mul_f32 v104, %[c4], v104\n v_mul_f32 v105, %[c4], v105\n"
            "v_and_b32 v106, 0x7f800000, v106\n v_and_b32 v108, 0x7f800000, v108\n"
            "v_cmp_lt_f32 vcc, s86, v104\n v_cmp_lt_f32 s[88:89], s87, v105\n"
            "v_add_u32 v86, 0xc00000, v106\n v_add_u32 v87, 0xc00000, v108\n"
            "v_lshrrev_b32 v106, 23, v106\n v_lshrrev_b32 v108, 15, v108\n"
            "v_or_b32 %[exs], v106, v108\n"
            "s_or_b64 vcc, vcc, s[88:89]\n"
            "s_cselect_b32 %[mode], 1, 0\n"
            "s_cbranch_scc1 Lend%=\n"
            "s_waitcnt vmcnt(8)\n"
            "s_mov_b32 m0, %[wb]\n"
            "s_nop 0\n"
            "ds_write_addtid_b32 v40 offset:0\n"
            "ds_write_addtid_b32 v41 offset:256\n"
            "ds_write_addtid_b32 v42 offset:512\n"
            "ds_write_addtid_b32 v43 offset:768\n"
            "ds_write_addtid_b32 v44 offset:16384\n"
            "ds_write_addtid_b32 v45 offset:16640\n"
            "ds_write_addtid_b32 v46 offset:16896\n"
            "ds_write_addtid_b32 v47 offset:17152\n"
            "ds_write_addtid_b32 v48 offset:32768\n"
            "ds_write_addtid_b32 v49 offset:33024\n"
            "ds_write_addtid_b32 v50 offset:33280\n"
            "ds_write_addtid_b32 v51 offset:33536\n"
            "ds_write_addtid_b32 v52 offset:49152\n"
            "ds_write_addtid_b32 v53 offset:49408\n"
            "ds_write_addtid_b32 v54 offset:49664\n"
            "ds_write_addtid_b32 v55 offset:49920\n"
            "s_add_u32 m0, %[wb], 16384\n"
            "s_nop 0\n"
            "ds_write_addtid_b32 v56 offset:49152\n"
            "ds_write_addtid_b32 v57 offset:49408\n"
            "ds_write_addtid_b32 v58 offset:49664\n"
            "ds_write_addtid_b32 v59 offset:49920\n"
            "s_mov_b32 s92, 0\n"
            "s_and_b32 s75, s76, 255\n"
            "s_movk_i32 s74, 1000\n"
            "s_sub_i32 s79, s75, %[j0]\n"
            "s_max_i32 s79, s79, 8\n"
            "s_cmp_eq_u32 s92, 7\n s_cselect_b32 s79, 8, s79\n"
            "s_setprio 3\n"
            "s_waitcnt lgkmcnt(0)\n"
            "s_barrier\n"
            "s_waitcnt vmcnt(7)\n"
            "v_and_b32 v104, 0x1fffc, v8\n"
            "v_and_b32 v105, 0x1fffc, v9\n"
            "v_and_b32 v106, 0x1fffc, v10\n"
            "ds_read_b32 v93, v104\n"
            "ds_read_b32 v95, v105\n"
            "ds_read_b32 v97, v106\n"
            "s_waitcnt lgkmcnt(0)\n"
            "Ls0_%=:\n"
            "s_cmp_gt_i32 s75, s74\n"
            "s_cbranch_scc1 1f\n"
            "s_waitcnt vmcnt(6)\n"
            "1:\n"
            "s_waitcnt vmcnt(11)\n"
            "s_cmp_eq_u32 s75, s79\n"
            "s_cbranch_scc1 Le0_%=\n"
            "Lc0_%=:\n"
            "v_and_b32 v104, 0x1fffc, v12\n"
            "v_and_b32 v105, 0x1fffc, v13\n"
            "v_and_b32 v106, 0x1fffc, v14\n"
            "ds_read_b32 v99, v104\n"
            "ds_read_b32 v101, v105\n"
            "ds_read_b32 v103, v106\n"
            "v_add_u32_dpp v111, v81, v82 row_newbcast:0 row_mask:0xf bank_mask:0x3\n"
            "v_add_u32_dpp v111, v81, v82 row_newbcast:8 row_mask:0xf bank_mask:0xc\n"
            "v_and_b32 v108, 0x7fe, v11\n"
            "v_and_b32 v109, 0x1ff800, v11\n"
            "v_lshrrev_b32 v110, 22, v11\n"
            "v_lshrrev_b32 v112, 15, v8\n"
            "v_lshrrev_b32 v113, 15, v9\n"
            "v_lshrrev_b32 v114, 15, v10\n"
            "v_and_b32 v111, " IDXMASK ", v111\n"
            "v_min_u32 v111, v111, v83\n"
            "buffer_load_dwordx4 v[8:11], v111, %[rs], 0 idxen " RAUX "\n"
            "v_fmamk_f32 v108, v108, 0x7e800000, v84\n"
            "v_fmamk_f32 v109, v109, 0x79800000, v84\n"
            "v_fmamk_f32 v110, v110, 0x7f000000, v84\n"
            "v_or_b32 v112, 3, v112\n"
            "v_or_b32 v113, 3, v113\n"
            "v_or_b32 v114, 3, v114\n"
            "s_sub_u32 s75, s75, 1\n"
            "s_waitcnt lgkmcnt(6)\n"
            "v_lshlrev_b32 v92, 16, v93\n"
            "v_lshlrev_b32 v94, 16, v95\n"
            "v_lshlrev_b32 v96, 16, v97\n"
            "v_pk_fma_f32 v[92:93], v[92:93], v[108:109], v[86:87] op_sel:[0,0,0] op_sel_hi:[1,0,1]\n"
            "v_pk_fma_f32 v[94:95], v[94:95], v[108:109], v[86:87] op_sel:[0,1,0] op_sel_hi:[1,1,1]\n"
            "v_pk_fma_f32 v[96:97], v[96:97], v[110:111], v[86:87] op_sel:[0,0,0] op_sel_hi:[1,0,1]\n"
            "v_lshl_add_u32 v92, v93, 16, v92\n"
            "v_lshl_add_u32 v94, v95, 16, v94\n"
            "v_lshl_add_u32 v96, v97, 16, v96\n"
            "v_sub_u32 v92, v92, v86\n"
            "v_mad_i32_i16 v88, v93, v112, v88\n"
            "ds_add_u32 v112, v92 offset:65533\n"
            "v_sub_u32 v94, v94, v86\n"
            "v_mad_i32_i16 v88, v95, v113, v88\n"
            "ds_add_u32 v113, v94 offset:65533\n"
            "v_sub_u32 v96, v96, v86\n"
            "v_mad_i32_i16 v88, v97, v114, v88\n"
            "ds_add_u32 v114, v96 offset:65533\n"
            "Ls1_%=:\n"
            "s_cmp_gt_i32 s75, s74\n"
            "s_cbranch_scc1 1f\n"
            "s_waitcnt vmcnt(6)\n"
            "1:\n"
            "s_waitcnt vmcnt(11)\n"
            "s_cmp_eq_u32 s75, s79\n"
            "s_cbranch_scc1 Le1_%=\n"
            "Lc1_%=:\n"
            "v_and_b32 v104, 0x1fffc, v16\n"
            "v_and_b32 v105, 0x1fffc, v17\n"
            "v_and_b32 v106, 0x1fffc, v18\n"
            "ds_read_b32 v93, v104\n"
            "ds_read_b32 v95, v105\n"
            "ds_read_b32 v97, v106\n"
            "v_add_u32_dpp v111, v81, v82 row_newbcast:1 row_mask:0xf bank_mask:0x3\n"
            "v_add_u32_dpp v111, v81, v82 row_newbcast:9 row_mask:0xf bank_mask:0xc\n"
            "v_and_b32 v108, 0x7fe, v15\n"
            "v_and_b32 v109, 0x1ff800, v15\n"
            "v_lshrrev_b32 v110, 22, v15\n"
            "v_lshrrev_b32 v112, 15, v12\n"
            "v_lshrrev_b32 v113, 15, v13\n"
            "v_lshrrev_b32 v114, 15, v14\n"
            "v_and_b32 v111, " IDXMASK ", v111\n"
            "v_min_u32 v111, v111, v83\n"
            "buffer_load_dwordx4 v[12:15], v111, %[rs], 0 idxen " RAUX "\n"
            "v_fmamk_f32 v108, v108, 0x7e800000, v84\n"
            "v_fmamk_f32 v109, v109, 0x79800000, v84\n"
            "v_fmamk_f32 v110, v110, 0x7f000000, v84\n"
            "v_or_b32 v112, 3, v112\n"
            "v_or_b32 v113, 3, v113\n"
            "v_or_b32 v114, 3, v114\n"
            "s_sub_u32 s75, s75, 1\n"
            "s_waitcnt lgkmcnt(6)\n"
            "v_lshlrev_b32 v98, 16, v99\n"
            "v_lshlrev_b32 v100, 16, v101\n"
            "v_lshlrev_b32 v102, 16, v103\n"
            "v_pk_fma_f32 v[98:99], v[98:99], v[108:109], v[86:87] op_sel:[0,0,0] op_sel_hi:[1,0,1]\n"
            "v_pk_fma_f32 v[100:101], v[100:101], v[108:109], v[86:87] op_sel:[0,1,0] op_sel_hi:[1,1,1]\n"
            "v_pk_fma_f32 v[102:103], v[102:103], v[110:111], v[86:87] op_sel:[0,0,0] op_sel_hi:[1,0,1]\n"
            "v_lshl_add_u32 v98, v99, 16, v98\n"
            "v_lshl_add_u32 v100, v101, 16, v100\n"
            "v_lshl_add_u32 v102, v103, 16, v102\n"
            "v_sub_u32 v98, v98, v86\n"
            "v_mad_i32_i16 v88, v99, v112, v88\n"
            "ds_add_u32 v112, v98 offset:65533\n"
            "v_sub_u32 v100, v100, v86\n"
            "v_mad_i32_i16 v88, v101, v113, v88\n"
            "ds_add_u32 v113, v100 offset:65533\n"
            "v_sub_u32 v102, v102, v86\n"
            "v_mad_i32_i16 v88, v103, v114, v88\n"
            "ds_add_u32 v114, v102 offset:65533\n"
            "Ls2_%=:\n"
            "s_cmp_gt_i32 s75, s74\n"
            "s_cbranch_scc1 1f\n"
            "s_waitcnt vmcnt(6)\n"
            "1:\n"
            "s_waitcnt vmcnt(11)\n"
            "s_cmp_eq_u32 s75, s79\n"
            "s_cbranch_scc1 Le2_%=\n"
            "Lc2_%=:\n"
            "v_and_b32 v104, 0x1fffc, v20\n"
            "v_and_b32 v105, 0x1fffc, v21\n"
            "v_and_b32 v106, 0x1fffc, v22\n"
            "ds_read_b32 v99, v104\n"
            "ds_read_b32 v101, v105\n"
            "ds_read_b32 v103, v106\n"
            "v_add_u32_dpp v111, v81, v82 row_newbcast:2 row_mask:0xf bank_mask:0x3\n"
            "v_add_u32_dpp v111, v81, v82 row_newbcast:10 row_mask:0xf bank_mask:0xc\n"
            "v_and_b32 v108, 0x7fe, v19\n"
            "v_and_b32 v109, 0x1ff800, v19\n"
            "v_lshrrev_b32 v110, 22, v19\n"
            "v_lshrrev_b32 v112, 15, v16\n"
            "v_lshrrev_b32 v113, 15, v17\n"
            "v_lshrrev_b32 v114, 15, v18\n"
            "v_and_b32 v111, " IDXMASK ", v111\n"
            "v_min_u32 v111, v111, v83\n"
            "buffer_load_dwordx4 v[16:19], v111, %[rs], 0 idxen " RAUX "\n"
            "v_fmamk_f32 v108, v108, 0x7e800000, v84\n"
            "v_fmamk_f32 v109, v109, 0x79800000, v84\n"
            "v_fmamk_f32 v110, v110, 0x7f000000, v84\n"
            "v_or_b32 v112, 3, v112\n"
            "v_or_b32 v113, 3, v113\n"
            "v_or_b32 v114, 3, v114\n"
            "s_sub_u32 s75, s75, 1\n"
            "s_waitcnt lgkmcnt(6)\n"
            "v_lshlrev_b32 v92, 16, v93\n"
            "v_lshlrev_b32 v94, 16, v95\n"
            "v_lshlrev_b32 v96, 16, v97\n"
            "v_pk_fma_f32 v[92:93], v[92:93], v[108:109], v[86:87] op_sel:[0,0,0] op_sel_hi:[1,0,1]\n"
            "v_pk_fma_f32 v[94:95], v[94:95], v[108:109], v[86:87] op_sel:[0,1,0] op_sel_hi:[1,1,1]\n"
            "v_pk_fma_f32 v[96:97], v[96:97], v[110:111], v[86:87] op_sel:[0,0,0] op_sel_hi:[1,0,1]\n"
            "v_lshl_add_u32 v92, v93, 16, v92\n"
            "v_lshl_add_u32 v94, v95, 16, v94\n"
            "v_lshl_add_u32 v96, v97, 16, v96\n"
            "v_sub_u32 v92, v92, v86\n"
            "v_mad_i32_i16 v88, v93, v112, v88\n"
            "ds_add_u32 v112, v92 offset:65533\n"
            "v_sub_u32 v94, v94, v86\n"
            "v_mad_i32_i16 v88, v95, v113, v88\n"
            "ds_add_u32 v113, v94 offset:65533\n"
            "v_sub_u32 v96, v96, v86\n"
            "v_mad_i32_i16 v88, v97, v114, v88\n"
            "ds_add_u32 v114, v96 offset:65533\n"
            "Ls3_%=:\n"
            "s_cmp_gt_i32 s75, s74\n"
            "s_cbranch_scc1 1f\n"
            "s_waitcnt vmcnt(6)\n"
            "1:\n"
            "s_waitcnt vmcnt(11)\n"
            "s_cmp_eq_u32 s75, s79\n"
            "s_cbranch_scc1 Le3_%=\n"
            "Lc3_%=:\n"
            "v_and_b32 v104, 0x1fffc, v24\n"
            "v_and_b32 v105, 0x1fffc, v25\n"
            "v_and_b32 v106, 0x1fffc, v26\n"
            "ds_read_b32 v93, v104\n"
            "ds_read_b32 v95, v105\n"
            "ds_read_b32 v97, v106\n"
            "v_add_u32_dpp v111, v81, v82 row_newbcast:3 row_mask:0xf bank_mask:0x3\n"
            "v_add_u32_dpp v111, v81, v82 row_newbcast:11 row_mask:0xf bank_mask:0xc\n"
            "v_and_b32 v108, 0x7fe, v23\n"
            "v_and_b32 v109, 0x1ff800, v23\n"
            "v_lshrrev_b32 v110, 22, v23\n"
            "v_lshrrev_b32 v112, 15, v20\n"
            "v_lshrrev_b32 v113, 15, v21\n"
            "v_lshrrev_b32 v114, 15, v22\n"
            "v_and_b32 v111, " IDXMASK ", v111\n"
            "v_min_u32 v111, v111, v83\n"
            "buffer_load_dwordx4 v[20:23], v111, %[rs], 0 idxen " RAUX "\n"
            "v_fmamk_f32 v108, v108, 0x7e800000, v84\n"
            "v_fmamk_f32 v109, v109, 0x79800000, v84\n"
            "v_fmamk_f32 v110, v110, 0x7f000000, v84\n"
            "v_or_b32 v112, 3, v112\n"
            "v_or_b32 v113, 3, v113\n"
            "v_or_b32 v114, 3, v114\n"
            "s_sub_u32 s75, s75, 1\n"
            "s_waitcnt lgkmcnt(6)\n"
            "v_lshlrev_b32 v98, 16, v99\n"
            "v_lshlrev_b32 v100, 16, v101\n"
            "v_lshlrev_b32 v102, 16, v103\n"
            "v_pk_fma_f32 v[98:99], v[98:99], v[108:109], v[86:87] op_sel:[0,0,0] op_sel_hi:[1,0,1]\n"
            "v_pk_fma_f32 v[100:101], v[100:101], v[108:109], v[86:87] op_sel:[0,1,0] op_sel_hi:[1,1,1]\n"
            "v_pk_fma_f32 v[102:103], v[102:103], v[110:111], v[86:87] op_sel:[0,0,0] op_sel_hi:[1,0,1]\n"
            "v_lshl_add_u32 v98, v99, 16, v98\n"
            "v_lshl_add_u32 v100, v101, 16, v100\n"
            "v_lshl_add_u32 v102, v103, 16, v102\n"
            "v_sub_u32 v98, v98, v86\n"
            "v_mad_i32_i16 v88, v99, v112, v88\n"
            "ds_add_u32 v112, v98 offset:65533\n"
            "v_sub_u32 v100, v100, v86\n"
            "v_mad_i32_i16 v88, v101, v113, v88\n"
            "ds_add_u32 v113, v100 offset:65533\n"
            "v_sub_u32 v102, v102, v86\n"
            "v_mad_i32_i16 v88, v103, v114, v88\n"
            "ds_add_u32 v114, v102 offset:65533\n"
            "Ls4_%=:\n"
            "s_cmp_gt_i32 s75, s74\n"
            "s_cbranch_scc1 1f\n"
            "s_waitcnt vmcnt(6)\n"
            "1:\n"
            "s_waitcnt vmcnt(11)\n"
            "s_cmp_eq_u32 s75, s79\n"
            "s_cbranch_scc1 Le4_%=\n"
            "Lc4_%=:\n"
            "v_and_b32 v104, 0x1fffc, v28\n"
            "v_and_b32 v105, 0x1fffc, v29\n"
            "v_and_b32 v106, 0x1fffc, v30\n"
            "ds_read_b32 v99, v104\n"
            "ds_read_b32 v101, v105\n"
            "ds_read_b32 v103, v106\n"
            "v_add_u32_dpp v111, v81, v82 row_newbcast:4 row_mask:0xf bank_mask:0x3\n"
            "v_add_u32_dpp v111, v81, v82 row_newbcast:12 row_mask:0xf bank_mask:0xc\n"
            "v_and_b32 v108, 0x7fe, v27\n"
            "v_and_b32 v109, 0x1ff800, v27\n"
            "v_lshrrev_b32 v110, 22, v27\n"
            "v_lshrrev_b32 v112, 15, v24\n"
            "v_lshrrev_b32 v113, 15, v25\n"
            "v_lshrrev_b32 v114, 15, v26\n"
            "v_and_b32 v111, " IDXMASK ", v111\n"
            "v_min_u32 v111, v111, v83\n"
            "buffer_load_dwordx4 v[24:27], v111, %[rs], 0 idxen " RAUX "\n"
            "v_fmamk_f32 v108, v108, 0x7e800000, v84\n"
            "v_fmamk_f32 v109, v109, 0x79800000, v84\n"
            "v_fmamk_f32 v110, v110, 0x7f000000, v84\n"
            "v_or_b32 v112, 3, v112\n"
            "v_or_b32 v113, 3, v113\n"
            "v_or_b32 v114, 3, v114\n"
            "s_sub_u32 s75, s75, 1\n"
            "s_waitcnt lgkmcnt(6)\n"
            "v_lshlrev_b32 v92, 16, v93\n"
            "v_lshlrev_b32 v94, 16, v95\n"
            "v_lshlrev_b32 v96, 16, v97\n"
            "v_pk_fma_f32 v[92:93], v[92:93], v[108:109], v[86:87] op_sel:[0,0,0] op_sel_hi:[1,0,1]\n"
            "v_pk_fma_f32 v[94:95], v[94:95], v[108:109], v[86:87] op_sel:[0,1,0] op_sel_hi:[1,1,1]\n"
            "v_pk_fma_f32 v[96:97], v[96:97], v[110:111], v[86:87] op_sel:[0,0,0] op_sel_hi:[1,0,1]\n"
            "v_lshl_add_u32 v92, v93, 16, v92\n"
            "v_lshl_add_u32 v94, v95, 16, v94\n"
            "v_lshl_add_u32 v96, v97, 16, v96\n"
            "v_sub_u32 v92, v92, v86\n"
            "v_mad_i32_i16 v88, v93, v112, v88\n"
            "ds_add_u32 v112, v92 offset:65533\n"
            "v_sub_u32 v94, v94, v86\n"
            "v_mad_i32_i16 v88, v95, v113, v88\n"
            "ds_add_u32 v113, v94 offset:65533\n"
            "v_sub_u32 v96, v96, v86\n"
            "v_mad_i32_i16 v88, v97, v114, v88\n"
            "ds_add_u32 v114, v96 offset:65533\n"
            "Ls5_%=:\n"
            "s_cmp_gt_i32 s75, s74\n"
            "s_cbranch_scc1 1f\n"
            "s_waitcnt vmcnt(6)\n"
            "1:\n"
            "s_waitcnt vmcnt(11)\n"
            "s_cmp_eq_u32 s75, s79\n"
            "s_cbranch_scc1 Le5_%=\n"
            "Lc5_%=:\n"
            "v_and_b32 v104, 0x1fffc, v32\n"
            "v_and_b32 v105, 0x1fffc, v33\n"
            "v_and_b32 v106, 0x1fffc, v34\n"
            "ds_read_b32 v93, v104\n"
            "ds_read_b32 v95, v105\n"
            "ds_read_b32 v97, v106\n"
            "v_add_u32_dpp v111, v81, v82 row_newbcast:5 row_mask:0xf bank_mask:0x3\n"
            "v_add_u32_dpp v111, v81, v82 row_newbcast:13 row_mask:0xf bank_mask:0xc\n"
            "v_and_b32 v108, 0x7fe, v31\n"
            "v_and_b32 v109, 0x1ff800, v31\n"
            "v_lshrrev_b32 v110, 22, v31\n"
            "v_lshrrev_b32 v112, 15, v28\n"
            "v_lshrrev_b32 v113, 15, v29\n"
            "v_lshrrev_b32 v114, 15, v30\n"
            "v_and_b32 v111, " IDXMASK ", v111\n"
            "v_min_u32 v111, v111, v83\n"
            "buffer_load_dwordx4 v[28:31], v111, %[rs], 0 idxen " RAUX "\n"
            "v_fmamk_f32 v108, v108, 0x7e800000, v84\n"
            "v_fmamk_f32 v109, v109, 0x79800000, v84\n"
            "v_fmamk_f32 v110, v110, 0x7f000000, v84\n"
            "v_or_b32 v112, 3, v112\n"
            "v_or_b32 v113, 3, v113\n"
            "v_or_b32 v114, 3, v114\n"
            "s_sub_u32 s75, s75, 1\n"
            "s_waitcnt lgkmcnt(6)\n"
            "v_lshlrev_b32 v98, 16, v99\n"
            "v_lshlrev_b32 v100, 16, v101\n"
            "v_lshlrev_b32 v102, 16, v103\n"
            "v_pk_fma_f32 v[98:99], v[98:99], v[108:109], v[86:87] op_sel:[0,0,0] op_sel_hi:[1,0,1]\n"
            "v_pk_fma_f32 v[100:101], v[100:101], v[108:109], v[86:87] op_sel:[0,1,0] op_sel_hi:[1,1,1]\n"
            "v_pk_fma_f32 v[102:103], v[102:103], v[110:111], v[86:87] op_sel:[0,0,0] op_sel_hi:[1,0,1]\n"
            "v_lshl_add_u32 v98, v99, 16, v98\n"
            "v_lshl_add_u32 v100, v101, 16, v100\n"
            "v_lshl_add_u32 v102, v103, 16, v102\n"
            "v_sub_u32 v98, v98, v86\n"
            "v_mad_i32_i16 v88, v99, v112, v88\n"
            "ds_add_u32 v112, v98 offset:65533\n"
            "v_sub_u32 v100, v100, v86\n"
            "v_mad_i32_i16 v88, v101, v113, v88\n"
            "ds_add_u32 v113, v100 offset:65533\n"
            "v_sub_u32 v102, v102, v86\n"
            "v_mad_i32_i16 v88, v103, v114, v88\n"
            "ds_add_u32 v114, v102 offset:65533\n"
            "Ls6_%=:\n"
            "s_cmp_gt_i32 s75, s74\n"
            "s_cbranch_scc1 1f\n"
            "s_waitcnt vmcnt(6)\n"
            "1:\n"
            "s_waitcnt vmcnt(11)\n"
            "s_cmp_eq_u32 s75, s79\n"
            "s_cbranch_scc1 Le6_%=\n"
            "Lc6_%=:\n"
            "v_and_b32 v104, 0x1fffc, v36\n"
            "v_and_b32 v105, 0x1fffc, v37\n"
            "v_and_b32 v106, 0x1fffc, v38\n"
            "ds_read_b32 v99, v104\n"
            "ds_read_b32 v101, v105\n"
            "ds_read_b32 v103, v106\n"
            "v_add_u32_dpp v111, v81, v82 row_newbcast:6 row_mask:0xf bank_mask:0x3\n"
            "v_add_u32_dpp v111, v81, v82 row_newbcast:14 row_mask:0xf bank_mask:0xc\n"
            "v_and_b32 v108, 0x7fe, v35\n"
            "v_and_b32 v109, 0x1ff800, v35\n"
            "v_lshrrev_b32 v110, 22, v35\n"
            "v_lshrrev_b32 v112, 15, v32\n"
            "v_lshrrev_b32 v113, 15, v33\n"
            "v_lshrrev_b32 v114, 15, v34\n"
            "v_and_b32 v111, " IDXMASK ", v111\n"
            "v_min_u32 v111, v111, v83\n"
            "buffer_load_dwordx4 v[32:35], v111, %[rs], 0 idxen " RAUX "\n"
            "v_fmamk_f32 v108, v108, 0x7e800000, v84\n"
            "v_fmamk_f32 v109, v109, 0x79800000, v84\n"
            "v_fmamk_f32 v110, v110, 0x7f000000, v84\n"
            "v_or_b32 v112, 3, v112\n"
            "v_or_b32 v113, 3, v113\n"
            "v_or_b32 v114, 3, v114\n"
            "s_sub_u32 s75, s75, 1\n"
            "s_waitcnt lgkmcnt(6)\n"
            "v_lshlrev_b32 v92, 16, v93\n"
            "v_lshlrev_b32 v94, 16, v95\n"
            "v_lshlrev_b32 v96, 16, v97\n"
            "v_pk_fma_f32 v[92:93], v[92:93], v[108:109], v[86:87] op_sel:[0,0,0] op_sel_hi:[1,0,1]\n"
            "v_pk_fma_f32 v[94:95], v[94:95], v[108:109], v[86:87] op_sel:[0,1,0] op_sel_hi:[1,1,1]\n"
            "v_pk_fma_f32 v[96:97], v[96:97], v[110:111], v[86:87] op_sel:[0,0,0] op_sel_hi:[1,0,1]\n"
            "v_lshl_add_u32 v92, v93, 16, v92\n"
            "v_lshl_add_u32 v94, v95, 16, v94\n"
            "v_lshl_add_u32 v96, v97, 16, v96\n"
            "v_sub_u32 v92, v92, v86\n"
            "v_mad_i32_i16 v88, v93, v112, v88\n"
            "ds_add_u32 v112, v92 offset:65533\n"
            "v_sub_u32 v94, v94, v86\n"
            "v_mad_i32_i16 v88, v95, v113, v88\n"
            "ds_add_u32 v113, v94 offset:65533\n"
            "v_sub_u32 v96, v96, v86\n"
            "v_mad_i32_i16 v88, v97, v114, v88\n"
            "ds_add_u32 v114, v96 offset:65533\n"
            "Ls7_%=:\n"
            "s_cmp_gt_i32 s75, s74\n"
            "s_cbranch_scc1 1f\n"
            "s_waitcnt vmcnt(6)\n"
            "1:\n"
            "s_waitcnt vmcnt(11)\n"
            "s_cmp_eq_u32 s75, s79\n"
            "s_cbranch_scc1 Le7_%=\n"
            "Lc7_%=:\n"
            "v_and_b32 v104, 0x1fffc, v8\n"
            "v_and_b32 v105, 0x1fffc, v9\n"
            "v_and_b32 v106, 0x1fffc, v10\n"
            "ds_read_b32 v93, v104\n"
            "ds_read_b32 v95, v105\n"
            "ds_read_b32 v97, v106\n"
            "v_add_u32_dpp v111, v81, v82 row_newbcast:7 row_mask:0xf bank_mask:0x3\n"
            "v_add_u32_dpp v111, v81, v82 row_newbcast:15 row_mask:0xf bank_mask:0xc\n"
            "v_and_b32 v108, 0x7fe, v39\n"
            "v_and_b32 v109, 0x1ff800, v39\n"
            "v_lshrrev_b32 v110, 22, v39\n"
            "v_lshrrev_b32 v112, 15, v36\n"
            "v_lshrrev_b32 v113, 15, v37\n"
            "v_lshrrev_b32 v114, 15, v38\n"
            "v_and_b32 v111, " IDXMASK ", v111\n"
            "v_min_u32 v111, v111, v83\n"
            "buffer_load_dwordx4 v[36:39], v111, %[rs], 0 idxen " RAUX "\n"
            "s_min_u32 s82, s82, 19\n"
            "s_add_u32 s82, s82, 1\n"
            "s_set_gpr_idx_on s82, gpr_idx(SRC0)\n"
            "s_nop 0\n"
            "v_mov_b32 v81, v60\n"
            "s_set_gpr_idx_off\n"
            "v_fmamk_f32 v108, v108, 0x7e800000, v84\n"
            "v_fmamk_f32 v109, v109, 0x79800000, v84\n"
            "v_fmamk_f32 v110, v110, 0x7f000000, v84\n"
            "v_or_b32 v112, 3, v112\n"
            "v_or_b32 v113, 3, v113\n"
            "v_or_b32 v114, 3, v114\n"
            "s_sub_u32 s75, s75, 1\n"
            "s_waitcnt lgkmcnt(6)\n"
            "v_lshlrev_b32 v98, 16, v99\n"
            "v_lshlrev_b32 v100, 16, v101\n"
            "v_lshlrev_b32 v102, 16, v103\n"
            "v_pk_fma_f32 v[98:99], v[98:99], v[108:109], v[86:87] op_sel:[0,0,0] op_sel_hi:[1,0,1]\n"
            "v_pk_fma_f32 v[100:101], v[100:101], v[108:109], v[86:87] op_sel:[0,1,0] op_sel_hi:[1,1,1]\n"
            "v_pk_fma_f32 v[102:103], v[102:103], v[110:111], v[86:87] op_sel:[0,0,0] op_sel_hi:[1,0,1]\n"
            "v_lshl_add_u32 v98, v99, 16, v98\n"
            "v_lshl_add_u32 v100, v101, 16, v100\n"
            "v_lshl_add_u32 v102, v103, 16, v102\n"
            "v_sub_u32 v98, v98, v86\n"
            "v_mad_i32_i16 v88, v99, v112, v88\n"
            "ds_add_u32 v112, v98 offset:65533\n"
            "v_sub_u32 v100, v100, v86\n"
            "v_mad_i32_i16 v88, v101, v113, v88\n"
            "ds_add_u32 v113, v100 offset:65533\n"
            "v_sub_u32 v102, v102, v86\n"
            "v_mad_i32_i16 v88, v103, v114, v88\n"
            "ds_add_u32 v114, v102 offset:65533\n"
            "s_branch Ls0_%=\n"
            "Le0_%=:\n"
            "s_cmp_lt_u32 s75, 8\n"
            "s_cbranch_scc1 1f\n"
            "s_swappc_b64 s[80:81], s[66:67]\n"
            "s_branch Lc0_%=\n"
            "1:\n"
            "s_cmp_lg_u32 s75, 7\n"
            "s_cbranch_scc1 1f\n"
            "s_setprio 2\n"
            "s_mov_b32 s79, 3\n"
            "s_branch Lc0_%=\n"
            "1:\n"
            "s_cmp_lg_u32 s75, 3\n"
            "s_cbranch_scc1 1f\n"
            "s_setprio 1\n"
            "s_mov_b32 s79, 1\n"
            "s_branch Lc0_%=\n"
            "1:\n"
            "s_setprio 0\n"
            "v_add_u32_dpp v111, v81, v82 row_newbcast:0 row_mask:0xf bank_mask:0x3\n"
            "v_add_u32_dpp v111, v81, v82 row_newbcast:8 row_mask:0xf bank_mask:0xc\n"
            "v_and_b32 v108, 0x7fe, v11\n"
            "v_and_b32 v109, 0x1ff800, v11\n"
            "v_lshrrev_b32 v110, 22, v11\n"
            "v_lshrrev_b32 v112, 15, v8\n"
            "v_lshrrev_b32 v113, 15, v9\n"
            "v_lshrrev_b32 v114, 15, v10\n"
            "v_and_b32 v111, " IDXMASK ", v111\n"
            "v_min_u32 v111, v111, v83\n"
            "buffer_load_dwordx4 v[8:11], v111, %[rs], 0 idxen " RAUX "\n"
            "v_fmamk_f32 v108, v108, 0x7e800000, v84\n"
            "v_fmamk_f32 v109, v109, 0x79800000, v84\n"
            "v_fmamk_f32 v110, v110, 0x7f000000, v84\n"
            "v_or_b32 v112, 3, v112\n"
            "v_or_b32 v113, 3, v113\n"
            "v_or_b32 v114, 3, v114\n"
            "s_waitcnt lgkmcnt(3)\n"
            "s_swappc_b64 s[80:81], s[70:71]\n"
            "v_lshlrev_b32 v92, 16, v93\n"
            "v_lshlrev_b32 v94, 16, v95\n"
            "v_lshlrev_b32 v96, 16, v97\n"
            "v_pk_fma_f32 v[92:93], v[92:93], v[108:109], v[86:87] op_sel:[0,0,0] op_sel_hi:[1,0,1]\n"
            "v_pk_fma_f32 v[94:95], v[94:95], v[108:109], v[86:87] op_sel:[0,1,0] op_sel_hi:[1,1,1]\n"
            "v_pk_fma_f32 v[96:97], v[96:97], v[110:111], v[86:87] op_sel:[0,0,0] op_sel_hi:[1,0,1]\n"
            "v_lshl_add_u32 v92, v93, 16, v92\n"
            "v_lshl_add_u32 v94, v95, 16, v94\n"
            "v_lshl_add_u32 v96, v97, 16, v96\n"
            "v_sub_u32 v92, v92, v86\n"
            "v_mad_i32_i16 v88, v93, v112, v88\n"
            "ds_add_u32 v112, v92 offset:65533\n"
            "v_sub_u32 v94, v94, v86\n"
            "v_mad_i32_i16 v88, v95, v113, v88\n"
            "ds_add_u32 v113, v94 offset:65533\n"
            "v_sub_u32 v96, v96, v86\n"
            "v_mad_i32_i16 v88, v97, v114, v88\n"
            "ds_add_u32 v114, v96 offset:65533\n"
            "v_and_b32 v104, 0x1fffc, v12\n"
            "v_and_b32 v105, 0x1fffc, v13\n"
            "v_and_b32 v106, 0x1fffc, v14\n"
            "s_swappc_b64 s[80:81], s[72:73]\n"
            "ds_read_b32 v99, v104\n"
            "ds_read_b32 v101, v105\n"
            "ds_read_b32 v103, v106\n"
            "s_waitcnt lgkmcnt(0)\n"
            "s_branch Ls1_%=\n"
            "Le1_%=:\n"
            "s_cmp_lt_u32 s75, 8\n"
            "s_cbranch_scc1 1f\n"
            "s_swappc_b64 s[80:81], s[66:67]\n"
            "s_branch Lc1_%=\n"
            "1:\n"
            "s_cmp_lg_u32 s75, 7\n"
            "s_cbranch_scc1 1f\n"
            "s_setprio 2\n"
            "s_mov_b32 s79, 3\n"
            "s_branch Lc1_%=\n"
            "1:\n"
            "s_cmp_lg_u32 s75, 3\n"
            "s_cbranch_scc1 1f\n"
            "s_setprio 1\n"
            "s_mov_b32 s79, 1\n"
            "s_branch Lc1_%=\n"
            "1:\n"
            "s_setprio 0\n"
            "v_add_u32_dpp v111, v81, v82 row_newbcast:1 row_mask:0xf bank_mask:0x3\n"
            "v_add_u32_dpp v111, v81, v82 row_newbcast:9 row_mask:0xf bank_mask:0xc\n"
            "v_and_b32 v108, 0x7fe, v15\n"
            "v_and_b32 v109, 0x1ff800, v15\n"
            "v_lshrrev_b32 v110, 22, v15\n"
            "v_lshrrev_b32 v112, 15, v12\n"
            "v_lshrrev_b32 v113, 15, v13\n"
            "v_lshrrev_b32 v114, 15, v14\n"
            "v_and_b32 v111, " IDXMASK ", v111\n"
            "v_min_u32 v111, v111, v83\n"
            "buffer_load_dwordx4 v[12:15], v111, %[rs], 0 idxen " RAUX "\n"
            "v_fmamk_f32 v108, v108, 0x7e800000, v84\n"
            "v_fmamk_f32 v109, v109, 0x79800000, v84\n"
            "v_fmamk_f32 v110, v110, 0x7f000000, v84\n"
            "v_or_b32 v112, 3, v112\n"
            "v_or_b32 v113, 3, v113\n"
            "v_or_b32 v114, 3, v114\n"
            "s_waitcnt lgkmcnt(3)\n"
            "s_swappc_b64 s[80:81], s[70:71]\n"
            "v_lshlrev_b32 v98, 16, v99\n"
            "v_lshlrev_b32 v100, 16, v101\n"
            "v_lshlrev_b32 v102, 16, v103\n"
            "v_pk_fma_f32 v[98:99], v[98:99], v[108:109], v[86:87] op_sel:[0,0,0] op_sel_hi:[1,0,1]\n"
            "v_pk_fma_f32 v[100:101], v[100:101], v[108:109], v[86:87] op_sel:[0,1,0] op_sel_hi:[1,1,1]\n"
            "v_pk_fma_f32 v[102:103], v[102:103], v[110:111], v[86:87] op_sel:[0,0,0] op_sel_hi:[1,0,1]\n"
            "v_lshl_add_u32 v98, v99, 16, v98\n"
            "v_lshl_add_u32 v100, v101, 16, v100\n"
            "v_lshl_add_u32 v102, v103, 16, v102\n"
            "v_sub_u32 v98, v98, v86\n"
            "v_mad_i32_i16 v88, v99, v112, v88\n"
            "ds_add_u32 v112, v98 offset:65533\n"
            "v_sub_u32 v100, v100, v86\n"
            "v_mad_i32_i16 v88, v101, v113, v88\n"
            "ds_add_u32 v113, v100 offset:65533\n"
            "v_sub_u32 v102, v102, v86\n"
            "v_mad_i32_i16 v88, v103, v114, v88\n"
            "ds_add_u32 v114, v102 offset:65533\n"
            "v_and_b32 v104, 0x1fffc, v16\n"
            "v_and_b32 v105, 0x1fffc, v17\n"
            "v_and_b32 v106, 0x1fffc, v18\n"
            "s_swappc_b64 s[80:81], s[72:73]\n"
            "ds_read_b32 v93, v104\n"
            "ds_read_b32 v95, v105\n"
            "ds_read_b32 v97, v106\n"
            "s_waitcnt lgkmcnt(0)\n"
            "s_branch Ls2_%=\n"
            "Le2_%=:\n"
            "s_cmp_lt_u32 s75, 8\n"
            "s_cbranch_scc1 1f\n"
            "s_swappc_b64 s[80:81], s[66:67]\n"
            "s_branch Lc2_%=\n"
            "1:\n"
            "s_cmp_lg_u32 s75, 7\n"
            "s_cbranch_scc1 1f\n"
            "s_setprio 2\n"
            "s_mov_b32 s79, 3\n"
            "s_branch Lc2_%=\n"
            "1:\n"
            "s_cmp_lg_u32 s75, 3\n"
            "s_cbranch_scc1 1f\n"
            "s_setprio 1\n"
            "s_mov_b32 s79, 1\n"
            "s_branch Lc2_%=\n"
            "1:\n"
            "s_setprio 0\n"
            "v_add_u32_dpp v111, v81, v82 row_newbcast:2 row_mask:0xf bank_mask:0x3\n"
            "v_add_u32_dpp v111, v81, v82 row_newbcast:10 row_mask:0xf bank_mask:0xc\n"
            "v_and_b32 v108, 0x7fe, v19\n"
            "v_and_b32 v109, 0x1ff800, v19\n"
            "v_lshrrev_b32 v110, 22, v19\n"
            "v_lshrrev_b32 v112, 15, v16\n"
            "v_lshrrev_b32 v113, 15, v17\n"
            "v_lshrrev_b32 v114, 15, v18\n"
            "v_and_b32 v111, " IDXMASK ", v111\n"
            "v_min_u32 v111, v111, v83\n"
            "buffer_load_dwordx4 v[16:19], v111, %[rs], 0 idxen " RAUX "\n"
            "v_fmamk_f32 v108, v108, 0x7e800000, v84\n"
            "v_fmamk_f32 v109, v109, 0x79800000, v84\n"
            "v_fmamk_f32 v110, v110, 0x7f000000, v84\n"
            "v_or_b32 v112, 3, v112\n"
            "v_or_b32 v113, 3, v113\n"
            "v_or_b32 v114, 3, v114\n"
            "s_waitcnt lgkmcnt(3)\n"
            "s_swappc_b64 s[80:81], s[70:71]\n"
            "v_lshlrev_b32 v92, 16, v93\n"
            "v_lshlrev_b32 v94, 16, v95\n"
            "v_lshlrev_b32 v96, 16, v97\n"
            "v_pk_fma_f32 v[92:93], v[92:93], v[108:109], v[86:87] op_sel:[0,0,0] op_sel_hi:[1,0,1]\n"
            "v_pk_fma_f32 v[94:95], v[94:95], v[108:109], v[86:87] op_sel:[0,1,0] op_sel_hi:[1,1,1]\n"
            "v_pk_fma_f32 v[96:97], v[96:97], v[110:111], v[86:87] op_sel:[0,0,0] op_sel_hi:[1,0,1]\n"
            "v_lshl_add_u32 v92, v93, 16, v92\n"
            "v_lshl_add_u32 v94, v95, 16, v94\n"
            "v_lshl_add_u32 v96, v97, 16, v96\n"
            "v_sub_u32 v92, v92, v86\n"
            "v_mad_i32_i16 v88, v93, v112, v88\n"
            "ds_add_u32 v112, v92 offset:65533\n"
            "v_sub_u32 v94, v94, v86\n"
            "v_mad_i32_i16 v88, v95, v113, v88\n"
            "ds_add_u32 v113, v94 offset:65533\n"
            "v_sub_u32 v96, v96, v86\n"
            "v_mad_i32_i16 v88, v97, v114, v88\n"
            "ds_add_u32 v114, v96 offset:65533\n"
            "v_and_b32 v104, 0x1fffc, v20\n"
            "v_and_b32 v105, 0x1fffc, v21\n"
            "v_and_b32 v106, 0x1fffc, v22\n"
            "s_swappc_b64 s[80:81], s[72:73]\n"
            "ds_read_b32 v99, v104\n"
            "ds_read_b32 v101, v105\n"
            "ds_read_b32 v103, v106\n"
            "s_waitcnt lgkmcnt(0)\n"
            "s_branch Ls3_%=\n"
            "Le3_%=:\n"
            "s_cmp_lt_u32 s75, 8\n"
            "s_cbranch_scc1 1f\n"
            "s_swappc_b64 s[80:81], s[66:67]\n"
            "s_branch Lc3_%=\n"
            "1:\n"
            "s_cmp_lg_u32 s75, 7\n"
            "s_cbranch_scc1 1f\n"
            "s_setprio 2\n"
            "s_mov_b32 s79, 3\n"
            "s_branch Lc3_%=\n"
            "1:\n"
            "s_cmp_lg_u32 s75, 3\n"
            "s_cbranch_scc1 1f\n"
            "s_setprio 1\n"
            "s_mov_b32 s79, 1\n"
            "s_branch Lc3_%=\n"
            "1:\n"
            "s_setprio 0\n"
            "v_add_u32_dpp v111, v81, v82 row_newbcast:3 row_mask:0xf bank_mask:0x3\n"
            "v_add_u32_dpp v111, v81, v82 row_newbcast:11 row_mask:0xf bank_mask:0xc\n"
            "v_and_b32 v108, 0x7fe, v23\n"
            "v_and_b32 v109, 0x1ff800, v23\n"
            "v_lshrrev_b32 v110, 22, v23\n"
            "v_lshrrev_b32 v112, 15, v20\n"
            "v_lshrrev_b32 v113, 15, v21\n"
            "v_lshrrev_b32 v114, 15, v22\n"
            "v_and_b32 v111, " IDXMASK ", v111\n"
            "v_min_u32 v111, v111, v83\n"
            "buffer_load_dwordx4 v[20:23], v111, %[rs], 0 idxen " RAUX "\n"
            "v_fmamk_f32 v108, v108, 0x7e800000, v84\n"
            "v_fmamk_f32 v109, v109, 0x79800000, v84\n"
            "v_fmamk_f32 v110, v110, 0x7f000000, v84\n"
            "v_or_b32 v112, 3, v112\n"
            "v_or_b32 v113, 3, v113\n"
            "v_or_b32 v114, 3, v114\n"
            "s_waitcnt lgkmcnt(3)\n"
            "s_swappc_b64 s[80:81], s[70:71]\n"
            "v_lshlrev_b32 v98, 16, v99\n"
            "v_lshlrev_b32 v100, 16, v101\n"
            "v_lshlrev_b32 v102, 16, v103\n"
            "v_pk_fma_f32 v[98:99], v[98:99], v[108:109], v[86:87] op_sel:[0,0,0] op_sel_hi:[1,0,1]\n"
            "v_pk_fma_f32 v[100:101], v[100:101], v[108:109], v[86:87] op_sel:[0,1,0] op_sel_hi:[1,1,1]\n"
            "v_pk_fma_f32 v[102:103], v[102:103], v[110:111], v[86:87] op_sel:[0,0,0] op_sel_hi:[1,0,1]\n"
            "v_lshl_add_u32 v98, v99, 16, v98\n"
            "v_lshl_add_u32 v100, v101, 16, v100\n"
            "v_lshl_add_u32 v102, v103, 16, v102\n"
            "v_sub_u32 v98, v98, v86\n"
            "v_mad_i32_i16 v88, v99, v112, v88\n"
            "ds_add_u32 v112, v98 offset:65533\n"
            "v_sub_u32 v100, v100, v86\n"
            "v_mad_i32_i16 v88, v101, v113, v88\n"
            "ds_add_u32 v113, v100 offset:65533\n"
            "v_sub_u32 v102, v102, v86\n"
            "v_mad_i32_i16 v88, v103, v114, v88\n"
            "ds_add_u32 v114, v102 offset:65533\n"
            "v_and_b32 v104, 0x1fffc, v24\n"
            "v_and_b32 v105, 0x1fffc, v25\n"
            "v_and_b32 v106, 0x1fffc, v26\n"
            "s_swappc_b64 s[80:81], s[72:73]\n"
            "ds_read_b32 v93, v104\n"
            "ds_read_b32 v95, v105\n"
            "ds_read_b32 v97, v106\n"
            "s_waitcnt lgkmcnt(0)\n"
            "s_branch Ls4_%=\n"
            "Le4_%=:\n"
            "s_cmp_lt_u32 s75, 8\n"
            "s_cbranch_scc1 1f\n"
            "s_swappc_b64 s[80:81], s[66:67]\n"
            "s_branch Lc4_%=\n"
            "1:\n"
            "s_cmp_lg_u32 s75, 7\n"
            "s_cbranch_scc1 1f\n"
            "s_setprio 2\n"
            "s_mov_b32 s79, 3\n"
            "s_branch Lc4_%=\n"
            "1:\n"
            "s_cmp_lg_u32 s75, 3\n"
            "s_cbranch_scc1 1f\n"
            "s_setprio 1\n"
            "s_mov_b32 s79, 1\n"
            "s_branch Lc4_%=\n"
            "1:\n"
            "s_setprio 0\n"
            "v_add_u32_dpp v111, v81, v82 row_newbcast:4 row_mask:0xf bank_mask:0x3\n"
            "v_add_u32_dpp v111, v81, v82 row_newbcast:12 row_mask:0xf bank_mask:0xc\n"
            "v_and_b32 v108, 0x7fe, v27\n"
            "v_and_b32 v109, 0x1ff800, v27\n"
            "v_lshrrev_b32 v110, 22, v27\n"
            "v_lshrrev_b32 v112, 15, v24\n"
            "v_lshrrev_b32 v113, 15, v25\n"
            "v_lshrrev_b32 v114, 15, v26\n"
            "v_and_b32 v111, " IDXMASK ", v111\n"
            "v_min_u32 v111, v111, v83\n"
            "buffer_load_dwordx4 v[24:27], v111, %[rs], 0 idxen " RAUX "\n"
            "v_fmamk_f32 v108, v108, 0x7e800000, v84\n"
            "v_fmamk_f32 v109, v109, 0x79800000, v84\n"
            "v_fmamk_f32 v110, v110, 0x7f000000, v84\n"
            "v_or_b32 v112, 3, v112\n"
            "v_or_b32 v113, 3, v113\n"
            "v_or_b32 v114, 3, v114\n"
            "s_waitcnt lgkmcnt(3)\n"
            "s_swappc_b64 s[80:81], s[70:71]\n"
            "v_lshlrev_b32 v92, 16, v93\n"
            "v_lshlrev_b32 v94, 16, v95\n"
            "v_lshlrev_b32 v96, 16, v97\n"
            "v_pk_fma_f32 v[92:93], v[92:93], v[108:109], v[86:87] op_sel:[0,0,0] op_sel_hi:[1,0,1]\n"
            "v_pk_fma_f32 v[94:95], v[94:95], v[108:109], v[86:87] op_sel:[0,1,0] op_sel_hi:[1,1,1]\n"
            "v_pk_fma_f32 v[96:97], v[96:97], v[110:111], v[86:87] op_sel:[0,0,0] op_sel_hi:[1,0,1]\n"
            "v_lshl_add_u32 v92, v93, 16, v92\n"
            "v_lshl_add_u32 v94, v95, 16, v94\n"
            "v_lshl_add_u32 v96, v97, 16, v96\n"
            "v_sub_u32 v92, v92, v86\n"
            "v_mad_i32_i16 v88, v93, v112, v88\n"
            "ds_add_u32 v112, v92 offset:65533\n"
            "v_sub_u32 v94, v94, v86\n"
            "v_mad_i32_i16 v88, v95, v113, v88\n"
            "ds_add_u32 v113, v94 offset:65533\n"
            "v_sub_u32 v96, v96, v86\n"
            "v_mad_i32_i16 v88, v97, v114, v88\n"
            "ds_add_u32 v114, v96 offset:65533\n"
            "v_and_b32 v104, 0x1fffc, v28\n"
            "v_and_b32 v105, 0x1fffc, v29\n"
            "v_and_b32 v106, 0x1fffc, v30\n"
            "s_swappc_b64 s[80:81], s[72:73]\n"
            "ds_read_b32 v99, v104\n"
            "ds_read_b32 v101, v105\n"
            "ds_read_b32 v103, v106\n"
            "s_waitcnt lgkmcnt(0)\n"
            "s_branch Ls5_%=\n"
            "Le5_%=:\n"
            "s_cmp_lt_u32 s75, 8\n"
            "s_cbranch_scc1 1f\n"
            "s_swappc_b64 s[80:81], s[66:67]\n"
            "s_branch Lc5_%=\n"
            "1:\n"
            "s_cmp_lg_u32 s75, 7\n"
            "s_cbranch_scc1 1f\n"
            "s_setprio 2\n"
            "s_mov_b32 s79, 3\n"
            "s_branch Lc5_%=\n"
            "1:\n"
            "s_cmp_lg_u32 s75, 3\n"
            "s_cbranch_scc1 1f\n"
            "s_setprio 1\n"
            "s_mov_b32 s79, 1\n"
            "s_branch Lc5_%=\n"
            "1:\n"
            "s_setprio 0\n"
            "v_add_u32_dpp v111, v81, v82 row_newbcast:5 row_mask:0xf bank_mask:0x3\n"
            "v_add_u32_dpp v111, v81, v82 row_newbcast:13 row_mask:0xf bank_mask:0xc\n"
            "v_and_b32 v108, 0x7fe, v31\n"
            "v_and_b32 v109, 0x1ff800, v31\n"
            "v_lshrrev_b32 v110, 22, v31\n"
            "v_lshrrev_b32 v112, 15, v28\n"
            "v_lshrrev_b32 v113, 15, v29\n"
            "v_lshrrev_b32 v114, 15, v30\n"
            "v_and_b32 v111, " IDXMASK ", v111\n"
            "v_min_u32 v111, v111, v83\n"
            "buffer_load_dwordx4 v[28:31], v111, %[rs], 0 idxen " RAUX "\n"
            "v_fmamk_f32 v108, v108, 0x7e800000, v84\n"
            "v_fmamk_f32 v109, v109, 0x79800000, v84\n"
            "v_fmamk_f32 v110, v110, 0x7f000000, v84\n"
            "v_or_b32 v112, 3, v112\n"
            "v_or_b32 v113, 3, v113\n"
            "v_or_b32 v114, 3, v114\n"
            "s_waitcnt lgkmcnt(3)\n"
            "s_swappc_b64 s[80:81], s[70:71]\n"
            "v_lshlrev_b32 v98, 16, v99\n"
            "v_lshlrev_b32 v100, 16, v101\n"
            "v_lshlrev_b32 v102, 16, v103\n"
            "v_pk_fma_f32 v[98:99], v[98:99], v[108:109], v[86:87] op_sel:[0,0,0] op_sel_hi:[1,0,1]\n"
            "v_pk_fma_f32 v[100:101], v[100:101], v[108:109], v[86:87] op_sel:[0,1,0] op_sel_hi:[1,1,1]\n"
            "v_pk_fma_f32 v[102:103], v[102:103], v[110:111], v[86:87] op_sel:[0,0,0] op_sel_hi:[1,0,1]\n"
            "v_lshl_add_u32 v98, v99, 16, v98\n"
            "v_lshl_add_u32 v100, v101, 16, v100\n"
            "v_lshl_add_u32 v102, v103, 16, v102\n"
            "v_sub_u32 v98, v98, v86\n"
            "v_mad_i32_i16 v88, v99, v112, v88\n"
            "ds_add_u32 v112, v98 offset:65533\n"
            "v_sub_u32 v100, v100, v86\n"
            "v_mad_i32_i16 v88, v101, v113, v88\n"
            "ds_add_u32 v113, v100 offset:65533\n"
            "v_sub_u32 v102, v102, v86\n"
            "v_mad_i32_i16 v88, v103, v114, v88\n"
            "ds_add_u32 v114, v102 offset:65533\n"
            "v_and_b32 v104, 0x1fffc, v32\n"
            "v_and_b32 v105, 0x1fffc, v33\n"
            "v_and_b32 v106, 0x1fffc, v34\n"
            "s_swappc_b64 s[80:81], s[72:73]\n"
            "ds_read_b32 v93, v104\n"
            "ds_read_b32 v95, v105\n"
            "ds_read_b32 v97, v106\n"
            "s_waitcnt lgkmcnt(0)\n"
            "s_branch Ls6_%=\n"
            "Le6_%=:\n"
            "s_cmp_lt_u32 s75, 8\n"
            "s_cbranch_scc1 1f\n"
            "s_swappc_b64 s[80:81], s[66:67]\n"
            "s_branch Lc6_%=\n"
            "1:\n"
            "s_cmp_lg_u32 s75, 7\n"
            "s_cbranch_scc1 1f\n"
            "s_setprio 2\n"
            "s_mov_b32 s79, 3\n"
            "s_branch Lc6_%=\n"
            "1:\n"
            "s_cmp_lg_u32 s75, 3\n"
            "s_cbranch_scc1 1f\n"
            "s_setprio 1\n"
            "s_mov_b32 s79, 1\n"
            "s_branch Lc6_%=\n"
            "1:\n"
            "s_setprio 0\n"
            "v_add_u32_dpp v111, v81, v82 row_newbcast:6 row_mask:0xf bank_mask:0x3\n"
            "v_add_u32_dpp v111, v81, v82 row_newbcast:14 row_mask:0xf bank_mask:0xc\n"
            "v_and_b32 v108, 0x7fe, v35\n"
            "v_and_b32 v109, 0x1ff800, v35\n"
            "v_lshrrev_b32 v110, 22, v35\n"
            "v_lshrrev_b32 v112, 15, v32\n"
            "v_lshrrev_b32 v113, 15, v33\n"
            "v_lshrrev_b32 v114, 15, v34\n"
            "v_and_b32 v111, " IDXMASK ", v111\n"
            "v_min_u32 v111, v111, v83\n"
            "buffer_load_dwordx4 v[32:35], v111, %[rs], 0 idxen " RAUX "\n"
            "v_fmamk_f32 v108, v108, 0x7e800000, v84\n"
            "v_fmamk_f32 v109, v109, 0x79800000, v84\n"
            "v_fmamk_f32 v110, v110, 0x7f000000, v84\n"
            "v_or_b32 v112, 3, v112\n"
            "v_or_b32 v113, 3, v113\n"
            "v_or_b32 v114, 3, v114\n"
            "s_waitcnt lgkmcnt(3)\n"
            "s_swappc_b64 s[80:81], s[70:71]\n"
            "v_lshlrev_b32 v92, 16, v93\n"
            "v_lshlrev_b32 v94, 16, v95\n"
            "v_lshlrev_b32 v96, 16, v97\n"
            "v_pk_fma_f32 v[92:93], v[92:93], v[108:109], v[86:87] op_sel:[0,0,0] op_sel_hi:[1,0,1]\n"
            "v_pk_fma_f32 v[94:95], v[94:95], v[108:109], v[86:87] op_sel:[0,1,0] op_sel_hi:[1,1,1]\n"
            "v_pk_fma_f32 v[96:97], v[96:97], v[110:111], v[86:87] op_sel:[0,0,0] op_sel_hi:[1,0,1]\n"
            "v_lshl_add_u32 v92, v93, 16, v92\n"
            "v_lshl_add_u32 v94, v95, 16, v94\n"
            "v_lshl_add_u32 v96, v97, 16, v96\n"
            "v_sub_u32 v92, v92, v86\n"
            "v_mad_i32_i16 v88, v93, v112, v88\n"
            "ds_add_u32 v112, v92 offset:65533\n"
            "v_sub_u32 v94, v94, v86\n"
            "v_mad_i32_i16 v88, v95, v113, v88\n"
            "ds_add_u32 v113, v94 offset:65533\n"
            "v_sub_u32 v96, v96, v86\n"
            "v_mad_i32_i16 v88, v97, v114, v88\n"
            "ds_add_u32 v114, v96 offset:65533\n"
            "v_and_b32 v104, 0x1fffc, v36\n"
            "v_and_b32 v105, 0x1fffc, v37\n"
            "v_and_b32 v106, 0x1fffc, v38\n"
            "s_swappc_b64 s[80:81], s[72:73]\n"
            "ds_read_b32 v99, v104\n"
            "ds_read_b32 v101, v105\n"
            "ds_read_b32 v103, v106\n"
            "s_waitcnt lgkmcnt(0)\n"
            "s_branch Ls7_%=\n"
            "Le7_%=:\n"
            "s_cmp_lt_u32 s75, 8\n"
            "s_cbranch_scc1 1f\n"
            "s_swappc_b64 s[80:81], s[66:67]\n"
            "s_branch Lc7_%=\n"
            "1:\n"
            "s_cmp_lg_u32 s75, 7\n"
            "s_cbranch_scc1 1f\n"
            "s_setprio 2\n"
            "s_mov_b32 s79, 3\n"
            "s_branch Lc7_%=\n"
            "1:\n"
            "s_cmp_lg_u32 s75, 3\n"
            "s_cbranch_scc1 1f\n"
            "s_setprio 1\n"
            "s_mov_b32 s79, 1\n"
            "s_branch Lc7_%=\n"
            "1:\n"
            "s_setprio 0\n"
            "v_add_u32_dpp v111, v81, v82 row_newbcast:7 row_mask:0xf bank_mask:0x3\n"
            "v_add_u32_dpp v111, v81, v82 row_newbcast:15 row_mask:0xf bank_mask:0xc\n"
            "v_and_b32 v108, 0x7fe, v39\n"
            "v_and_b32 v109, 0x1ff800, v39\n"
            "v_lshrrev_b32 v110, 22, v39\n"
            "v_lshrrev_b32 v112, 15, v36\n"
            "v_lshrrev_b32 v113, 15, v37\n"
            "v_lshrrev_b32 v114, 15, v38\n"
            "v_and_b32 v111, " IDXMASK ", v111\n"
            "v_min_u32 v111, v111, v83\n"
            "buffer_load_dwordx4 v[36:39], v111, %[rs], 0 idxen " RAUX "\n"
            "s_min_u32 s82, s82, 19\n"
            "s_add_u32 s82, s82, 1\n"
            "s_set_gpr_idx_on s82, gpr_idx(SRC0)\n"
            "s_nop 0\n"
            "v_mov_b32 v81, v60\n"
            "s_set_gpr_idx_off\n"
            "v_fmamk_f32 v108, v108, 0x7e800000, v84\n"
            "v_fmamk_f32 v109, v109, 0x79800000, v84\n"
            "v_fmamk_f32 v110, v110, 0x7f000000, v84\n"
            "v_or_b32 v112, 3, v112\n"
            "v_or_b32 v113, 3, v113\n"
            "v_or_b32 v114, 3, v114\n"
            "s_waitcnt lgkmcnt(3)\n"
            "s_swappc_b64 s[80:81], s[70:71]\n"
            "v_lshlrev_b32 v98, 16, v99\n"
            "v_lshlrev_b32 v100, 16, v101\n"
            "v_lshlrev_b32 v102, 16, v103\n"
            "v_pk_fma_f32 v[98:99], v[98:99], v[108:109], v[86:87] op_sel:[0,0,0] op_sel_hi:[1,0,1]\n"
            "v_pk_fma_f32 v[100:101], v[100:101], v[108:109], v[86:87] op_sel:[0,1,0] op_sel_hi:[1,1,1]\n"
            "v_pk_fma_f32 v[102:103], v[102:103], v[110:111], v[86:87] op_sel:[0,0,0] op_sel_hi:[1,0,1]\n"
            "v_lshl_add_u32 v98, v99, 16, v98\n"
            "v_lshl_add_u32 v100, v101, 16, v100\n"
            "v_lshl_add_u32 v102, v103, 16, v102\n"
            "v_sub_u32 v98, v98, v86\n"
            "v_mad_i32_i16 v88, v99, v112, v88\n"
            "ds_add_u32 v112, v98 offset:65533\n"
            "v_sub_u32 v100, v100, v86\n"
            "v_mad_i32_i16 v88, v101, v113, v88\n"
            "ds_add_u32 v113, v100 offset:65533\n"
            "v_sub_u32 v102, v102, v86\n"
            "v_mad_i32_i16 v88, v103, v114, v88\n"
            "ds_add_u32 v114, v102 offset:65533\n"
            "v_and_b32 v104, 0x1fffc, v8\n"
            "v_and_b32 v105, 0x1fffc, v9\n"
            "v_and_b32 v106, 0x1fffc, v10\n"
            "s_swappc_b64 s[80:81], s[72:73]\n"
            "ds_read_b32 v93, v104\n"
            "ds_read_b32 v95, v105\n"
            "ds_read_b32 v97, v106\n"
            "s_waitcnt lgkmcnt(0)\n"
            "s_branch Ls0_%=\n"
            "Lchg1%=:\n"
            "s_add_u32 s92, s92, 1\n"
            "s_cmp_eq_u32 s92, 8\n"
            "s_cbranch_scc1 1f\n"
            "s_barrier\n"
            "s_mov_b32 m0, %[wb]\n"
            "s_nop 0\n"
            "ds_write_addtid_b32 v40 offset:0\n"
            "ds_write_addtid_b32 v41 offset:256\n"
            "ds_write_addtid_b32 v42 offset:512\n"
            "ds_write_addtid_b32 v43 offset:768\n"
            "ds_write_addtid_b32 v44 offset:16384\n"
            "ds_write_addtid_b32 v45 offset:16640\n"
            "ds_write_addtid_b32 v46 offset:16896\n"
            "ds_write_addtid_b32 v47 offset:17152\n"
            "ds_write_addtid_b32 v48 offset:32768\n"
            "ds_write_addtid_b32 v49 offset:33024\n"
            "ds_write_addtid_b32 v50 offset:33280\n"
            "ds_write_addtid_b32 v51 offset:33536\n"
            "ds_write_addtid_b32 v52 offset:49152\n"
            "ds_write_addtid_b32 v53 offset:49408\n"
            "ds_write_addtid_b32 v54 offset:49664\n"
            "ds_write_addtid_b32 v55 offset:49920\n"
            "s_add_u32 m0, %[wb], 16384\n"
            "s_nop 0\n"
            "ds_write_addtid_b32 v56 offset:49152\n"
            "ds_write_addtid_b32 v57 offset:49408\n"
            "ds_write_addtid_b32 v58 offset:49664\n"
            "ds_write_addtid_b32 v59 offset:49920\n"
            "1:\n"
            "s_setpc_b64 s[80:81]\n"
            "Lchg2%=:\n"
            "s_cmp_eq_u32 s92, 8\n"
            "s_cbranch_scc1 Lfin%=\n"
            "s_lshr_b64 s[76:77], s[76:77], 8\n"
            "s_and_b32 s75, s76, 255\n"
            "s_movk_i32 s74, 1000\n"
            "s_sub_i32 s79, s75, %[j0]\n"
            "s_max_i32 s79, s79, 8\n"
            "s_cmp_eq_u32 s92, 7\n s_cselect_b32 s79, 8, s79\n"
            "s_setprio 3\n"
            "s_waitcnt lgkmcnt(3)\n"
            "s_barrier\n"
            "s_setpc_b64 s[80:81]\n"
            "Lchg3%=:\n"
            "s_sub_u32 s74, s75, 7\n"
            "s_mov_b32 s79, 7\n"
            "s_cmp_eq_u32 s92, 7\n"
            "s_cbranch_scc1 1f\n"
            "s_add_u32 s94, s94, %[i4]\n"
            "buffer_load_dwordx4 v[40:43], %[tid16], %[rm], s94 offen\n"
            "s_add_u32 s96, s94, 16384\n"
            "buffer_load_dwordx4 v[44:47], %[tid16], %[rm], s96 offen\n"
            "s_add_u32 s96, s94, 32768\n"
            "buffer_load_dwordx4 v[48:51], %[tid16], %[rm], s96 offen\n"
            "s_add_u32 s96, s94, 49152\n"
            "buffer_load_dwordx4 v[52:55], %[tid16], %[rm], s96 offen\n"
            "s_add_u32 s96, s94, 65536\n"
            "buffer_load_dwordx4 v[56:59], %[tid16], %[rm], s96 offen\n"
            "s_branch 2f\n"
            "1:\n"
            "buffer_load_ushort v48, %[ed], %[rd], 0 offen " NTH "\n"
            "buffer_load_ushort v49, %[ed], %[rd], 0 offen offset:2 " NTH "\n"
            "buffer_load_ushort v50, %[ed], %[rd], %[e1] offen " NTH "\n"
            "buffer_load_ushort v51, %[ed], %[rd], %[e1] offen offset:2 " NTH "\n"
            "v_lshlrev_b32 v52, 2, %[ed]\n"
            "s_lshl_b32 s96, %[e1], 2\n"
            "buffer_load_dwordx4 v[40:43], v52, %[rmm], 0 offen " NTH "\n"
            "buffer_load_dwordx4 v[44:47], v52, %[rmm], s96 offen " NTH "\n"
            "2:\n"
            "s_setpc_b64 s[80:81]\n"
            "Lfin%=:\n"
            "s_waitcnt vmcnt(0) lgkmcnt(0)\n"
            "s_barrier\n"
            "ds_read_b128 v[8:11], v89 offset:0\n"
            "ds_read_b128 v[12:15], v89 offset:16384\n"
            "ds_read_b128 v[16:19], v89 offset:32768\n"
            "ds_read_b128 v[20:23], v89 offset:49152\n"
            "ds_read_b128 v[24:27], v90\n"
            "v_subrev_u32 v117, 65533, v89\n"
            "v_mov_b32 v118, 0\n"
            "v_bfe_u32 v116, %[tid16], 9, 3\n"
            "v_cmp_eq_u32 vcc, %[q], v116\n"
            "v_bfrev_b32 v115, 1\n"
            "s_nop 0\n"
            "v_cndmask_b32 v115, %[tid16], v115, vcc\n"
            "s_mov_b32 s96, 16384\n s_mov_b32 s97, 32768\n s_mov_b32 s92, 49152\n s_mov_b32 s93, 65536\n"
            "s_waitcnt lgkmcnt(4)\n"
            "buffer_store_dwordx4 v[8:11], v115, %[rp0], 0 offen sc1\n"
            "s_waitcnt lgkmcnt(3)\n"
            "buffer_store_dwordx4 v[12:15], v115, %[rp0], s96 offen sc1\n"
            "s_waitcnt lgkmcnt(2)\n"
            "buffer_store_dwordx4 v[16:19], v115, %[rp0], s97 offen sc1\n"
            "s_waitcnt lgkmcnt(1)\n"
            "buffer_store_dwordx4 v[20:23], v115, %[rp0], s92 offen sc1\n"
            "s_waitcnt lgkmcnt(0)\n"
            "buffer_store_dwordx4 v[24:27], v115, %[rp0], s93 offen sc1\n"
            "v_add_u32 v112, 0x8000, v8\n"
            "v_add_u32 v116, 0, v117\n"
            "v_mad_i32_i16 v118, v112, v116, v118 op_sel:[1,0,0,0]\n"
            "v_add_u32 v112, 0x8000, v9\n"
            "v_add_u32 v116, 4, v117\n"
            "v_mad_i32_i16 v118, v112, v116, v118 op_sel:[1,0,0,0]\n"
            "v_add_u32 v112, 0x8000, v10\n"
            "v_add_u32 v116, 8, v117\n"
            "v_mad_i32_i16 v118, v112, v116, v118 op_sel:[1,0,0,0]\n"
            "v_add_u32 v112, 0x8000, v11\n"
            "v_add_u32 v116, 12, v117\n"
            "v_mad_i32_i16 v118, v112, v116, v118 op_sel:[1,0,0,0]\n"
            "v_add_u32 v117, 16384, v117\n"
            "v_add_u32 v112, 0x8000, v12\n"
            "v_add_u32 v116, 0, v117\n"
            "v_mad_i32_i16 v118, v112, v116, v118 op_sel:[1,0,0,0]\n"
            "v_add_u32 v112, 0x8000, v13\n"
            "v_add_u32 v116, 4, v117\n"
            "v_mad_i32_i16 v118, v112, v116, v118 op_sel:[1,0,0,0]\n"
            "v_add_u32 v112, 0x8000, v14\n"
            "v_add_u32 v116, 8, v117\n"
            "v_mad_i32_i16 v118, v112, v116, v118 op_sel:[1,0,0,0]\n"
            "v_add_u32 v112, 0x8000, v15\n"
            "v_add_u32 v116, 12, v117\n"
            "v_mad_i32_i16 v118, v112, v116, v118 op_sel:[1,0,0,0]\n"
            "v_add_u32 v117, 16384, v117\n"
            "v_add_u32 v112, 0x8000, v16\n"
            "v_add_u32 v116, 0, v117\n"
            "v_mad_i32_i16 v118, v112, v116, v118 op_sel:[1,0,0,0]\n"
            "v_add_u32 v112, 0x8000, v17\n"
            "v_add_u32 v116, 4, v117\n"
            "v_mad_i32_i16 v118, v112, v116, v118 op_sel:[1,0,0,0]\n"
            "v_add_u32 v112, 0x8000, v18\n"
            "v_add_u32 v116, 8, v117\n"
            "v_mad_i32_i16 v118, v112, v116, v118 op_sel:[1,0,0,0]\n"
            "v_add_u32 v112, 0x8000, v19\n"
            "v_add_u32 v116, 12, v117\n"
            "v_mad_i32_i16 v118, v112, v116, v118 op_sel:[1,0,0,0]\n"
            "v_add_u32 v117, 16384, v117\n"
            "v_add_u32 v112, 0x8000, v20\n"
            "v_add_u32 v116, 0, v117\n"
            "v_mad_i32_i16 v118, v112, v116, v118 op_sel:[1,0,0,0]\n"
            "v_add_u32 v112, 0x8000, v21\n"
            "v_add_u32 v116, 4, v117\n"
            "v_mad_i32_i16 v118, v112, v116, v118 op_sel:[1,0,0,0]\n"
            "v_add_u32 v112, 0x8000, v22\n"
            "v_add_u32 v116, 8, v117\n"
            "v_mad_i32_i16 v118, v112, v116, v118 op_sel:[1,0,0,0]\n"
            "v_add_u32 v112, 0x8000, v23\n"
            "v_add_u32 v116, 12, v117\n"
            "v_mad_i32_i16 v118, v112, v116, v118 op_sel:[1,0,0,0]\n"
            "v_add_u32 v117, 16384, v117\n"
            "v_add_u32 v112, 0x8000, v24\n"
            "v_add_u32 v116, 0, v117\n"
            "v_mad_i32_i16 v118, v112, v116, v118 op_sel:[1,0,0,0]\n"
            "v_add_u32 v112, 0x8000, v25\n"
            "v_add_u32 v116, 4, v117\n"
            "v_mad_i32_i16 v118, v112, v116, v118 op_sel:[1,0,0,0]\n"
            "v_add_u32 v112, 0x8000, v26\n"
            "v_add_u32 v116, 8, v117\n"
            "v_mad_i32_i16 v118, v112, v116, v118 op_sel:[1,0,0,0]\n"
            "v_add_u32 v112, 0x8000, v27\n"
            "v_add_u32 v116, 12, v117\n"
            "v_mad_i32_i16 v118, v112, v116, v118 op_sel:[1,0,0,0]\n"
            "v_add_u32 v117, 16384, v117\n"
            "v_sub_u32 v118, v88, v118\n"
            "v_mov_b32 v116, %[wb]\n"
            "v_mov_b32 v117, %[bad]\n"
            "v_add_u32_dpp v118, v118, v118 row_shr:1 row_mask:0xf bank_mask:0xf bound_ctrl:1\n"
            "s_nop 1\n"
            "v_add_u32_dpp v118, v118, v118 row_shr:2 row_mask:0xf bank_mask:0xf bound_ctrl:1\n"
            "s_nop 1\n"
            "v_add_u32_dpp v118, v118, v118 row_shr:4 row_mask:0xf bank_mask:0xf bound_ctrl:1\n"
            "s_nop 1\n"
            "v_add_u32_dpp v118, v118, v118 row_shr:8 row_mask:0xf bank_mask:0xf bound_ctrl:1\n"
            "s_nop 1\n"
            "v_add_u32_dpp v118, v118, v118 row_bcast:15 row_mask:0xa bank_mask:0xf\n"
            "s_nop 1\n"
            "v_add_u32_dpp v118, v118, v118 row_bcast:31 row_mask:0xc bank_mask:0xf\n"
            "s_nop 1\n"
            "v_lshrrev_b32 v116, 7, v116\n"
            "s_mov_b32 exec_lo, 0\n s_mov_b32 exec_hi, 0x80000000\n"
            "ds_write2_b32 v116, v118, v117 offset1:1\n"
            "s_mov_b64 exec, -1\n"
            "s_branch Lend2%=\n"
            "Lend%=:\n"
            "s_waitcnt vmcnt(0)\n"
            "Lend2%=:\n"
            "s_setprio 0\n"
            "s_waitcnt lgkmcnt(0)\n"

            : [mode] "=&s"(mode), [bad] "=&s"(bad), [exs] "=&v"(exs), [tid16] "+v"(t16), "+v"(tc2), "=&{v[40:43]}"(mm[0]), "=&{v[44:47]}"(mm[1]), "=&{v[48:51]}"(dd)
            : [rs] "s"(rs), [rm] "s"(rm), [rp0] "s"(rp0), [rt] "s"(rt), [i4] "s"(LMAX * 4), [c4] "s"(a_.c4), [pb] "s"(81920 + wave * 5120), [wb] "s"(wave * 1024), [q] "s"(q), [j0] "s"(J0),
              [rmm] "s"(rmm), [rd] "s"(rd), [e1] "s"(128 * 32 * 128 * 2), [ed] "v"((((wave * 8 + q) * 32 + b) * 128 + lane * 2) * 2),
              [fof] "s"((NT * NWS + cp * (P1_GRID * 2)) * 4), [dumu] "s"(ly.dumu), [k0] "s"(ly.K0),
              [tof] "v"((w * NT + t0) * 4), [fb] "v"(frag_unit(w, ly)), [xb] "v"((blk_line(w) + tile_line(t0, w)) * 8)
            : "memory", "vcc", "scc", "m0", "s66", "s67", "s70", "s71", "s72", "s73", "s74", "s75", "s76", "s77", "s79", "s80", "s81", "s82",
              "s84", "s85", "s86", "s87", "s88", "s89", "s90", "s91", "s92", "s93", "s94", "s96", "s97",
              "v8", "v9", "v10", "v11", "v12", "v13", "v14", "v15", "v16", "v17", "v18", "v19", "v20", "v21", "v22", "v23", "v24", "v25", "v26", "v27", "v28", "v29", "v30", "v31", "v32", "v33", "v34", "v35", "v36", "v37", "v38", "v39", "v52", "v53", "v54", "v55", "v56", "v57", "v58", "v59", "v60", "v61", "v62", "v63", "v64", "v65", "v66", "v67", "v68", "v69", "v70", "v71", "v72", "v73", "v74", "v75", "v76", "v77", "v78", "v79", "v80", "v81", "v82", "v83", "v84", "v85", "v86", "v87", "v88", "v89", "v90", "v91", "v92", "v93", "v94", "v95", "v96", "v97", "v98", "v99", "v100", "v101", "v102", "v103", "v104", "v105", "v106", "v107", "v108", "v109", "v110", "v111", "v112", "v113", "v114", "v115", "v116", "v117", "v118");
    }
    const int tid2 = (int)(t16 >> 4);
    kaptr ka = (kaptr)__builtin_amdgcn_kernarg_segment_ptr();
    asm volatile("" : "+s"(ka));
    unsigned long long* wm = (unsigned long long*)(ka->wgmode + b * 32);
    const int E = ka->E;
    const unsigned nblk = ka->O >> 7;
    const int ln = tid2 & 63, wv = __builtin_amdgcn_readfirstlane(tid2 >> 6);
    const bool two = (16 + wv) * 8 + q < nblk;
    float g[2][2][4], h[2][2][4];
    auto gh = [&]() {
#pragma unroll
        for (int p = 0; p < 2; p++) {
            if (p == 0 || two) {
                edge_gh(lo_bf(p ? dd.z : dd.x), ka->centers, ka->w_sbf, ka->w_rbf, g[p][0], h[p][0]);
                edge_gh(lo_bf(p ? dd.w : dd.y), ka->centers, ka->w_sbf, ka->w_rbf, g[p][1], h[p][1]);
            }
        }
    };
    v2u own[2] = {{0, 0}, {0, 0}};
    if (mode == 0) {
        gh();
        own[0] = *(const v2u*)(lds + OBASE + (wv * 8 + q) * 128 + ln * 2);
        if (two) own[1] = *(const v2u*)(lds + OBASE + ((16 + wv) * 8 + q) * 128 + ln * 2);
        asm volatile("s_waitcnt vmcnt(0)" : "+v"(g[0][0][0]), "+v"(h[0][1][3]) :: "memory");
        __syncthreads();
        const v2u cs = *(const v2u*)(lds + (ln & 15) * 2);
        if (__builtin_amdgcn_readlane(row_scan_add(cs.x), 15) != 0 || __ballot(cs.y != 0)) mode = 1;
    }
    if (mode != 0) {
        acc_fallback(lds, mode, *ka, tid2);
        asm volatile("s_waitcnt vmcnt(0)" ::: "memory");
        __syncthreads();
        if (tid2 == P2_THREADS - 1) { __builtin_amdgcn_fence(__ATOMIC_RELEASE, "agent"); asm volatile("s_waitcnt vmcnt(0)" ::: "memory"); }
        const __amdgpu_buffer_rsrc_t rmm = __builtin_amdgcn_make_buffer_rsrc((void*)ka->m, (short)0, E * 8, 0x00020000);
        const __amdgpu_buffer_rsrc_t rdd = __builtin_amdgcn_make_buffer_rsrc((void*)ka->dist, (short)0, E * 2, 0x00020000);
        const unsigned e = ((wv * 8 + q) * 32 + b) * 128 + ln * 2, e1 = e + 128 * 32 * 128;
        mm[0] = __builtin_amdgcn_raw_buffer_load_b128(rmm, e * 8, 0, 0); mm[1] = __builtin_amdgcn_raw_buffer_load_b128(rmm, e1 * 8, 0, 0);
        dd.x = __builtin_amdgcn_raw_buffer_load_b16(rdd, e * 2, 0, 0); dd.y = __builtin_amdgcn_raw_buffer_load_b16(rdd, e * 2 + 2, 0, 0);
        dd.z = __builtin_amdgcn_raw_buffer_load_b16(rdd, e1 * 2, 0, 0); dd.w = __builtin_amdgcn_raw_buffer_load_b16(rdd, e1 * 2 + 2, 0, 0);
        gh();
        own[0].x = own[0].y = own[1].x = own[1].y = 0;
    }
    const unsigned* prb = ka->praw + (size_t)b * ka->O; unsigned short* outp = ka->out;
    int EP = ka->EP;
    unsigned wu[8];
#pragma unroll
    for (int i = 0; i < 8; i++) wu[i] = ((cptr)ka->w_up)[i];
    if (tid2 == P2_THREADS - 1) {
        const unsigned long long add = (1u | (mode ? 256u << q : 0u)) | (qt == 0 ? (unsigned long long)(__builtin_amdgcn_readfirstlane(exs) & 0xFFFF) << (32 + 16 * cp) : 0ull);
        unsigned long long v = __hip_atomic_fetch_add(wm, add, __ATOMIC_RELAXED, __HIP_MEMORY_SCOPE_AGENT) + add;
        for (int spin = 0; spin < (1 << 22) && (v & 255) != 8; spin++) {
            v = __hip_atomic_load(wm, __ATOMIC_RELAXED, __HIP_MEMORY_SCOPE_AGENT);
            if ((v & 255) != 8) __builtin_amdgcn_s_sleep(2);
        }
        if (v & 0xFF00) { __builtin_amdgcn_fence(__ATOMIC_ACQUIRE, "agent"); asm volatile("s_waitcnt vmcnt(0)" ::: "memory"); }
        lds[64] = ((unsigned)v >> 8) & 255; lds[65] = (unsigned)(v >> 32);
    }
    asm volatile("" : "+s"(prb), "+s"(outp), "+s"(EP), "+s"(wu[0]), "+s"(wu[1]), "+s"(wu[2]), "+s"(wu[3]), "+s"(wu[4]), "+s"(wu[5]), "+s"(wu[6]), "+s"(wu[7]));
    const __amdgpu_buffer_rsrc_t rpr = __builtin_amdgcn_make_buffer_rsrc((void*)prb, (short)0, (7 * EP + (int)(nblk << 7)) * 4, 0x00020000);
    const __amdgpu_buffer_rsrc_t rou = __builtin_amdgcn_make_buffer_rsrc((void*)outp, (short)0, E * 8, 0x00020000);
    __syncthreads();
    v2u w[2][8];
#pragma unroll
    for (int p = 0; p < 2; p++) {
        if (p == 0 || two) {
#pragma unroll
            for (int j = 0; j < 8; j++)
                w[p][j] = __builtin_amdgcn_raw_buffer_load_b64(rpr, (((p * 16 + wv) * 8 + q) * 128 + ln * 2) * 4, j == q ? (int)OOB : j * EP * 4, PR_AUX);
        }
    }
    const unsigned fbm = __builtin_amdgcn_readfirstlane(lds[64]), exw = __builtin_amdgcn_readfirstlane(lds[65]);
    float inv[4];
#pragma unroll
    for (int k = 0; k < 4; k++) inv[k] = __uint_as_float(((exw >> (8 * k)) & 255) << 23) * (1.0f / 511.0f);
#pragma unroll
    for (int p = 0; p < 2; p++) {
        const unsigned blk = (p * 16 + wv) * 8 + q;
        if (p == 0 || two) {
            int s[2][4];
            {
                const int l0 = (int)(short)(own[p].x & 0xFFFF), h0 = (int)(own[p].x + 0x8000u) >> 16, l1 = (int)(short)(own[p].y & 0xFFFF), h1 = (int)(own[p].y + 0x8000u) >> 16;
                s[0][0] = cp ? 0 : l0; s[0][1] = cp ? 0 : h0; s[0][2] = cp ? l0 : 0; s[0][3] = cp ? h0 : 0;
                s[1][0] = cp ? 0 : l1; s[1][1] = cp ? 0 : h1; s[1][2] = cp ? l1 : 0; s[1][3] = cp ? h1 : 0;
            }
#pragma unroll
            for (int j = 0; j < 8; j++) {
                s[0][(j & 1) * 2] += (int)(short)(w[p][j].x & 0xFFFF); s[0][(j & 1) * 2 + 1] += (int)(w[p][j].x + 0x8000u) >> 16;
                s[1][(j & 1) * 2] += (int)(short)(w[p][j].y & 0xFFFF); s[1][(j & 1) * 2 + 1] += (int)(w[p][j].y + 0x8000u) >> 16;
            }
            float a[2][4];
#pragma unroll
            for (int i = 0; i < 2; i++)
#pragma unroll
                for (int k = 0; k < 4; k++) a[i][k] = s[i][k] ? (float)s[i][k] * inv[k] : 0.f;
            if (fbm && blk < nblk) {
                const float* part = ka->part + (size_t)b * ka->O + blk * 128 + ln * 2;
#pragma unroll 1
                for (int j = 0; j < 8; j++)
                    if ((fbm >> j) & 1) {
#pragma unroll
                        for (int i = 0; i < 2; i++) {
                            const float x = part[(size_t)(j * 2) * EP + i], y = part[(size_t)(j * 2 + 1) * EP + i];
                            if (j & 1) { a[i][2] += x; a[i][3] += y; } else { a[i][0] += x; a[i][1] += y; }
                        }
                    }
            }
            const v2u r0 = edge_out(a[0], g[p][0], h[p][0], mm[p].x, mm[p].y, wu), r1 = edge_out(a[1], g[p][1], h[p][1], mm[p].z, mm[p].w, wu);
            const v4u r = {r0.x, r0.y, r1.x, r1.y};
            __builtin_amdgcn_raw_buffer_store_b128(r, rou, ((blk * 32 + b) * 128 + ln * 2) * 8, 0, OUT_AUX);
        }
    }
}

extern "C" __global__ __launch_bounds__(256) void k_naive(const int* __restrict__ idx_in, const int* __restrict__ idx_out,
                                               const unsigned short* __restrict__ angle, const unsigned short* __restrict__ ang_freq,
                                               const unsigned short* __restrict__ m, const unsigned short* __restrict__ w_down,
                                               float* __restrict__ part, int T, int EP) {
    const float freq = bf2f(ang_freq[0]);
    for (long long t = (long long)blockIdx.x * 256 + threadIdx.x; t < T; t += (long long)gridDim.x * 256) {
        v2u mv = ((const v2u*)m)[idx_in[t]];
        float a[4] = {lo_bf(mv.x), hi_bf(mv.x), lo_bf(mv.y), hi_bf(mv.y)};
        float c = __cosf(freq * bf2f(angle[t]));
        int eo = idx_out[t];
#pragma unroll
        for (int d = 0; d < 4; d++) {
            float s = 0.f;
#pragma unroll
            for (int k = 0; k < 4; k++) s += a[k] * bf2f(w_down[k * 4 + d]);
            atomicAdd(&part[(size_t)d * EP + eo], s * c);
        }
    }
}

__global__ __launch_bounds__(256) void k_final(const unsigned short* __restrict__ m, const unsigned short* __restrict__ dist,
                                               const unsigned short* __restrict__ centers, const unsigned short* __restrict__ w_rbf,
                                               const unsigned short* __restrict__ w_sbf, const unsigned short* __restrict__ w_up,
                                               const float* __restrict__ part, unsigned short* __restrict__ out, int E, int EP) {
    const int e = blockIdx.x * 256 + threadIdx.x;
    if (e >= E) return;
    float a[4], g[4], h[4];
#pragma unroll
    for (int k = 0; k < 4; k++) a[k] = part[(size_t)k * EP + e];
    edge_gh(bf2f(dist[e]), centers, w_sbf, w_rbf, g, h);
    const v2u mv = ((const v2u*)m)[e];
    unsigned wu[8];
#pragma unroll
    for (int i = 0; i < 8; i++) wu[i] = ((cptr)w_up)[i];
    ((v2u*)out)[e] = edge_out(a, g, h, mv.x, mv.y, wu);
}

extern "C" void kernel_launch(void* const* d_in, const int* in_sizes, int n_in,
                              void* d_out, int out_size, void* d_ws, size_t ws_size,
                              hipStream_t stream) {
    const unsigned short* m = (const unsigned short*)d_in[0];
    const int* idx_in = (const int*)d_in[1];
    const int* idx_out = (const int*)d_in[2];
    const unsigned short* dist = (const unsigned short*)d_in[3];
    const unsigned short* angle = (const unsigned short*)d_in[4];
    const unsigned short* centers = (const unsigned short*)d_in[5];
    const unsigned short* ang_freq = (const unsigned short*)d_in[6];
    const unsigned short* w_rbf = (const unsigned short*)d_in[7];
    const unsigned short* w_sbf = (const unsigned short*)d_in[8];
    const unsigned short* w_down = (const unsigned short*)d_in[9];
    const unsigned short* w_up = (const unsigned short*)d_in[10];
    const int E = in_sizes[0] / 4;
    const int T = in_sizes[1];
    const int NR = T / (P1_GRID * CH);
    const int per = ((T - NR * (P1_GRID * CH) + P1_GRID - 1) / P1_GRID + 3) / 4 * 4;
    const unsigned O = (unsigned)(((E + 127) / 128 + 31) / 32) * 128;
    const unsigned I = (unsigned)(E + 31) / 32;
    const int EP = (int)O * 32;
    Lay ly;
    const unsigned meanr = (unsigned)(((size_t)NR * CH + per) / NT);
    ly.K0 = meanr >= 30 ? (meanr - 30) / 24 : 0;
    ly.KX = ly.K0 + ly.K0 * 2 / 3 + 4;
#define FCAP 4928
    ly.fcap = meanr >= 100 ? FCAP : 8192;
    const size_t lines = (size_t)PLANE * (ly.KX + 1);
    char* ws = (char*)d_ws;
    size_t o = 0;
    unsigned* praw = (unsigned*)(ws + o); o += (size_t)8 * EP * 4; o = (o + 255) / 256 * 256;
    unsigned* wgmode = (unsigned*)(ws + o); o += 16384;
    unsigned* mdp = (unsigned*)(ws + o); o += (size_t)2 * 32 * LMAX * 4;
    unsigned* tab = (unsigned*)(ws + o); o += (size_t)NT * NWS * 4;
    unsigned* flags = (unsigned*)(ws + o); o += FL_SIZE * 4; o = (o + 255) / 256 * 256;
    o += (size_t)NWS * ly.fcap * 16;
    char* rec = ws + o;
    ly.dumu = (unsigned)(lines * 8);
    o += 0x200000 + ((size_t)ly.dumu + 64) * 16 - (size_t)PLANE * 128;
    float* part = (float*)(ws + o); o += (size_t)16 * EP * 4;
    const bool ok = E >= 16384 && O <= LMAX && I <= LMAX && T > 0 && T < (1 << 29) && ly.KX <= 30 && o <= ws_size;
    if (!ok) {
        part = (float*)ws;
        const int EQ = (E + 3) / 4 * 4;
        (void)hipMemsetAsync(part, 0, (size_t)4 * EQ * 4, stream);
        if (T > 0) k_naive<<<2048, 256, 0, stream>>>(idx_in, idx_out, angle, ang_freq, m, w_down, part, T, EQ);
        k_final<<<(E + 255) / 256, 256, 0, stream>>>(m, dist, centers, w_rbf, w_sbf, w_up, part, (unsigned short*)d_out, E, EQ);
        return;
    }
    k_bin<<<P1_GRID, P1_THREADS, 0, stream>>>(idx_in, idx_out, angle, ang_freq, m, w_down, mdp, E, T, rec, tab, flags, NR, per, ly, wgmode);
    AccArgs aa;
    aa.rec = rec; aa.tab = tab; aa.flags = flags; aa.mdp = mdp; aa.part = part; aa.praw = praw; aa.wgmode = wgmode;
    aa.idx_in = idx_in; aa.idx_out = idx_out; aa.angle = angle; aa.ang_freq = ang_freq; aa.m = m; aa.w_down = w_down;
    aa.EP = EP; aa.O = O; aa.T = T; aa.c4 = (float)((double)E / 144.0);
    aa.dist = dist; aa.centers = centers; aa.w_rbf = w_rbf; aa.w_sbf = w_sbf; aa.w_up = w_up; aa.out = (unsigned short*)d_out; aa.E = E; aa.unused = 0;
    aa.ly = ly;
    k_acc<<<256, P2_THREADS, 0, stream>>>(aa);
}
```

```cpp
#include <hip/hip_runtime.h>
#include <hip/hip_bf16.h>
#include <stdint.h>

typedef int v4i __attribute__((ext_vector_type(4)));
typedef unsigned v4u __attribute__((ext_vector_type(4)));
typedef unsigned v2u __attribute__((ext_vector_type(2)));
typedef int v4i_rs __attribute__((ext_vector_type(4)));
typedef __attribute__((address_space(3))) unsigned lds_u32;
typedef __attribute__((address_space(3))) v4u lds_v4u;

static __device__ __forceinline__ float bf2f(unsigned short v) { return __uint_as_float(((unsigned)v) << 16); }
static __device__ __forceinline__ unsigned f2bf_bits(float f) {
    unsigned u = __float_as_uint(f);
    u += 0x7FFF + ((u >> 16) & 1);
    return u >> 16;
}
static __device__ __forceinline__ float lo_bf(unsigned u) { return __uint_as_float(u << 16); }
static __device__ __forceinline__ float hi_bf(unsigned u) { return __uint_as_float(u & 0xFFFF0000u); }
static __device__ __forceinline__ v4i_rs mk_rsrc(const void* p, unsigned bytes) {
    unsigned long long a = (unsigned long long)p;
    v4i_rs r = {(int)(unsigned)a, (int)(unsigned)((a >> 32) & 0xFFFF), (int)bytes, 0x00020000};
    return r;
}
static __device__ __forceinline__ lds_u32* ldsp(unsigned byte) { return (lds_u32*)(size_t)byte; }

#define P1_THREADS 1024
#define P1_GRID 256
#define NWS 256
#define CH (P1_THREADS * 4)
#define NT 1024
#define LMAX 20480
#define P2_THREADS 1024
#define OOB 0x80000000u
#define OBASE 20480
#define IDXB 4096
#define ST_AUX 16
#define LB 4096
#define LS 132
#define TINC 683
#define WL_OFF (LB + NT * LS)
#define MS_OFF (WL_OFF + 16 * 1024)
#define TB_OFF (MS_OFF + 128 + 320)
#define PC_OFF (TB_OFF + 32 * 33 * 4)
#define BIN_LDS (PC_OFF + 128)
#define SINC ((1u << 5) | (1u << 15) | (1u << 24))
#define SW0 (((tid & 3) << 6) | ((run & 16) << 1))
#define NSTAT 14
#define WM_CTR 1024
#define WM_BOX (WM_CTR + 128)
#define WM_LOC (WM_BOX + 256)
#define NTH "nt"
#define OUT_AUX 16
#define PR_AUX 18
#define TCH 7
#define CZERO ((512u << 1) | (512u << 11) | (512u << 22))
#define FL_SIZE (2 * P1_GRID * 2)

#define CB(c) ((((c) >> 3) << 15) | (((c) & 7) << 10))
#define TO(t) ((((t) & 15) * 4) | (((t) >> 6) << 6) | ((((t) >> 4) & 3) << 13))
#define HO 512
struct Grp { v4i io; v4i ii; v2u an; };
static __device__ __forceinline__ void load_grp(Grp& r, v4i_rs ro, v4i_rs ri, v4i_rs ra, unsigned c, unsigned vo16, unsigned vo8) {
    asm volatile("buffer_load_dwordx4 %0, %1, %2, %3 offen nt" : "=v"(r.io) : "v"(vo16), "s"(ro), "s"(c * 2));
    asm volatile("buffer_load_dwordx4 %0, %1, %2, %3 offen nt" : "=v"(r.ii) : "v"(vo16), "s"(ri), "s"(c * 2));
    asm volatile("buffer_load_dwordx2 %0, %1, %2, %3 offen nt" : "=v"(r.an) : "v"(vo8), "s"(ra), "s"(c));
}
#define TELLC ((rd & 6) == 0)

static __device__ __forceinline__ unsigned wave_scan_max(unsigned x) {
    x = max(x, (unsigned)__builtin_amdgcn_update_dpp(0, (int)x, 0x111, 0xF, 0xF, false));
    x = max(x, (unsigned)__builtin_amdgcn_update_dpp(0, (int)x, 0x112, 0xF, 0xF, false));
    x = max(x, (unsigned)__builtin_amdgcn_update_dpp(0, (int)x, 0x114, 0xF, 0xF, false));
    x = max(x, (unsigned)__builtin_amdgcn_update_dpp(0, (int)x, 0x118, 0xF, 0xF, false));
    x = max(x, (unsigned)__builtin_amdgcn_update_dpp(0, (int)x, 0x142, 0xA, 0xF, false));
    x = max(x, (unsigned)__builtin_amdgcn_update_dpp(0, (int)x, 0x143, 0xC, 0xF, false));
    return x;
}
static __device__ __forceinline__ unsigned row_scan_add(unsigned x) {
    x += (unsigned)__builtin_amdgcn_update_dpp(0, (int)x, 0x111, 0xF, 0xF, false);
    x += (unsigned)__builtin_amdgcn_update_dpp(0, (int)x, 0x112, 0xF, 0xF, false);
    x += (unsigned)__builtin_amdgcn_update_dpp(0, (int)x, 0x114, 0xF, 0xF, false);
    x += (unsigned)__builtin_amdgcn_update_dpp(0, (int)x, 0x118, 0xF, 0xF, false);
    return x;
}
static __device__ __forceinline__ unsigned wave_scan_add(unsigned x) {
    x = row_scan_add(x);
    x += (unsigned)__builtin_amdgcn_update_dpp(0, (int)x, 0x142, 0xA, 0xF, false);
    x += (unsigned)__builtin_amdgcn_update_dpp(0, (int)x, 0x143, 0xC, 0xF, false);
    return x;
}

static __device__ __forceinline__ float wave_scan_addf(float x) {
    x += __int_as_float(__builtin_amdgcn_update_dpp(0, __float_as_int(x), 0x111, 0xF, 0xF, false));
    x += __int_as_float(__builtin_amdgcn_update_dpp(0, __float_as_int(x), 0x112, 0xF, 0xF, false));
    x += __int_as_float(__builtin_amdgcn_update_dpp(0, __float_as_int(x), 0x114, 0xF, 0xF, false));
    x += __int_as_float(__builtin_amdgcn_update_dpp(0, __float_as_int(x), 0x118, 0xF, 0xF, false));
    x += __int_as_float(__builtin_amdgcn_update_dpp(0, __float_as_int(x), 0x142, 0xA, 0xF, false));
    x += __int_as_float(__builtin_amdgcn_update_dpp(0, __float_as_int(x), 0x143, 0xC, 0xF, false));
    return x;
}

struct Lay { unsigned K0, KX, dumu, fcap; };
#define PLANE (NWS * NT)
static __device__ __forceinline__ unsigned blk_line(unsigned w) { return (((w & 3) | ((w >> 3) << 2)) << 11) | ((w & 4) << 4); }
static __device__ __forceinline__ unsigned grp_line(unsigned q) { return ((q & 1) << 7) | ((q & 2) << 9); }
static __device__ __forceinline__ unsigned slot_line(unsigned s) { return (s >> 2) | ((s & 3) << 8); }
static __device__ __forceinline__ unsigned frag_unit(unsigned w, const Lay& ly) { return PLANE * 8 - (w + 1) * ly.fcap; }
static __device__ __forceinline__ char* rec_base(const char* p) { return (char*)p + ((int)(-(unsigned)(size_t)p & 0x1FFFFF) - PLANE * 128); }

static __device__ __forceinline__ void conv_tile(unsigned io, unsigned ii, unsigned& t4, unsigned& x) {
    unsigned y;
    asm("v_lshrrev_b32 %0, 5, %3\n v_and_b32 %1, 31, %4\n v_and_b32 %2, 0x7c, %0\n v_lshl_or_b32 %2, %1, 7, %2" : "=&v"(x), "=&v"(y), "=&v"(t4) : "v"(io), "v"(ii));
}
static __device__ __forceinline__ void conv_rec(unsigned io, unsigned ii, float ang, float frev, unsigned& A, unsigned& cq, unsigned& seen) {
    unsigned y;
    asm("v_lshrrev_b32 %1, 3, %3\n v_bfi_b32 %0, %4, %2, %0\n v_add_u32 %1, %5, %1\n v_lshl_add_u32 %0, %0, 17, %1"
        : "+v"(A), "=&v"(y) : "v"(io), "v"(ii), "s"(127u), "n"(IDXB << 17));
    float c = __builtin_amdgcn_cosf(__builtin_amdgcn_fractf(frev * ang));
    const unsigned cb = __float_as_uint(__builtin_fmaf(c, 511.0f, 12583424.0f));
    seen |= cb;
    cq = cb & 0x3FFu;
}
static __device__ __forceinline__ unsigned pad_rec(unsigned q) { return (0x7FFFu - (q & 511)) << 17; }

static __device__ __forceinline__ void prep_md(const v2u* mv, unsigned dst, const unsigned short* w_down, unsigned* mdp, unsigned plane, unsigned* mx, float* sq) {
    float wd[16];
#pragma unroll
    for (int i = 0; i < 8; i++) { unsigned u = ((const __attribute__((address_space(4))) unsigned*)w_down)[i]; wd[2 * i] = lo_bf(u); wd[2 * i + 1] = hi_bf(u); }
    unsigned o[2][4];
#pragma unroll
    for (int j = 0; j < 4; j++) {
        const float a[4] = {lo_bf(mv[j].x), hi_bf(mv[j].x), lo_bf(mv[j].y), hi_bf(mv[j].y)};
        float s[4];
#pragma unroll
        for (int d = 0; d < 4; d++) {
            s[d] = 0.f;
#pragma unroll
            for (int k = 0; k < 4; k++) s[d] += a[k] * wd[k * 4 + d];
            sq[d] += s[d] * s[d];
            mx[d] = max(mx[d], __float_as_uint(s[d]) & 0x7FFFFFFFu);
        }
#pragma unroll
        for (int p = 0; p < 2; p++) {
            const unsigned lo = f2bf_bits(s[2 * p]) & 0xFFFF;
            const unsigned sb = __float_as_uint(s[2 * p + 1]);
            const unsigned h = (unsigned)max((int)((sb & 0x7FFFFFFFu) - lo + 0x8000u), 0) & 0x7FFF0000u;
            o[p][j] = lo | h | (sb & 0x80000000u);
        }
    }
#pragma unroll
    for (int p = 0; p < 2; p++) {
        v4u v = {o[p][0], o[p][1], o[p][2], o[p][3]};
        *(v4u*)(mdp + (size_t)p * plane + dst) = v;
    }
}

static __device__ __forceinline__ unsigned touch_code(int tid) {
    unsigned alo, ahi, blo, r = 0;
    asm volatile("s_getpc_b64 s[96:97]\n s_add_u32 %0, s96, k_bin@rel32@lo+4\n s_addc_u32 %1, s97, k_bin@rel32@hi+12\n"
                 "s_getpc_b64 s[96:97]\n s_add_u32 %2, s96, k_naive@rel32@lo+4\n" : "=&s"(alo), "=&s"(ahi), "=&s"(blo) :: "scc", "s96", "s97");
    const unsigned size = blo - alo < 65536u ? blo - alo : 0u;
    const v4i_rs rc = {(int)alo, (int)(ahi & 0xFFFF), (int)size, 0x00020000};
    if (tid < 512) asm volatile("buffer_load_dword %0, %1, %2, 0 offen" : "=v"(r) : "v"(tid * 128), "s"(rc));
    return r;
}

struct Held { unsigned a[4], t[4], A[4], cd[4]; unsigned long long m[4]; };
struct Conv { unsigned A[4], t4[4], cq[4]; };
#define HB(k) "s_mov_b64 exec, %[m" #k "]\n ds_write_b32 %[a" #k "], %[A" #k "]\n ds_or_b32 %[t" #k "], %[c" #k "]\n"
static __device__ __forceinline__ void held_out(const Held& H) {
    asm volatile(HB(0) HB(1) HB(2) HB(3) "s_mov_b64 exec, -1\n"
                 :: [m0] "s"(H.m[0]), [m1] "s"(H.m[1]), [m2] "s"(H.m[2]), [m3] "s"(H.m[3]), [a0] "v"(H.a[0]), [a1] "v"(H.a[1]), [a2] "v"(H.a[2]), [a3] "v"(H.a[3]),
                   [t0] "v"(H.t[0]), [t1] "v"(H.t[1]), [t2] "v"(H.t[2]), [t3] "v"(H.t[3]),
                   [A0] "v"(H.A[0]), [A1] "v"(H.A[1]), [A2] "v"(H.A[2]), [A3] "v"(H.A[3]), [c0] "v"(H.cd[0]), [c1] "v"(H.cd[1]), [c2] "v"(H.cd[2]), [c3] "v"(H.cd[3])
                 : "memory");
}
static __device__ __forceinline__ void tickets(unsigned* v, const Conv& c, unsigned inc) {
    asm volatile("ds_add_rtn_u32 %0, %4, %8\n ds_add_rtn_u32 %1, %5, %8\n ds_add_rtn_u32 %2, %6, %8\n ds_add_rtn_u32 %3, %7, %8\n"
                 : "=&v"(v[0]), "=&v"(v[1]), "=&v"(v[2]), "=&v"(v[3]) : "v"(c.t4[0]), "v"(c.t4[1]), "v"(c.t4[2]), "v"(c.t4[3]), "v"(inc) : "memory");
}
static __device__ __forceinline__ void tickets_masked(unsigned* v, const Conv& c, unsigned inc, const unsigned long long* vm) {
#pragma unroll
    for (int k = 0; k < 4; k++) {
        v[k] = 0;
        asm volatile("s_mov_b64 exec, %3\n ds_add_rtn_u32 %0, %1, %2\n s_mov_b64 exec, -1" : "+v"(v[k]) : "v"(c.t4[k]), "v"(inc), "s"(vm[k]) : "memory");
    }
}
static __device__ __forceinline__ void place(Held& H, unsigned* v, Conv& c, const unsigned long long* vm, unsigned lb) {
    unsigned bt[4];
#pragma unroll
    for (int k = 0; k < 4; k++) asm("v_mad_u32_u24 %0, %1, %2, %3" : "=v"(bt[k]) : "v"(c.t4[k]), "n"(LS / 4), "v"(lb));
    asm volatile("s_waitcnt lgkmcnt(0)" : "+v"(v[0]), "+v"(v[1]), "+v"(v[2]), "+v"(v[3]));
#pragma unroll
    for (int k = 0; k < 4; k++) {
        H.m[k] = __ballot(v[k] >= 16384u);
        unsigned sh;
        asm("v_lshrrev_b32 %0, 7, %3\n v_lshrrev_b32 %2, 6, %3\n v_and_b32 %0, 0x7c, %0\n v_or_b32 %1, 12, %0\n v_add_u32 %0, %0, %4\n v_add_u32 %1, %1, %4"
            : "=&v"(H.a[k]), "=&v"(H.t[k]), "=&v"(sh) : "v"(v[k]), "v"(bt[k]));
        H.A[k] = c.A[k];
        asm("v_lshlrev_b32 %0, %1, %2" : "=v"(H.cd[k]) : "v"(sh), "v"(c.cq[k]));
        asm volatile("s_andn2_b64 exec, %[w], %[m]\n ds_write_b32 %[a], %[A]\n ds_or_b32 %[t], %[c]\n s_mov_b64 exec, -1\n"
                     :: [w] "s"(vm[k]), [m] "s"(H.m[k]), [a] "v"(H.a[k]), [A] "v"(H.A[k]), [t] "v"(H.t[k]), [c] "v"(H.cd[k]) : "memory", "scc");
    }
}
struct Own { v2u e; unsigned pc, fm, ovf, c0, c1; };
static __device__ __forceinline__ v4u read_unit(unsigned la) {
    const lds_u32* p = ldsp(la);
    v4u v = {p[0], p[1], p[2], p[3]};
    return v;
}
typedef __attribute__((address_space(3))) v2u lds_v2u;
static __device__ __forceinline__ unsigned long long low_lanes(unsigned groups) { return groups >= 8 ? ~0ull : (1ull << (groups * 8)) - 1; }
static __device__ __forceinline__ void flush_pass(v2u e, v4u v, unsigned long long act, unsigned k16, __amdgpu_buffer_rsrc_t rr) {
    unsigned vo;
    asm volatile("s_mov_b64 exec, %1\n ds_write_b32 %2, %3 offset:12\n s_mov_b64 exec, -1\n v_cndmask_b32 %0, %4, %5, %1"
                 : "=v"(vo) : "s"(act), "v"(e.x), "v"(0u), "v"(OOB), "v"(e.y + k16) : "memory");
    __builtin_amdgcn_raw_buffer_store_b128(v, rr, vo, 0, ST_AUX);
}
static __device__ __forceinline__ void flush(Own& o, unsigned& cnt, int tid, unsigned sinc, __amdgpu_buffer_rsrc_t rr) {
    const bool full = cnt >= 16384u;
    const unsigned long long bal = __ballot(full);
    const unsigned nfull = __popcll(bal);
    const unsigned wl = WL_OFF + (tid >> 6) * 1024;
    if (full) {
        unsigned sl;
        asm volatile("ds_add_rtn_u32 %0, %1, %2" : "=v"(sl) : "v"(o.pc), "v"(sinc) : "memory");
        const unsigned pos = __builtin_amdgcn_mbcnt_hi((unsigned)(bal >> 32), __builtin_amdgcn_mbcnt_lo((unsigned)bal, 0));
        cnt -= 24 * TINC;
        *ldsp(tid * 4) = cnt;
        o.ovf |= cnt;
        v2u e = o.e;
        o.fm >>= 1;
        o.e.y = min((o.e.y + PLANE * 128) ^ (o.fm & 8192u), OOB);
        const bool late = o.pc >= PC_OFF + 128;
        o.pc += 16;
        asm volatile("s_waitcnt lgkmcnt(0)" : "+v"(sl) :: "memory");
        sl ^= (sl << 3) & 0x18000300u;
        if (__builtin_expect(__ballot(late) != 0, 0)) {
            if (late) sl = (tid & 255) * SINC;
            else { o.c1 = __builtin_amdgcn_alignbit(o.c1, o.c0, 24); o.c0 = __builtin_amdgcn_alignbit(o.c0, sl, 24); }
        } else { o.c1 = __builtin_amdgcn_alignbit(o.c1, o.c0, 24); o.c0 = __builtin_amdgcn_alignbit(o.c0, sl, 24); }
        e.y |= sl & 0x19F80u;
        *(lds_v2u*)(size_t)(wl + pos * 8) = e;
    }
    const unsigned g = (tid >> 3) & 7, k16 = (tid & 7) * 16;
    v2u e0 = *(lds_v2u*)(size_t)(wl + g * 8), e1 = *(lds_v2u*)(size_t)(wl + g * 8 + 64);
    e0.x += k16; e1.x += k16;
    const v4u v0 = read_unit(e0.x), v1 = read_unit(e1.x);
    asm volatile("" ::: "memory");
    flush_pass(e0, v0, low_lanes(nfull), k16, rr);
    flush_pass(e1, v1, nfull > 8 ? low_lanes(nfull - 8) : 0ull, k16, rr);
    for (unsigned p = 16; p < nfull; p += 8) {
        v2u e = *(lds_v2u*)(size_t)(wl + (p + g) * 8);
        e.x += k16;
        const v4u v = read_unit(e.x);
        asm volatile("" ::: "memory");
        flush_pass(e, v, low_lanes(nfull - p), k16, rr);
    }
    asm volatile("s_waitcnt lgkmcnt(0)" ::: "memory");
}

extern "C" __global__ __launch_bounds__(P1_THREADS) void k_bin(const int* __restrict__ idx_in, const int* __restrict__ idx_out,
                                                    const unsigned short* __restrict__ angle, const unsigned short* __restrict__ ang_freq,
                                                    const unsigned short* __restrict__ m, const unsigned short* __restrict__ w_down,
                                                    unsigned* __restrict__ mdp, int E, int T,
                                                    char* __restrict__ rec_end, unsigned* __restrict__ tab, unsigned* __restrict__ flags,
                                                    int NR, int per, Lay ly, unsigned* __restrict__ wgmode) {
    __shared__ __attribute__((aligned(16))) unsigned L[BIN_LDS / 4];
    const int tid = threadIdx.x;
    const unsigned run = blockIdx.x;
    char* const rec = rec_base(rec_end);
    float freq;
    const v4i_rs ro = mk_rsrc(idx_out, (unsigned)T * 4), ri = mk_rsrc(idx_in, (unsigned)T * 4), ra = mk_rsrc(angle, (unsigned)T * 2);
    const __amdgpu_buffer_rsrc_t rr = __builtin_amdgcn_make_buffer_rsrc((void*)rec, (short)0,
        (int)(ly.dumu * 16),
        0x00020000);
    const unsigned vo16 = TO(tid) * 4, vo8 = TO(tid) * 2;
    const unsigned shard = ((run >> 3) ^ (run >> 5) ^ run) & 3;
    const bool leader = run == shard * 8;
    const v4i_rs rw = mk_rsrc(wgmode + WM_CTR, (128 + 256 + 2048) * 4);
    const int wvi = __builtin_amdgcn_readfirstlane(tid >> 6), wave0 = wvi == 0;
    const unsigned ctr_o = shard * 128, box_o = (128 + shard * 64 + ((run & 7) | ((run >> 5) << 3))) * 4;
#define PLO(j) (((j) & 1) * 8192 + ((j) & 2) * 2048 + ((j) >> 2) * 16384 + ((j) >> 1) * 1280)
    const bool prober = ((run ^ (run >> 2)) & 8) == 0;
    const unsigned loc_o = (tid & 63) - 1u < 8u ? (128 + 256 + (run >> 4) * 128 + ((tid & 63) - 1) * 16 + (run & 15)) * 4 : OOB;
    const unsigned ppg = (((run & 7) + 1) * PLANE + (run >> 4) * 16384) * 128;
    if (wave0) {
        const v4u z = {0, 0, 0, 0};
        asm volatile("buffer_store_dwordx4 %0, %1, %2, 0 offen sc0 sc1" :: "v"(z), "v"(prober ? ppg + PLO(tid >> 3) + (tid & 7) * 16 : OOB), "s"(mk_rsrc(rec, ly.dumu * 16)) : "memory");
        asm volatile("buffer_store_dword %0, %1, %2, 0 offen sc1" :: "v"(0u), "v"(tid == 0 ? (leader ? ctr_o : box_o) : loc_o), "s"(rw) : "memory");
    }
    unsigned tc0 = touch_code(tid);
    unsigned fbits;
    asm volatile("buffer_load_ushort %0, off, %1, 0" : "=v"(fbits) : "s"(mk_rsrc(ang_freq, 2)));
    v2u mv[4];
    const unsigned gp = run * 80 + (tid >> 5) * 4;
    {
        const v4i_rs rm = mk_rsrc(m, (unsigned)E * 8);
        const unsigned o = tid < 640 ? (((gp & ~255u) | ((gp >> 2) & 63)) * 32 + (tid & 31)) * 8 : OOB;
#pragma unroll
        for (int j = 0; j < 4; j++) asm volatile("buffer_load_dwordx2 %0, %1, %2, %3 offen " NTH : "=v"(mv[j]) : "v"(o), "s"(rm), "s"(j * 64 * 32 * 8));
    }
    Grp tg; bool tv[4];
    const unsigned tbase = (unsigned)NR * (P1_GRID * CH) + run * per;
    {
        const bool in = tid * 4 < per;
#pragma unroll
        for (int k = 0; k < 4; k++) tv[k] = in && tbase + tid * 4 + k < (unsigned)T;
        const unsigned o16 = in ? tid * 16 : OOB, o8 = in ? tid * 8 : OOB;
        asm volatile("buffer_load_dwordx4 %0, %1, %2, %3 offen nt" : "=v"(tg.io) : "v"(o16), "s"(ro), "s"(tbase * 4));
        asm volatile("buffer_load_dwordx4 %0, %1, %2, %3 offen nt" : "=v"(tg.ii) : "v"(o16), "s"(ri), "s"(tbase * 4));
        asm volatile("buffer_load_dwordx2 %0, %1, %2, %3 offen nt" : "=v"(tg.an) : "v"(o8), "s"(ra), "s"(tbase * 2));
    }
    Grp cur[2];
    load_grp(cur[0], ro, ri, ra, NR > 0 ? CB(run) * 2 : 0x7F000000u, vo16, vo8);
    load_grp(cur[1], ro, ri, ra, NR > 1 ? CB(run + P1_GRID) * 2 : 0x7F000000u, vo16, vo8);
    Own own;
    own.e.x = LB + tid * LS; own.pc = PC_OFF + (tid >> 8) * 4; own.c0 = own.c1 = 0;
    own.ovf = (unsigned)(size_t)L << 14;
    own.fm = 0; own.e.y = OOB;
    *ldsp(tid * 4) = 64;
#pragma unroll
    for (int i = 0; i < 8; i++) *ldsp(LB + tid * LS + i * 16 + 12) = 0;
    { const v2u z = {0, 0}; *(lds_v2u*)(size_t)(WL_OFF + (tid >> 6) * 1024 + (tid & 63) * 8) = z; }
    if (tid < 32) *ldsp(MS_OFF + tid * 4) = 0;
    if (tid < 32) *ldsp(PC_OFF + tid * 4) = (SW0 & 0xFC) * SINC;
    if (run == 0 && tid < 64) wgmode[(tid >> 1) * 32 + (tid & 1)] = 0;
    if (run == 1 && tid < 64) {
        v4u v = {pad_rec(tid), pad_rec(tid + 64), pad_rec(tid + 128), CZERO};
        *(v4u*)(rec + ((size_t)ly.dumu + tid) * 16) = v;
    }
    unsigned ans = 2;
    const unsigned tell_o = (tid & 63) < 16 ? (128 + 256 + (run >> 4) * 128 + (run & 7) * 16 + (tid & 63)) * 4 : OOB;
    if (wave0 && prober) {
        asm volatile("s_waitcnt vmcnt(16) lgkmcnt(0)" ::: "memory");
        if ((run & 7) < ly.KX) {
            unsigned long long t[7];
            unsigned x;
#define PRB1(i, j) "s_memtime %" #i "\n s_load_dword %7, %8, %" #j "\n s_waitcnt lgkmcnt(0)\n"
            asm volatile(PRB1(0, 9) PRB1(1, 10) PRB1(2, 11) PRB1(3, 12) PRB1(4, 13) PRB1(5, 14) "s_memtime %6\n s_waitcnt lgkmcnt(0)"
                         : "=&s"(t[0]), "=&s"(t[1]), "=&s"(t[2]), "=&s"(t[3]), "=&s"(t[4]), "=&s"(t[5]), "=&s"(t[6]), "=&s"(x)
                         : "s"(rec + ppg), "n"(PLO(0)), "n"(PLO(1)), "n"(PLO(2)), "n"(PLO(3)), "n"(PLO(4)), "n"(PLO(5)) : "memory");
            unsigned d[6];
#pragma unroll
            for (int i = 0; i < 6; i++) d[i] = (unsigned)t[i + 1] - (unsigned)t[i];
            ans = 2 | (((min(min(d[0], d[2]), d[4]) > min(min(d[1], d[3]), d[5]) ? 1 : 0) ^ (run >> 2)) & 1);
        }
        asm volatile("buffer_store_dword %0, %1, %2, 0 offen sc1" :: "v"(ans), "v"(tell_o), "s"(rw) : "memory");
    }
    unsigned seen = 0;
    auto convert1 = [&](Grp& gp, Conv& o) {
        conv_tile(gp.io.x, gp.ii.x, o.t4[0], o.A[0]); conv_tile(gp.io.y, gp.ii.y, o.t4[1], o.A[1]);
        conv_tile(gp.io.z, gp.ii.z, o.t4[2], o.A[2]); conv_tile(gp.io.w, gp.ii.w, o.t4[3], o.A[3]);
    };
    auto convert2 = [&](Grp& gp, Conv& o) {
        asm volatile("" : "+v"(gp.an), "+v"(o.A[0]), "+v"(o.A[1]), "+v"(o.A[2]), "+v"(o.A[3]));
        conv_rec(gp.io.x, gp.ii.x, lo_bf(gp.an.x), freq, o.A[0], o.cq[0], seen); conv_rec(gp.io.y, gp.ii.y, hi_bf(gp.an.x), freq, o.A[1], o.cq[1], seen);
        conv_rec(gp.io.z, gp.ii.z, lo_bf(gp.an.y), freq, o.A[2], o.cq[2], seen); conv_rec(gp.io.w, gp.ii.w, hi_bf(gp.an.y), freq, o.A[3], o.cq[3], seen);
#pragma unroll
        for (int k = 0; k < 4; k++) asm volatile("" : "+v"(o.A[k]), "+v"(o.cq[k]));
    };
    const unsigned tinc = TINC, lb7 = LB, sinc = SINC;
    Held H;
    {
        asm volatile("s_waitcnt vmcnt(9)" : "+v"(mv[0]), "+v"(mv[1]), "+v"(mv[2]), "+v"(mv[3]));
        if (tid < 640) {
            unsigned mx[4] = {0, 0, 0, 0};
            float sq[4] = {0.f, 0.f, 0.f, 0.f};
            prep_md(mv, (tid & 31) * LMAX + gp, w_down, mdp, 32 * LMAX, mx, sq);
#pragma unroll
            for (int d = 0; d < 4; d++) {
                const unsigned v = wave_scan_max(mx[d]);
                const float s = wave_scan_addf(sq[d]);
                if ((tid & 63) == 63) { *ldsp(MS_OFF + 128 + (tid >> 6) * 32 + d * 4) = v; *ldsp(MS_OFF + 128 + (tid >> 6) * 32 + 16 + d * 4) = __float_as_uint(s); }
            }
        }
        asm volatile("s_waitcnt vmcnt(3)" : "+v"(tg.io), "+v"(tg.ii), "+v"(tg.an), "+v"(fbits));
        asm volatile("" :: "v"(tc0));
        freq = lo_bf(fbits) * 0.15915494309189535f;
        if (T & 1) {
            const unsigned rel = (unsigned)T - 1 - (tbase + tid * 4);
            if (tv[0] && rel < 4) { const unsigned av = angle[T - 1]; if (rel == 0) tg.an.x = av; else tg.an.y = av; }
        }
        __syncthreads();
        if (tid >= P1_THREADS - 64) {
            const unsigned sa = MS_OFF + 128 + (tid & 15) * 32 + ((tid >> 4) & 3) * 4;
            unsigned mx = (tid & 15) < 10 ? *ldsp(sa) : 0u;
            float sq = (tid & 15) < 10 ? __uint_as_float(*ldsp(sa + 16)) : 0.f;
#pragma unroll
            for (int i = 0; i < 4; i++) {
                mx = max(mx, (unsigned)(i == 0 ? __builtin_amdgcn_update_dpp(0, (int)mx, 0x111, 0xF, 0xF, false) : i == 1 ? __builtin_amdgcn_update_dpp(0, (int)mx, 0x112, 0xF, 0xF, false)
                                        : i == 2 ? __builtin_amdgcn_update_dpp(0, (int)mx, 0x114, 0xF, 0xF, false) : __builtin_amdgcn_update_dpp(0, (int)mx, 0x118, 0xF, 0xF, false)));
                const int si = __float_as_int(sq);
                sq += __int_as_float(i == 0 ? __builtin_amdgcn_update_dpp(0, si, 0x111, 0xF, 0xF, false) : i == 1 ? __builtin_amdgcn_update_dpp(0, si, 0x112, 0xF, 0xF, false)
                                     : i == 2 ? __builtin_amdgcn_update_dpp(0, si, 0x114, 0xF, 0xF, false) : __builtin_amdgcn_update_dpp(0, si, 0x118, 0xF, 0xF, false));
            }
            mx = min(mx ? (mx >> 16) + 2 : 0u, 0x7F80u);
            const unsigned fx = (mx << 16) | (unsigned)__builtin_amdgcn_update_dpp(0, (int)mx, 0x142, 0xA, 0xF, false);
            const unsigned fy = (__float_as_uint(sq) & 0xFFFF0000u) | ((unsigned)__builtin_amdgcn_update_dpp(0, __float_as_int(sq), 0x142, 0xA, 0xF, false) >> 16);
            if ((tid & 31) == 31) __hip_atomic_store((unsigned long long*)(flags + (((tid >> 5) & 1) * P1_GRID + run) * 2), ((unsigned long long)fy << 32) | fx, __ATOMIC_RELAXED, __HIP_MEMORY_SCOPE_AGENT);
        }
        unsigned long long vm[4];
#pragma unroll
        for (int k = 0; k < 4; k++) vm[k] = __ballot(tv[k]);
        if (vm[0]) {
            unsigned v[4];
            Conv cv;
            convert1(tg, cv);
            tickets_masked(v, cv, tinc, vm);
            convert2(tg, cv);
            place(H, v, cv, vm, lb7);
            if ((H.m[0] & vm[0]) | (H.m[1] & vm[1]) | (H.m[2] & vm[2]) | (H.m[3] & vm[3])) own.ovf |= 16384u;
        }
#pragma unroll
        for (int k = 0; k < 4; k++) { H.m[k] = 0; H.a[k] = 0; H.t[k] = 0; H.A[k] = 0; H.cd[k] = 0; }
        if (NR < 2) asm volatile("s_waitcnt vmcnt(0)" ::: "memory");
    }
    const unsigned long long vm[4] = {~0ull, ~0ull, ~0ull, ~0ull};
    unsigned ca = NR > 1 ? CB(run + P1_GRID) * 2 : 16u;
    bool act = true;
#define HH 16
    const unsigned G = NR > NSTAT ? (NR - NSTAT) * (P1_GRID / 4) : 0, GH = G >= HH ? G - HH : G, GE = G >= HH ? G + HH : G;
    unsigned got = 0;
    unsigned dl = 0;
    int st = leader ? 1 : 0;
    auto answers = [&](bool need) {
        unsigned lc = __builtin_amdgcn_readfirstlane(*ldsp(MS_OFF + 108));
        if (!lc && need) {
            for (int i = 0; i < 4096 && !lc; i++) {
                unsigned x = 0;
                asm volatile("buffer_atomic_add %0, %1, %2, 0 offen sc0\n s_waitcnt vmcnt(0)" : "+v"(x) : "v"(loc_o), "s"(rw) : "memory");
                if ((__ballot(x != 0) & 510) == 510) lc = (unsigned)__ballot(x & 1) | 1;
            }
            if (!lc) { own.ovf |= 16384u; dl = 256; }
        }
        if (lc) {
            dl = (((lc >> 1) ^ lc ^ 1) & 255) | 256;
            own.fm = (dl & 255) << 13;
            own.e.y = ((PLANE + blk_line(run) + grp_line(tid >> 8)) * 128) ^ (own.fm & 8192u);
        }
    };
    unsigned cnt;
    if (NR == 0) { asm volatile("s_waitcnt lgkmcnt(0)" ::: "memory"); __syncthreads(); cnt = *ldsp(tid * 4); own.ovf |= cnt; }
    for (int rd = 0; NR > 0; rd += 2) {
        bool done = false;
#pragma unroll
        for (int r = 0; r < 2; r++) {
            unsigned v[4];
            Conv cv;
            const bool nact = wvi >= (ca & 31);
            const unsigned cl = nact ? ca & ~31u : 0x7F000000u;
            if (r || rd) load_grp(cur[r ^ 1], ro, ri, ra, cl, vo16, vo8);
            held_out(H);
            asm volatile("s_cmp_eq_u32 %3, 0\n s_cbranch_scc1 1f\n s_waitcnt vmcnt(5)\n 1:\n s_waitcnt vmcnt(8)" : "+v"(cur[r].io), "+v"(cur[r].ii), "+v"(cur[r].an) : "s"(wvi) : "scc");
            if (act) {
                convert1(cur[r], cv);
                tickets(v, cv, tinc);
                convert2(cur[r], cv);
                place(H, v, cv, vm, lb7);
            } else H.m[0] = H.m[1] = H.m[2] = H.m[3] = 0;
            act = nact;
            if (wave0) {
                asm volatile("s_waitcnt vmcnt(5)" : "+v"(got));
                const unsigned g = __builtin_amdgcn_readfirstlane(got);
                unsigned nx = rd + r + 2 < NR ? ca + P1_GRID * (CH * 2) : 16u;
                if (rd + r + 2 >= NSTAT) {
                    unsigned c = g, sub = 0;
                    if (g >= GH) { c = GH + ((g - GH) >> 1); sub = ((g - GH) & 1) * (HO * 2) - HO * 2 + 8; }
                    nx = st == 2 && g < GE ? CB(NSTAT * P1_GRID + c * 4 + shard) * 2 + sub : 16u;
                }
                if (st == 0 && g != 0) st = 1;
                if (st == 0 && rd + r + 3 == NSTAT) {
                    for (int i = 0; i < 128 && st == 0; i++) {
                        unsigned x = 0;
                        asm volatile("buffer_atomic_add %0, %1, %2, 0 offen sc0\n s_waitcnt vmcnt(0)" : "+v"(x) : "v"(tid == 0 ? box_o : OOB), "s"(rw) : "memory");
                        if (__builtin_amdgcn_readfirstlane(x) != 0) st = 1;
                    }
                }
                if (st == 1 && rd + r + 3 >= NSTAT) st = 2;
                if (ca == 16) nx = 16;
                if ((tid & 63) == 0) *ldsp(MS_OFF + 104) = nx;
                bool ask = !dl;
                if (ask && (__ballot(got != 0) & 510) == 510) { const unsigned lc = (unsigned)__ballot(got & 1) | 1; if (tid == 0) *ldsp(MS_OFF + 108) = lc; ask = false; }
                got = st == 2 && tid == 0 ? 1u : 0u;
                const bool tk = st != 1 && nx != 16;
                asm volatile("buffer_atomic_add %0, %1, %2, 0 offen sc0" : "+v"(got) : "v"(tid == 0 ? (tk ? (st == 2 ? ctr_o : box_o) : OOB) : ask ? loc_o : OOB), "s"(rw) : "memory");
                asm volatile("buffer_store_dword %0, %1, %2, 0 offen sc1" :: "v"(ans), "v"(prober && r == 0 && TELLC ? tell_o : OOB), "s"(rw) : "memory");
                asm volatile("buffer_store_dword %0, %1, %2, 0 offen sc1" :: "v"(1u), "v"(leader ? (128 + shard * 64 + tid) * 4 : OOB), "s"(rw) : "memory");
            }
            asm volatile("s_waitcnt lgkmcnt(0)" ::: "memory");
            __syncthreads();
            cnt = *ldsp(tid * 4);
            const unsigned nx = *ldsp(MS_OFF + 104);
            if (!dl) answers(__ballot(cnt >= 16384u) != 0);
            flush(own, cnt, tid, sinc, rr);
            if (ca == 16) { done = true; break; }
            ca = __builtin_amdgcn_readfirstlane(nx);
            __syncthreads();
        }
        if (done) break;
    }
    unsigned tc1 = touch_code(tid);
    {
        cnt &= 16383u;
        const unsigned nu = (cnt + 2 * TINC) >> 11;
        const unsigned inc = wave_scan_add(nu), off = inc - nu;
        const unsigned tot = ((unsigned)__builtin_amdgcn_readlane((int)inc, 63) + 7) & ~7u;
        unsigned fb = 0;
        if ((tid & 63) == 0) fb = atomicAdd(L + MS_OFF / 4, tot);
        const unsigned wl = WL_OFF + (tid >> 6) * 1024;
        if (nu) {
            const unsigned la = wl + off * 2, src = (tid & 63) * LS;
#pragma unroll
            for (int k = 7; k >= 0; k--) asm volatile("ds_write_b16 %0, %1 offset:%2" :: "v"(la), "v"(src + k * 16), "n"(k * 2) : "memory");
        }
        if ((cnt >> 11) < nu) {
            const unsigned c2 = cnt + TINC, ua = own.e.x + ((cnt >> 7) & 0x70);
            const bool two = (c2 >> 11) < nu;
            *ldsp(ua + 8) = pad_rec(run);
            if (two) *ldsp(ua + 4) = pad_rec(run + 256);
            asm volatile("ds_or_b32 %0, %1 offset:12" :: "v"(ua), "v"(two ? (512u << 11) | (512u << 22) : 512u << 22) : "memory");
        }
        fb = __builtin_amdgcn_readfirstlane(fb);
        const unsigned nl = (own.pc - PC_OFF) >> 4, tp = ((tid & 31) << 5) | ((tid ^ (tid >> 5)) & 31), tq = tid ^ (tid >> 5);
        const unsigned long long cc = (((unsigned long long)own.c1 << 32) | own.c0) << ((64 - 8 * min(nl, 8u)) & 63);
        *ldsp(TB_OFF + ((tid & 31) * 33 + (tid >> 5)) * 4) = min(nl, 31u) | (nu << 5) | ((dl & 255) << 9) | ((fb + off) << 18);
        *ldsp(LB + 128 + tp * LS) = (unsigned)(cc >> 32);
        if (own.ovf >= 16384u || nl > ly.KX || (seen & 0x30000000u) || fb + tot > ly.fcap) {
            __hip_atomic_store(flags + run * 2, 0x7F807F80u, __ATOMIC_RELAXED, __HIP_MEMORY_SCOPE_AGENT);
            __hip_atomic_store(flags + (P1_GRID + run) * 2, 0x7F807F80u, __ATOMIC_RELAXED, __HIP_MEMORY_SCOPE_AGENT);
        }
        typedef __attribute__((address_space(3))) unsigned short lds_u16;
        const unsigned lb = LB + (tid >> 6) * (64 * LS), ln = tid & 63;
        unsigned sa[5];
#pragma unroll
        for (int i = 0; i < 5; i++) sa[i] = lb + (*(lds_u16*)(size_t)(wl + i * 128 + ln * 2) & 0x3FFCu);
        asm volatile("s_waitcnt lgkmcnt(0)" ::: "memory");
        __syncthreads();
        held_out(H);
        *ldsp(tp * 4) = (unsigned)cc;
        typedef unsigned v3u __attribute__((ext_vector_type(3)));
        v3u tw;
        tw.x = *ldsp(TB_OFF + (tid + (tid >> 5)) * 4); tw.y = *ldsp(LB + 128 + tq * LS);
        asm volatile("s_waitcnt lgkmcnt(0)" ::: "memory");
        __syncthreads();
        const __amdgpu_buffer_rsrc_t rf = __builtin_amdgcn_make_buffer_rsrc((void*)(rec + (size_t)(frag_unit(run, ly) + fb) * 16), (short)0, fb + tot > ly.fcap ? 0 : tot * 16, 0x00020000);
        v4u fv[5];
#pragma unroll
        for (int i = 0; i < 5; i++) fv[i] = read_unit(sa[i]);
        tw.z = *ldsp(tq * 4);
        asm volatile("buffer_store_dwordx3 %0, %1, %2, 0 offen sc1" :: "v"(tw), "v"(tid * 12), "s"(mk_rsrc(tab + run * (NT * 3), NT * 12)) : "memory");
#pragma unroll
        for (int i = 0; i < 5; i++) __builtin_amdgcn_raw_buffer_store_b128(fv[i], rf, ln * 16 + i * 1024, 0, 16);
        for (unsigned p = 320; p < tot; p += 64) {
            const v4u v = read_unit(lb + (*(lds_u16*)(size_t)(wl + (p + ln) * 2) & 0x3FFCu));
            __builtin_amdgcn_raw_buffer_store_b128(v, rf, (p + ln) * 16, 0, 16);
        }
        asm volatile("s_waitcnt vmcnt(0)" :: "v"(tc1));
    }
    asm volatile("s_waitcnt vmcnt(0)" :: "v"(cur[0].io), "v"(cur[0].ii), "v"(cur[0].an), "v"(cur[1].io), "v"(cur[1].ii), "v"(cur[1].an), "v"(got));
}

struct AccArgs {
    const char* rec; const unsigned* tab; const unsigned* flags; const unsigned* mdp; float* part; unsigned* praw; unsigned* wgmode;
    const int* idx_in; const int* idx_out; const unsigned short* angle; const unsigned short* ang_freq; const unsigned short* m; const unsigned short* w_down;
    const unsigned short* dist; const unsigned short* centers; const unsigned short* w_rbf; const unsigned short* w_sbf; const unsigned short* w_up;
    unsigned short* out;
    int EP; unsigned O; int T; float c4; int E; int unused;
    Lay ly;
};
typedef char AccArgs_has_no_padding[sizeof(AccArgs) == 19 * 8 + 10 * 4 ? 1 : -1];

typedef const __attribute__((address_space(4))) unsigned* cptr;
static __device__ __forceinline__ void edge_gh(float d, const unsigned short* centers, const unsigned short* w_sbf, const unsigned short* w_rbf, float* g, float* h) {
#pragma unroll
    for (int k = 0; k < 4; k++) g[k] = h[k] = 0.f;
#pragma unroll
    for (int r = 0; r < 8; r++) {
        const unsigned cu = ((cptr)centers)[r >> 1];
        const float df = d - ((r & 1) ? hi_bf(cu) : lo_bf(cu));
        const float rb = __expf(-10.f * df * df);
#pragma unroll
        for (int k = 0; k < 2; k++) {
            const unsigned u = ((cptr)w_sbf)[r * 2 + k], v = ((cptr)w_rbf)[r * 2 + k];
            g[2 * k] += rb * lo_bf(u); g[2 * k + 1] += rb * hi_bf(u); h[2 * k] += rb * lo_bf(v); h[2 * k + 1] += rb * hi_bf(v);
        }
    }
}
static __device__ __forceinline__ v2u edge_out(const float* agg, const float* g, const float* h, unsigned m0, unsigned m1, const unsigned* wu) {
    float a[4];
#pragma unroll
    for (int k = 0; k < 4; k++) a[k] = agg[k] * g[k];
    const float mm[4] = {lo_bf(m0), hi_bf(m0), lo_bf(m1), hi_bf(m1)};
    float o[4];
#pragma unroll
    for (int dd = 0; dd < 4; dd++) {
        float s = 0.f;
#pragma unroll
        for (int k = 0; k < 4; k++) { const unsigned u = wu[k * 2 + (dd >> 1)]; s += a[k] * ((dd & 1) ? hi_bf(u) : lo_bf(u)); }
        o[dd] = mm[dd] + s * h[dd];
    }
    v2u r;
    asm("v_cvt_pk_bf16_f32 %0, %1, %2" : "=v"(r.x) : "v"(o[0]), "v"(o[1]));
    asm("v_cvt_pk_bf16_f32 %0, %1, %2" : "=v"(r.y) : "v"(o[2]), "v"(o[3]));
    return r;
}

typedef const __attribute__((address_space(4))) AccArgs* kaptr;
static __device__ __forceinline__ void acc_fallback(unsigned* lds, unsigned mode, const __attribute__((address_space(4))) AccArgs& a_, int tid) {
    const char* rec = rec_base(a_.rec); const unsigned* flags = a_.flags; const unsigned* mdp = a_.mdp;
    const int EP = a_.EP, T = a_.T; const Lay ly = {a_.ly.K0, a_.ly.KX, a_.ly.dumu, a_.ly.fcap};
    const int lane = tid & 63;
    const int xcd = blockIdx.x & 7, jj = blockIdx.x >> 3;
    const int qt = xcd >> 1, cp = jj & 1, b = (xcd & 1) * 16 + (jj >> 1);
    const unsigned O = a_.O;
    float* pout0 = a_.part + (size_t)(qt * 4 + cp * 2) * EP + (size_t)b * O;
    const unsigned G = tid >> 3, k = tid & 7;
    {
        unsigned* praw = a_.praw + (size_t)(qt * 2 + cp) * EP + (size_t)b * O;
        for (unsigned i = tid; i < O; i += P2_THREADS) praw[i] = 0;
    }
    if (mode == 1) {
        unsigned* acc = lds + OBASE;
        const float Xm = 2139095040.0f / (float)(NWS * (ly.KX * 24 + 24)) - 2.0f;
#pragma unroll 1
        for (int c = 0; c < 2; c++) {
            __syncthreads();
            if (tid == 0) lds[0] = 0;
            __syncthreads();
            if (tid < NWS) { unsigned v = wave_scan_max((flags[(cp * P1_GRID + tid) * 2] >> (16 * c)) & 0xFFFF); if (lane == 63) atomicMax(&lds[0], v); }
            __syncthreads();
            unsigned mbits = __builtin_amdgcn_readfirstlane(lds[0]);
            if (mbits == 0) mbits = 0x3f80;
            const float M = __uint_as_float(mbits << 16);
            const float S = Xm / (511.0f * M), inv = M / Xm;
            float racc[20];
#pragma unroll
            for (int r = 0; r < 20; r++) racc[r] = 0.f;
#pragma unroll 1
            for (int s = 0; s < 8; s++) {
                __syncthreads();
                const unsigned* src = mdp + (size_t)cp * (32 * LMAX) + (size_t)(qt * 8 + s) * LMAX;
                for (unsigned i = tid; i < LMAX; i += P2_THREADS) {
                    unsigned w = src[i];
                    lds[(i & ~255u) | ((i & 3) << 6) | ((i >> 2) & 63)] = __float_as_uint((c ? __uint_as_float(w) : lo_bf(w)) * S);
                }
                for (int i = tid; i < OBASE; i += P2_THREADS) acc[i] = 0;
                __syncthreads();
                const unsigned t = b * 32 + qt * 8 + s;
                for (unsigned r = G; r < NWS; r += 128) {
                    const unsigned* te = a_.tab + (r * NT + t) * 3;
                    const unsigned tw = te[0], nl = tw & 31, nu = (tw >> 5) & 15;
                    for (unsigned l = 0; l <= nl; l++) {
                        unsigned u;
                        if (l < nl) u = (((l + 1) * PLANE + blk_line(r) + grp_line(qt) + slot_line(l < 8 ? (te[1 + (l >> 2)] >> (24 - 8 * (l & 3))) & 255 : s * 32 + b)) ^ ((__popc((tw >> 9) & 511 & ((2u << l) - 1)) & 1) << 6)) * 8 + k;
                        else { if (k >= nu) break; u = frag_unit(r, ly) + (tw >> 18) + k; }
                        v4u q = *(const v4u*)(rec + (size_t)u * 16);
                        unsigned a[3] = {q.x, q.y, q.z};
#pragma unroll
                        for (int j = 0; j < 3; j++) {
                            const int cq = (int)((q.w >> (j == 0 ? 1 : j == 1 ? 11 : 22)) & 1023) - 512;
                            const int x = __float2int_rn(__uint_as_float(lds[(a[j] >> 2) & 0x7FFF]) * (float)cq);
                            if ((a[j] >> 17) - IDXB < OBASE) atomicAdd(&acc[(a[j] >> 17) - IDXB], (unsigned)x);
                        }
                    }
                }
                __syncthreads();
#pragma unroll
                for (int r = 0; r < 20; r++) racc[r] += (float)(int)acc[r * P2_THREADS + tid] * inv;
            }
#pragma unroll
            for (int r = 0; r < 20; r++) if ((unsigned)(r * P2_THREADS + tid) < O) pout0[(size_t)c * EP + r * P2_THREADS + tid] = racc[r];
        }
    }
    if (mode == 2) {
        float* facc = (float*)lds;
        const float freq = bf2f(a_.ang_freq[0]);
        float wd[2][4];
#pragma unroll
        for (int kk = 0; kk < 4; kk++) { wd[0][kk] = bf2f(a_.w_down[kk * 4 + cp * 2]); wd[1][kk] = bf2f(a_.w_down[kk * 4 + cp * 2 + 1]); }
        __syncthreads();
        for (int i = tid; i < 2 * OBASE; i += P2_THREADS) facc[i] = 0.f;
        __syncthreads();
        for (int t = tid; t < T; t += P2_THREADS) {
            const unsigned io = a_.idx_out[t], ii = a_.idx_in[t];
            if (((io >> 7) & 31) == (unsigned)b && ((ii >> 3) & 3) == (unsigned)qt) {
                const v2u mv = ((const v2u*)a_.m)[ii];
                const float a[4] = {lo_bf(mv.x), hi_bf(mv.x), lo_bf(mv.y), hi_bf(mv.y)};
                const float cs = __cosf(freq * bf2f(a_.angle[t]));
                const unsigned ol = (io & 127u) | ((io >> 5) & ~127u);
#pragma unroll
                for (int c = 0; c < 2; c++)
                    atomicAdd(&facc[c * OBASE + ol], (a[0] * wd[c][0] + a[1] * wd[c][1] + a[2] * wd[c][2] + a[3] * wd[c][3]) * cs);
            }
        }
        __syncthreads();
        for (unsigned idx = tid; idx < O; idx += P2_THREADS) { pout0[idx] = facc[idx]; pout0[(size_t)EP + idx] = facc[OBASE + idx]; }
    }
}

#define SWAPX 2
#define WXOR ((wave & 1) << 2)
#define RAUX ""
#define J0 (wave >> 1)
#define IDXMASK "0x8fffffff"
#define STR2(x) #x
#define STR(x) STR2(x)
#define PLAN_IT 160

extern "C" __global__ __launch_bounds__(P2_THREADS) void k_acc(AccArgs a_) {
    const Lay ly = a_.ly;
    __shared__ __attribute__((aligned(16))) unsigned lds[2 * OBASE];
    const int tid = threadIdx.x;
    const int wave = __builtin_amdgcn_readfirstlane(tid >> 6), lane = tid & 63;
    const int xcd = blockIdx.x & 7, jj = blockIdx.x >> 3;
    const int qt = xcd >> 1, cp = jj & 1, b = (xcd & 1) * 16 + (jj >> 1);
    const unsigned O = a_.O;
    unsigned stv = 0;
    unsigned tc2 = touch_code(tid);
    unsigned* praw = a_.praw + (size_t)(qt * 2 + cp) * a_.EP + (size_t)b * O;
    const unsigned* mds = a_.mdp + (size_t)cp * (32 * LMAX) + (size_t)(qt * 8) * LMAX;
    unsigned mode = (unsigned)(size_t)lds ? 2u : 0u;
    unsigned exs = 0, bad = 0;
    v4u mm[2], dd;
    const int q = qt * 2 + cp;
    unsigned t16 = tid * 16;
    if (mode == 0) {
        const unsigned w = wave * 16 + ((lane & 15) ^ (cp ? SWAPX : 0) ^ (WXOR)), t0 = b * 32 + qt * 8 + (lane >> 4) * 2;
        v4i_rs rs = mk_rsrc(rec_base(a_.rec), ly.dumu + 64);
        rs.y |= 16 << 16;
        const v4i_rs rm = mk_rsrc(mds, 8 * LMAX * 4);
        const v4i_rs rp0 = mk_rsrc(praw, O * 4);
        const v4i_rs rt = mk_rsrc(a_.tab, (NT * NWS * 3 + FL_SIZE) * 4);
        const v4i_rs rmm = mk_rsrc(a_.m, a_.E * 8), rd = mk_rsrc(a_.dist, a_.E * 2);
        asm volatile(
            "s_setprio 3\n"
            "buffer_load_dwordx3 v[32:34], %[tof], %[rt], 0 offen\n"
            "buffer_load_dwordx3 v[36:38], %[tof], %[rt], 0 offen offset:12\n"
            "v_and_b32 v10, 1023, %[tid16]\n"
            "v_bfrev_b32 v12, 1\n"
            "v_add_u32 v11, %[pb], v10\n"
            "v_bfrev_b32 v13, 1\n"
            "v_bfrev_b32 v14, 1\n"
            "v_bfrev_b32 v15, 1\n"
            "ds_write_b128 v11, v[12:15] offset:0\n"
            "ds_write_b128 v11, v[12:15] offset:1024\n"
            "ds_write_b128 v11, v[12:15] offset:2048\n"
            "ds_write_b128 v11, v[12:15] offset:3072\n"
            "ds_write_b128 v11, v[12:15] offset:4096\n"
            "v_bfrev_b32 v80, 1\n"
            "v_mov_b32 v84, 0xb9000000\n"
            "v_mov_b32 v88, 0\n"
            "v_add_u32 v89, 81920, %[tid16]\n"
            "v_add_u32 v90, 65536, v89\n"
            "v_lshrrev_b32 v16, 4, v10\n"
            "v_add_u32 v83, %[dumu], v16\n"
            "v_and_b32 v17, 7, v16\n"
            "v_lshl_or_b32 v82, v17, 28, v17\n"
            "v_lshrrev_b32 v18, 3, v16\n"
            "v_lshl_add_u32 v17, v17, 3, v18\n"
            "v_lshlrev_b32 v17, 2, v17\n"
            "v_add_u32 v17, %[pb], v17\n"
            "s_getpc_b64 s[70:71]\n"
            "Lpc%=:\n"
            "s_add_u32 s72, s70, Lchg2%=-Lpc%=\n s_addc_u32 s73, s71, 0\n"
            "s_add_u32 s66, s70, Lchg3%=-Lpc%=\n s_addc_u32 s67, s71, 0\n"
            "s_add_u32 s70, s70, Lchg1%=-Lpc%=\n s_addc_u32 s71, s71, 0\n"
            "s_waitcnt vmcnt(0)\n"
            "buffer_load_dwordx4 v[92:95], v10, %[rt], %[fof] offen\n"
            "buffer_load_dwordx4 v[96:99], v10, %[rt], %[fof] offen offset:1024\n"
            "s_mov_b32 s94, 0\n"
            "buffer_load_dwordx4 v[40:43], %[tid16], %[rm], s94 offen\n"
            "s_add_u32 s96, s94, 16384\n"
            "buffer_load_dwordx4 v[44:47], %[tid16], %[rm], s96 offen\n"
            "s_add_u32 s96, s94, 32768\n"
            "buffer_load_dwordx4 v[48:51], %[tid16], %[rm], s96 offen\n"
            "s_add_u32 s96, s94, 49152\n"
            "buffer_load_dwordx4 v[52:55], %[tid16], %[rm], s96 offen\n"
            "s_add_u32 s96, s94, 65536\n"
            "buffer_load_dwordx4 v[56:59], %[tid16], %[rm], s96 offen\n"
            "v_and_b32 v20, 31, v32\n v_and_b32 v21, 31, v36\n v_bfe_u32 v22, v32, 5, 4\n v_bfe_u32 v23, v36, 5, 4\n"
            "v_min_u32 v24, 1, v22\n v_min_u32 v25, 1, v23\n v_add_u32 v24, v24, v20\n v_add_u32 v25, v25, v21\n"
            "v_lshl_or_b32 v24, v25, 16, v24\n"
            "v_and_b32 v19, 0x3fe00, v32\n v_lshrrev_b32 v8, 18, v32\n v_lshrrev_b32 v9, 18, v36\n"
            "v_add_u32_dpp v25, v24, v24 row_shr:1 row_mask:0xf bank_mask:0xf bound_ctrl:1\n"
            "v_add_u32 v8, %[fb], v8\n v_add_u32 v9, %[fb], v9\n"
            "v_add_u32_dpp v25, v25, v25 row_shr:2 row_mask:0xf bank_mask:0xf bound_ctrl:1\n"
            "v_sub_u32 v26, 8, v22\n v_sub_u32 v27, 8, v23\n"
            "v_add_u32_dpp v25, v25, v25 row_shr:4 row_mask:0xf bank_mask:0xf bound_ctrl:1\n"
            "v_lshl_or_b32 v8, v26, 28, v8\n v_lshl_or_b32 v9, v27, 28, v9\n"
            "v_add_u32_dpp v25, v25, v25 row_shr:8 row_mask:0xf bank_mask:0xf bound_ctrl:1\n"
            "s_nop 1\n"
            "v_readlane_b32 s84, v25, 15\n"
            "v_readlane_b32 s85, v25, 31\n"
            "v_readlane_b32 s86, v25, 47\n"
            "v_readlane_b32 s87, v25, 63\n"
            "v_sub_u32 v25, v25, v24\n"
            "s_mov_b32 s96, 0\n s_mov_b64 s[76:77], 0\n"
            "s_add_u32 s84, s84, 0x70007\n"
            "s_bfe_u32 s88, s84, 0xd0003\n s_lshr_b32 s89, s84, 19\n"
            "s_max_u32 s88, s88, 8\n s_max_u32 s89, s89, 8\n"
            "s_add_u32 s90, s96, s88\n"
            "s_pack_ll_b32_b16 s91, s96, s90\n"
            "s_add_u32 s96, s90, s89\n"
            "s_lshl_b32 s89, s89, 8\n s_or_b32 s88, s88, s89\n s_and_b32 s88, s88, 0xffff\n"
            "s_or_b32 s76, s76, s88\n"
            "s_lshl_b32 s91, s91, 3\n"
            "v_mov_b32 v26, s91\n"
            "s_add_u32 s85, s85, 0x70007\n"
            "s_bfe_u32 s88, s85, 0xd0003\n s_lshr_b32 s89, s85, 19\n"
            "s_max_u32 s88, s88, 8\n s_max_u32 s89, s89, 8\n"
            "s_add_u32 s90, s96, s88\n"
            "s_pack_ll_b32_b16 s91, s96, s90\n"
            "s_add_u32 s96, s90, s89\n"
            "s_lshl_b32 s89, s89, 8\n s_or_b32 s88, s88, s89\n s_and_b32 s88, s88, 0xffff\n"
            "s_lshl_b32 s88, s88, 16\n"
            "s_or_b32 s76, s76, s88\n"
            "s_lshl_b32 s91, s91, 3\n"
            "s_mov_b32 exec_lo, 0xffff0000\n s_mov_b32 exec_hi, 0xffffffff\n"
            "v_mov_b32 v26, s91\n"
            "s_add_u32 s86, s86, 0x70007\n"
            "s_bfe_u32 s88, s86, 0xd0003\n s_lshr_b32 s89, s86, 19\n"
            "s_max_u32 s88, s88, 8\n s_max_u32 s89, s89, 8\n"
            "s_add_u32 s90, s96, s88\n"
            "s_pack_ll_b32_b16 s91, s96, s90\n"
            "s_add_u32 s96, s90, s89\n"
            "s_lshl_b32 s89, s89, 8\n s_or_b32 s88, s88, s89\n s_and_b32 s88, s88, 0xffff\n"
            "s_or_b32 s77, s77, s88\n"
            "s_lshl_b32 s91, s91, 3\n"
            "s_mov_b32 exec_lo, 0x0\n s_mov_b32 exec_hi, 0xffffffff\n"
            "v_mov_b32 v26, s91\n"
            "s_add_u32 s87, s87, 0x70007\n"
            "s_bfe_u32 s88, s87, 0xd0003\n s_lshr_b32 s89, s87, 19\n"
            "s_max_u32 s88, s88, 8\n s_max_u32 s89, s89, 8\n"
            "s_add_u32 s90, s96, s88\n"
            "s_pack_ll_b32_b16 s91, s96, s90\n"
            "s_add_u32 s96, s90, s89\n"
            "s_lshl_b32 s89, s89, 8\n s_or_b32 s88, s88, s89\n s_and_b32 s88, s88, 0xffff\n"
            "s_lshl_b32 s88, s88, 16\n"
            "s_or_b32 s77, s77, s88\n"
            "s_lshl_b32 s91, s91, 3\n"
            "s_mov_b32 exec_lo, 0x0\n s_mov_b32 exec_hi, 0xffff0000\n"
            "v_mov_b32 v26, s91\n"
            "s_mov_b64 exec, -1\n"
            "s_mov_b32 %[bad], 0\n"
            "s_cmp_gt_u32 s96, " STR(PLAN_IT) "\n"
            "s_cbranch_scc0 1f\n"
            "s_mov_b32 %[bad], 1\n s_mov_b32 s76, 0x08080808\n s_mov_b32 s77, 0x08080808\n"
            "s_branch Lnoplan%=\n"
            "1:\n"
            "v_add_u32 v25, v25, v26\n"
            "v_and_b32 v26, 0xffff, v25\n v_lshrrev_b32 v27, 16, v25\n"
            "v_lshlrev_b32 v26, 2, v26\n v_lshlrev_b32 v27, 2, v27\n"
            "v_add_u32 v26, %[pb], v26\n v_add_u32 v27, %[pb], v27\n"
            "v_lshl_add_u32 v28, v20, 2, v26\n v_lshl_add_u32 v29, v21, 2, v27\n"
            "v_cmp_ne_u32 vcc, 0, v22\n v_cmp_ne_u32 s[88:89], 0, v23\n"
            "v_mov_b32 v18, %[xb]\n"
            "s_mov_b64 exec, vcc\n ds_write_b32 v28, v8\n s_mov_b64 exec, s[88:89]\n ds_write_b32 v29, v9\n s_mov_b64 exec, -1\n"
            "v_min_u32 v24, v20, v21\n"
            "v_mov_b32 v39, 0x802\n"
            "s_movk_i32 s90, 0x19f8\n"
            "v_cmp_gt_u32 vcc, 4, v24\n"
            "s_cmp_lg_u64 vcc, 0\n s_cbranch_scc0 4f\n"
            "v_cmp_lt_u32 vcc, 0, v20\n v_cmp_lt_u32 s[88:89], 0, v21\n"
            "v_and_b32 v35, 512, v19\n v_add_u32 v18, 2097152, v18\n v_lshrrev_b32 v19, 1, v19\n v_xor_b32 v18, v18, v35\n"
            "v_mul_u32_u24_sdwa v30, v33, v39 dst_sel:DWORD dst_unused:UNUSED_PAD src0_sel:BYTE_3 src1_sel:DWORD\n"
            "v_mul_u32_u24_sdwa v31, v37, v39 dst_sel:DWORD dst_unused:UNUSED_PAD src0_sel:BYTE_3 src1_sel:DWORD\n"
            "v_and_or_b32 v30, v30, s90, v18\n"
            "v_and_or_b32 v31, v31, s90, v18\n"
            "s_or_b64 s[84:85], vcc, s[88:89]\n"
            "s_cbranch_scc0 Lnoplan%=\n"
            "s_mov_b64 exec, vcc\n ds_write_b32 v26, v30 offset:0\n s_mov_b64 exec, s[88:89]\n ds_write_b32 v27, v31 offset:0\n s_mov_b64 exec, -1\n"
            "v_cmp_lt_u32 vcc, 1, v20\n v_cmp_lt_u32 s[88:89], 1, v21\n"
            "v_and_b32 v35, 512, v19\n v_add_u32 v18, 2097152, v18\n v_lshrrev_b32 v19, 1, v19\n v_xor_b32 v18, v18, v35\n"
            "v_mul_u32_u24_sdwa v30, v33, v39 dst_sel:DWORD dst_unused:UNUSED_PAD src0_sel:BYTE_2 src1_sel:DWORD\n"
            "v_mul_u32_u24_sdwa v31, v37, v39 dst_sel:DWORD dst_unused:UNUSED_PAD src0_sel:BYTE_2 src1_sel:DWORD\n"
            "v_and_or_b32 v30, v30, s90, v18\n"
            "v_and_or_b32 v31, v31, s90, v18\n"
            "s_or_b64 s[84:85], vcc, s[88:89]\n"
            "s_cbranch_scc0 Lnoplan%=\n"
            "s_mov_b64 exec, vcc\n ds_write_b32 v26, v30 offset:4\n s_mov_b64 exec, s[88:89]\n ds_write_b32 v27, v31 offset:4\n s_mov_b64 exec, -1\n"
            "v_cmp_lt_u32 vcc, 2, v20\n v_cmp_lt_u32 s[88:89], 2, v21\n"
            "v_and_b32 v35, 512, v19\n v_add_u32 v18, 2097152, v18\n v_lshrrev_b32 v19, 1, v19\n v_xor_b32 v18, v18, v35\n"
            "v_mul_u32_u24_sdwa v30, v33, v39 dst_sel:DWORD dst_unused:UNUSED_PAD src0_sel:BYTE_1 src1_sel:DWORD\n"
            "v_mul_u32_u24_sdwa v31, v37, v39 dst_sel:DWORD dst_unused:UNUSED_PAD src0_sel:BYTE_1 src1_sel:DWORD\n"
            "v_and_or_b32 v30, v30, s90, v18\n"
            "v_and_or_b32 v31, v31, s90, v18\n"
            "s_or_b64 s[84:85], vcc, s[88:89]\n"
            "s_cbranch_scc0 Lnoplan%=\n"
            "s_mov_b64 exec, vcc\n ds_write_b32 v26, v30 offset:8\n s_mov_b64 exec, s[88:89]\n ds_write_b32 v27, v31 offset:8\n s_mov_b64 exec, -1\n"
            "v_cmp_lt_u32 vcc, 3, v20\n v_cmp_lt_u32 s[88:89], 3, v21\n"
            "v_and_b32 v35, 512, v19\n v_add_u32 v18, 2097152, v18\n v_lshrrev_b32 v19, 1, v19\n v_xor_b32 v18, v18, v35\n"
            "v_mul_u32_u24_sdwa v30, v33, v39 dst_sel:DWORD dst_unused:UNUSED_PAD src0_sel:BYTE_0 src1_sel:DWORD\n"
            "v_mul_u32_u24_sdwa v31, v37, v39 dst_sel:DWORD dst_unused:UNUSED_PAD src0_sel:BYTE_0 src1_sel:DWORD\n"
            "v_and_or_b32 v30, v30, s90, v18\n"
            "v_and_or_b32 v31, v31, s90, v18\n"
            "s_or_b64 s[84:85], vcc, s[88:89]\n"
            "s_cbranch_scc0 Lnoplan%=\n"
            "s_mov_b64 exec, vcc\n ds_write_b32 v26, v30 offset:12\n s_mov_b64 exec, s[88:89]\n ds_write_b32 v27, v31 offset:12\n s_mov_b64 exec, -1\n"
            "s_branch 2f\n"
            "4:\n"
            "v_and_b32 v35, 512, v19\n v_add_u32 v18, 2097152, v18\n v_lshrrev_b32 v19, 1, v19\n v_xor_b32 v18, v18, v35\n"
            "v_mul_u32_u24_sdwa v30, v33, v39 dst_sel:DWORD dst_unused:UNUSED_PAD src0_sel:BYTE_3 src1_sel:DWORD\n"
            "v_mul_u32_u24_sdwa v31, v37, v39 dst_sel:DWORD dst_unused:UNUSED_PAD src0_sel:BYTE_3 src1_sel:DWORD\n"
            "v_and_or_b32 v30, v30, s90, v18\n"
            "v_and_or_b32 v31, v31, s90, v18\n"
            "v_and_b32 v35, 512, v19\n v_add_u32 v18, 2097152, v18\n v_lshrrev_b32 v19, 1, v19\n v_xor_b32 v18, v18, v35\n"
            "v_mul_u32_u24_sdwa v28, v33, v39 dst_sel:DWORD dst_unused:UNUSED_PAD src0_sel:BYTE_2 src1_sel:DWORD\n"
            "v_mul_u32_u24_sdwa v29, v37, v39 dst_sel:DWORD dst_unused:UNUSED_PAD src0_sel:BYTE_2 src1_sel:DWORD\n"
            "v_and_or_b32 v28, v28, s90, v18\n"
            "v_and_or_b32 v29, v29, s90, v18\n"
            "ds_write2_b32 v26, v30, v28 offset0:0 offset1:1\n ds_write2_b32 v27, v31, v29 offset0:0 offset1:1\n"
            "v_and_b32 v35, 512, v19\n v_add_u32 v18, 2097152, v18\n v_lshrrev_b32 v19, 1, v19\n v_xor_b32 v18, v18, v35\n"
            "v_mul_u32_u24_sdwa v30, v33, v39 dst_sel:DWORD dst_unused:UNUSED_PAD src0_sel:BYTE_1 src1_sel:DWORD\n"
            "v_mul_u32_u24_sdwa v31, v37, v39 dst_sel:DWORD dst_unused:UNUSED_PAD src0_sel:BYTE_1 src1_sel:DWORD\n"
            "v_and_or_b32 v30, v30, s90, v18\n"
            "v_and_or_b32 v31, v31, s90, v18\n"
            "v_and_b32 v35, 512, v19\n v_add_u32 v18, 2097152, v18\n v_lshrrev_b32 v19, 1, v19\n v_xor_b32 v18, v18, v35\n"
            "v_mul_u32_u24_sdwa v28, v33, v39 dst_sel:DWORD dst_unused:UNUSED_PAD src0_sel:BYTE_0 src1_sel:DWORD\n"
            "v_mul_u32_u24_sdwa v29, v37, v39 dst_sel:DWORD dst_unused:UNUSED_PAD src0_sel:BYTE_0 src1_sel:DWORD\n"
            "v_and_or_b32 v28, v28, s90, v18\n"
            "v_and_or_b32 v29, v29, s90, v18\n"
            "ds_write2_b32 v26, v30, v28 offset0:2 offset1:3\n ds_write2_b32 v27, v31, v29 offset0:2 offset1:3\n"
            "2:\n"
            "v_cmp_lt_u32 vcc, 4, v20\n v_cmp_lt_u32 s[88:89], 4, v21\n"
            "v_and_b32 v35, 512, v19\n v_add_u32 v18, 2097152, v18\n v_lshrrev_b32 v19, 1, v19\n v_xor_b32 v18, v18, v35\n"
            "v_mul_u32_u24_sdwa v30, v34, v39 dst_sel:DWORD dst_unused:UNUSED_PAD src0_sel:BYTE_3 src1_sel:DWORD\n"
            "v_mul_u32_u24_sdwa v31, v38, v39 dst_sel:DWORD dst_unused:UNUSED_PAD src0_sel:BYTE_3 src1_sel:DWORD\n"
            "v_and_or_b32 v30, v30, s90, v18\n"
            "v_and_or_b32 v31, v31, s90, v18\n"
            "s_or_b64 s[84:85], vcc, s[88:89]\n"
            "s_cbranch_scc0 Lnoplan%=\n"
            "s_mov_b64 exec, vcc\n ds_write_b32 v26, v30 offset:16\n s_mov_b64 exec, s[88:89]\n ds_write_b32 v27, v31 offset:16\n s_mov_b64 exec, -1\n"
            "v_cmp_lt_u32 vcc, 5, v20\n v_cmp_lt_u32 s[88:89], 5, v21\n"
            "v_and_b32 v35, 512, v19\n v_add_u32 v18, 2097152, v18\n v_lshrrev_b32 v19, 1, v19\n v_xor_b32 v18, v18, v35\n"
            "v_mul_u32_u24_sdwa v30, v34, v39 dst_sel:DWORD dst_unused:UNUSED_PAD src0_sel:BYTE_2 src1_sel:DWORD\n"
            "v_mul_u32_u24_sdwa v31, v38, v39 dst_sel:DWORD dst_unused:UNUSED_PAD src0_sel:BYTE_2 src1_sel:DWORD\n"
            "v_and_or_b32 v30, v30, s90, v18\n"
            "v_and_or_b32 v31, v31, s90, v18\n"
            "s_or_b64 s[84:85], vcc, s[88:89]\n"
            "s_cbranch_scc0 Lnoplan%=\n"
            "s_mov_b64 exec, vcc\n ds_write_b32 v26, v30 offset:20\n s_mov_b64 exec, s[88:89]\n ds_write_b32 v27, v31 offset:20\n s_mov_b64 exec, -1\n"
            "v_cmp_lt_u32 vcc, 6, v20\n v_cmp_lt_u32 s[88:89], 6, v21\n"
            "v_and_b32 v35, 512, v19\n v_add_u32 v18, 2097152, v18\n v_lshrrev_b32 v19, 1, v19\n v_xor_b32 v18, v18, v35\n"
            "v_mul_u32_u24_sdwa v30, v34, v39 dst_sel:DWORD dst_unused:UNUSED_PAD src0_sel:BYTE_1 src1_sel:DWORD\n"
            "v_mul_u32_u24_sdwa v31, v38, v39 dst_sel:DWORD dst_unused:UNUSED_PAD src0_sel:BYTE_1 src1_sel:DWORD\n"
            "v_and_or_b32 v30, v30, s90, v18\n"
            "v_and_or_b32 v31, v31, s90, v18\n"
            "s_or_b64 s[84:85], vcc, s[88:89]\n"
            "s_cbranch_scc0 Lnoplan%=\n"
            "s_mov_b64 exec, vcc\n ds_write_b32 v26, v30 offset:24\n s_mov_b64 exec, s[88:89]\n ds_write_b32 v27, v31 offset:24\n s_mov_b64 exec, -1\n"
            "v_cmp_lt_u32 vcc, 7, v20\n v_cmp_lt_u32 s[88:89], 7, v21\n"
            "v_and_b32 v35, 512, v19\n v_add_u32 v18, 2097152, v18\n v_lshrrev_b32 v19, 1, v19\n v_xor_b32 v18, v18, v35\n"
            "v_mul_u32_u24_sdwa v30, v34, v39 dst_sel:DWORD dst_unused:UNUSED_PAD src0_sel:BYTE_0 src1_sel:DWORD\n"
            "v_mul_u32_u24_sdwa v31, v38, v39 dst_sel:DWORD dst_unused:UNUSED_PAD src0_sel:BYTE_0 src1_sel:DWORD\n"
            "v_and_or_b32 v30, v30, s90, v18\n"
            "v_and_or_b32 v31, v31, s90, v18\n"
            "s_or_b64 s[84:85], vcc, s[88:89]\n"
            "s_cbranch_scc0 Lnoplan%=\n"
            "s_mov_b64 exec, vcc\n ds_write_b32 v26, v30 offset:28\n s_mov_b64 exec, s[88:89]\n ds_write_b32 v27, v31 offset:28\n s_mov_b64 exec, -1\n"
            "s_mov_b32 s90, 8\n"
            "v_or_b32 v18, v18, %[hm]\n"
            "3:\n"
            "v_cmp_lt_u32 vcc, s90, v20\n v_cmp_lt_u32 s[88:89], s90, v21\n"
            "v_add_u32 v18, 2097152, v18\n"
            "s_add_u32 s90, s90, 1\n"
            "v_xor_b32 v31, 64, v18\n"
            "s_or_b64 s[84:85], vcc, s[88:89]\n"
            "s_cbranch_scc0 Lnoplan%=\n"
            "s_mov_b64 exec, vcc\n ds_write_b32 v26, v18 offset:32\n s_mov_b64 exec, s[88:89]\n ds_write_b32 v27, v31 offset:32\n s_mov_b64 exec, -1\n"
            "v_add_u32 v26, 4, v26\n v_add_u32 v27, 4, v27\n"
            "s_branch 3b\n"
            "Lnoplan%=:\n"
            "ds_read_b32 v60, v17 offset:0\n"
            "ds_read_b32 v61, v17 offset:256\n"
            "ds_read_b32 v62, v17 offset:512\n"
            "ds_read_b32 v63, v17 offset:768\n"
            "ds_read_b32 v64, v17 offset:1024\n"
            "ds_read_b32 v65, v17 offset:1280\n"
            "ds_read_b32 v66, v17 offset:1536\n"
            "ds_read_b32 v67, v17 offset:1792\n"
            "ds_read_b32 v68, v17 offset:2048\n"
            "ds_read_b32 v69, v17 offset:2304\n"
            "ds_read_b32 v70, v17 offset:2560\n"
            "ds_read_b32 v71, v17 offset:2816\n"
            "ds_read_b32 v72, v17 offset:3072\n"
            "ds_read_b32 v73, v17 offset:3328\n"
            "ds_read_b32 v74, v17 offset:3584\n"
            "ds_read_b32 v75, v17 offset:3840\n"
            "ds_read_b32 v76, v17 offset:4096\n"
            "ds_read_b32 v77, v17 offset:4352\n"
            "ds_read_b32 v78, v17 offset:4608\n"
            "ds_read_b32 v79, v17 offset:4864\n"
            "v_mov_b32 v100, 0\n"
            "v_mov_b32 v101, 0\n"
            "v_mov_b32 v102, 0\n"
            "v_mov_b32 v103, 0\n"
            "v_mov_b32 v107, v11\n"
            "s_mov_b32 s82, 0\n"
            "s_waitcnt lgkmcnt(15)\n"
            "v_mov_b32 v81, v60\n"
            "s_nop 1\n"
            "v_add_u32_dpp v111, v81, v82 row_newbcast:0 row_mask:0xf bank_mask:0x3\n"
            "v_add_u32_dpp v111, v81, v82 row_newbcast:8 row_mask:0xf bank_mask:0xc\n"
            "v_and_b32 v111, " IDXMASK ", v111\n"
            "v_min_u32 v111, v111, v83\n"
            "buffer_load_dwordx4 v[8:11], v111, %[rs], 0 idxen " RAUX "\n"
            "v_add_u32_dpp v111, v81, v82 row_newbcast:1 row_mask:0xf bank_mask:0x3\n"
            "v_add_u32_dpp v111, v81, v82 row_newbcast:9 row_mask:0xf bank_mask:0xc\n"
            "v_and_b32 v111, " IDXMASK ", v111\n"
            "v_min_u32 v111, v111, v83\n"
            "buffer_load_dwordx4 v[12:15], v111, %[rs], 0 idxen " RAUX "\n"
            "v_add_u32_dpp v111, v81, v82 row_newbcast:2 row_mask:0xf bank_mask:0x3\n"
            "v_add_u32_dpp v111, v81, v82 row_newbcast:10 row_mask:0xf bank_mask:0xc\n"
            "v_and_b32 v111, " IDXMASK ", v111\n"
            "v_min_u32 v111, v111, v83\n"
            "buffer_load_dwordx4 v[16:19], v111, %[rs], 0 idxen " RAUX "\n"
            "v_add_u32_dpp v111, v81, v82 row_newbcast:3 row_mask:0xf bank_mask:0x3\n"
            "v_add_u32_dpp v111, v81, v82 row_newbcast:11 row_mask:0xf bank_mask:0xc\n"
            "v_and_b32 v111, " IDXMASK ", v111\n"
            "v_min_u32 v111, v111, v83\n"
            "buffer_load_dwordx4 v[20:23], v111, %[rs], 0 idxen " RAUX "\n"
            "v_add_u32_dpp v111, v81, v82 row_newbcast:4 row_mask:0xf bank_mask:0x3\n"
            "v_add_u32_dpp v111, v81, v82 row_newbcast:12 row_mask:0xf bank_mask:0xc\n"
            "v_and_b32 v111, " IDXMASK ", v111\n"
            "v_min_u32 v111, v111, v83\n"
            "buffer_load_dwordx4 v[24:27], v111, %[rs], 0 idxen " RAUX "\n"
            "v_add_u32_dpp v111, v81, v82 row_newbcast:5 row_mask:0xf bank_mask:0x3\n"
            "v_add_u32_dpp v111, v81, v82 row_newbcast:13 row_mask:0xf bank_mask:0xc\n"
            "v_and_b32 v111, " IDXMASK ", v111\n"
            "v_min_u32 v111, v111, v83\n"
            "buffer_load_dwordx4 v[28:31], v111, %[rs], 0 idxen " RAUX "\n"
            "v_add_u32_dpp v111, v81, v82 row_newbcast:6 row_mask:0xf bank_mask:0x3\n"
            "v_add_u32_dpp v111, v81, v82 row_newbcast:14 row_mask:0xf bank_mask:0xc\n"
            "v_and_b32 v111, " IDXMASK ", v111\n"
            "v_min_u32 v111, v111, v83\n"
            "buffer_load_dwordx4 v[32:35], v111, %[rs], 0 idxen " RAUX "\n"
            "v_add_u32_dpp v111, v81, v82 row_newbcast:7 row_mask:0xf bank_mask:0x3\n"
            "v_add_u32_dpp v111, v81, v82 row_newbcast:15 row_mask:0xf bank_mask:0xc\n"
            "v_and_b32 v111, " IDXMASK ", v111\n"
            "v_min_u32 v111, v111, v83\n"
            "buffer_load_dwordx4 v[36:39], v111, %[rs], 0 idxen " RAUX "\n"
            "s_min_u32 s82, s82, 19\n"
            "s_add_u32 s82, s82, 1\n"
            "s_set_gpr_idx_on s82, gpr_idx(SRC0)\n"
            "s_nop 0\n"
            "v_mov_b32 v81, v60\n"
            "s_set_gpr_idx_off\n"
            "s_setprio 0\n"
            "ds_write_b128 v107, v[100:103] offset:0\n"
            "ds_write_b128 v107, v[100:103] offset:1024\n"
            "ds_write_b128 v107, v[100:103] offset:2048\n"
            "ds_write_b128 v107, v[100:103] offset:3072\n"
            "ds_write_b128 v107, v[100:103] offset:4096\n"
            "s_waitcnt vmcnt(13)\n"
            "v_pk_max_u16 v92, v92, v94\n v_pk_max_u16 v96, v96, v98\n"
            "v_lshlrev_b32 v104, 16, v93\n v_lshlrev_b32 v105, 16, v95\n v_lshlrev_b32 v106, 16, v97\n v_lshlrev_b32 v108, 16, v99\n"
            "v_pk_max_u16 v92, v92, v96\n"
            "v_add_f32 v104, v104, v105\n v_add_f32 v106, v106, v108\n v_add_f32 v93, v93, v95\n v_add_f32 v97, v97, v99\n"
            "v_and_b32 v94, 0xffff, v92\n v_lshrrev_b32 v92, 16, v92\n"
            "v_add_f32 v104, v104, v106\n v_add_f32 v93, v93, v97\n"
            "s_nop 0\n"
            "v_max_u32_dpp v94, v94, v94 row_shr:1 row_mask:0xf bank_mask:0xf bound_ctrl:1\n"
            "v_max_u32_dpp v92, v92, v92 row_shr:1 row_mask:0xf bank_mask:0xf bound_ctrl:1\n"
            "v_add_f32_dpp v104, v104, v104 row_shr:1 row_mask:0xf bank_mask:0xf bound_ctrl:1\n"
            "v_add_f32_dpp v93, v93, v93 row_shr:1 row_mask:0xf bank_mask:0xf bound_ctrl:1\n"
            "v_max_u32_dpp v94, v94, v94 row_shr:2 row_mask:0xf bank_mask:0xf bound_ctrl:1\n"
            "v_max_u32_dpp v92, v92, v92 row_shr:2 row_mask:0xf bank_mask:0xf bound_ctrl:1\n"
            "v_add_f32_dpp v104, v104, v104 row_shr:2 row_mask:0xf bank_mask:0xf bound_ctrl:1\n"
            "v_add_f32_dpp v93, v93, v93 row_shr:2 row_mask:0xf bank_mask:0xf bound_ctrl:1\n"
            "v_max_u32_dpp v94, v94, v94 row_shr:4 row_mask:0xf bank_mask:0xf bound_ctrl:1\n"
            "v_max_u32_dpp v92, v92, v92 row_shr:4 row_mask:0xf bank_mask:0xf bound_ctrl:1\n"
            "v_add_f32_dpp v104, v104, v104 row_shr:4 row_mask:0xf bank_mask:0xf bound_ctrl:1\n"
            "v_add_f32_dpp v93, v93, v93 row_shr:4 row_mask:0xf bank_mask:0xf bound_ctrl:1\n"
            "v_max_u32_dpp v94, v94, v94 row_shr:8 row_mask:0xf bank_mask:0xf bound_ctrl:1\n"
            "v_max_u32_dpp v92, v92, v92 row_shr:8 row_mask:0xf bank_mask:0xf bound_ctrl:1\n"
            "v_add_f32_dpp v104, v104, v104 row_shr:8 row_mask:0xf bank_mask:0xf bound_ctrl:1\n"
            "v_add_f32_dpp v93, v93, v93 row_shr:8 row_mask:0xf bank_mask:0xf bound_ctrl:1\n"
            "v_max_u32_dpp v94, v94, v94 row_bcast:15 row_mask:0xa bank_mask:0xf\n"
            "v_max_u32_dpp v92, v92, v92 row_bcast:15 row_mask:0xa bank_mask:0xf\n"
            "v_add_f32_dpp v104, v104, v104 row_bcast:15 row_mask:0xa bank_mask:0xf\n"
            "v_add_f32_dpp v93, v93, v93 row_bcast:15 row_mask:0xa bank_mask:0xf\n"
            "v_max_u32_dpp v94, v94, v94 row_bcast:31 row_mask:0xc bank_mask:0xf\n"
            "v_max_u32_dpp v92, v92, v92 row_bcast:31 row_mask:0xc bank_mask:0xf\n"
            "v_add_f32_dpp v104, v104, v104 row_bcast:31 row_mask:0xc bank_mask:0xf\n"
            "v_add_f32_dpp v93, v93, v93 row_bcast:31 row_mask:0xc bank_mask:0xf\n"
            "s_nop 1\n"
            "v_readlane_b32 s84, v94, 63\n v_readlane_b32 s85, v92, 63\n v_readlane_b32 s86, v104, 63\n v_readlane_b32 s87, v93, 63\n"
            "s_mov_b32 %[mode], 0\n"
            "s_max_u32 s96, s84, s85\n s_cmp_ge_u32 s96, 0x7f00\n s_cselect_b32 %[mode], 2, 0\n"
            "s_cmp_eq_u32 s84, 0\n s_cselect_b32 s84, 0x3f80, s84\n s_cselect_b32 s86, 0x7f000000, s86\n"
            "s_cmp_eq_u32 s85, 0\n s_cselect_b32 s85, 0x3f80, s85\n s_cselect_b32 s87, 0x7f000000, s87\n"
            "s_min_u32 s96, s84, s85\n s_cmp_lt_u32 s96, 0x800\n s_cselect_b32 %[mode], 2, %[mode]\n"
            "s_cmp_lg_u32 %[mode], 0\n"
            "s_cbranch_scc1 Lend%=\n"
            "s_lshl_b32 s84, s84, 16\n s_lshl_b32 s85, s85, 16\n"
            "v_mov_b32 v104, s84\n v_mov_b32 v105, s85\n"
            "v_mul_f32 v106, 0x3e82d0e5, v104\n v_mul_f32 v108, 0x3e82d0e5, v105\n"
            "v_mul_f32 v104, v104, v104\n v_mul_f32 v105, v105, v105\n"
            "v_add_u32 v106, 0x7fffff, v106\n v_add_u32 v108, 0x7fffff, v108\n"
            "v_mul_f32 v104, %[c4], v104\n v_mul_f32 v105, %[c4], v105\n"
            "v_and_b32 v106, 0x7f800000, v106\n v_and_b32 v108, 0x7f800000, v108\n"
            "v_cmp_lt_f32 vcc, s86, v104\n v_cmp_lt_f32 s[88:89], s87, v105\n"
            "v_add_u32 v86, 0xc00000, v106\n v_add_u32 v87, 0xc00000, v108\n"
            "v_lshrrev_b32 v106, 23, v106\n v_lshrrev_b32 v108, 15, v108\n"
            "v_or_b32 %[exs], v106, v108\n"
            "s_or_b64 vcc, vcc, s[88:89]\n"
            "s_cselect_b32 %[mode], 1, 0\n"
            "s_cbranch_scc1 Lend%=\n"
            "s_waitcnt vmcnt(8)\n"
            "s_mov_b32 m0, %[wb]\n"
            "s_nop 0\n"
            "ds_write_addtid_b32 v40 offset:0\n"
            "ds_write_addtid_b32 v41 offset:256\n"
            "ds_write_addtid_b32 v42 offset:512\n"
            "ds_write_addtid_b32 v43 offset:768\n"
            "ds_write_addtid_b32 v44 offset:16384\n"
            "ds_write_addtid_b32 v45 offset:16640\n"
            "ds_write_addtid_b32 v46 offset:16896\n"
            "ds_write_addtid_b32 v47 offset:17152\n"
            "ds_write_addtid_b32 v48 offset:32768\n"
            "ds_write_addtid_b32 v49 offset:33024\n"
            "ds_write_addtid_b32 v50 offset:33280\n"
            "ds_write_addtid_b32 v51 offset:33536\n"
            "ds_write_addtid_b32 v52 offset:49152\n"
            "ds_write_addtid_b32 v53 offset:49408\n"
            "ds_write_addtid_b32 v54 offset:49664\n"
            "ds_write_addtid_b32 v55 offset:49920\n"
            "s_add_u32 m0, %[wb], 16384\n"
            "s_nop 0\n"
            "ds_write_addtid_b32 v56 offset:49152\n"
            "ds_write_addtid_b32 v57 offset:49408\n"
            "ds_write_addtid_b32 v58 offset:49664\n"
            "ds_write_addtid_b32 v59 offset:49920\n"
            "s_mov_b32 s92, 0\n"
            "s_and_b32 s75, s76, 255\n"
            "s_movk_i32 s74, 1000\n"
            "s_sub_i32 s79, s75, %[j0]\n"
            "s_max_i32 s79, s79, 8\n"
            "s_cmp_eq_u32 s92, 7\n s_cselect_b32 s79, 8, s79\n"
            "s_setprio 3\n"
            "s_waitcnt lgkmcnt(0)\n"
            "s_barrier\n"
            "s_waitcnt vmcnt(7)\n"
            "v_and_b32 v104, 0x1fffc, v8\n"
            "v_and_b32 v105, 0x1fffc, v9\n"
            "v_and_b32 v106, 0x1fffc, v10\n"
            "ds_read_b32 v93, v104\n"
            "ds_read_b32 v95, v105\n"
            "ds_read_b32 v97, v106\n"
            "s_waitcnt lgkmcnt(0)\n"
            "Ls0_%=:\n"
            "s_cmp_gt_i32 s75, s74\n"
            "s_cbranch_scc1 1f\n"
            "s_waitcnt vmcnt(6)\n"
            "1:\n"
            "s_waitcnt vmcnt(11)\n"
            "s_cmp_eq_u32 s75, s79\n"
            "s_cbranch_scc1 Le0_%=\n"
            "Lc0_%=:\n"
            "v_and_b32 v104, 0x1fffc, v12\n"
            "v_and_b32 v105, 0x1fffc, v13\n"
            "v_and_b32 v106, 0x1fffc, v14\n"
            "ds_read_b32 v99, v104\n"
            "ds_read_b32 v101, v105\n"
            "ds_read_b32 v103, v106\n"
            "v_add_u32_dpp v111, v81, v82 row_newbcast:0 row_mask:0xf bank_mask:0x3\n"
            "v_add_u32_dpp v111, v81, v82 row_newbcast:8 row_mask:0xf bank_mask:0xc\n"
            "v_and_b32 v108, 0x7fe, v11\n"
            "v_and_b32 v109, 0x1ff800, v11\n"
            "v_lshrrev_b32 v110, 22, v11\n"
            "v_lshrrev_b32 v112, 15, v8\n"
            "v_lshrrev_b32 v113, 15, v9\n"
            "v_lshrrev_b32 v114, 15, v10\n"
            "v_and_b32 v111, " IDXMASK ", v111\n"
            "v_min_u32 v111, v111, v83\n"
            "buffer_load_dwordx4 v[8:11], v111, %[rs], 0 idxen " RAUX "\n"
            "v_fmamk_f32 v108, v108, 0x7e800000, v84\n"
            "v_fmamk_f32 v109, v109, 0x79800000, v84\n"
            "v_fmamk_f32 v110, v110, 0x7f000000, v84\n"
            "v_or_b32 v112, 3, v112\n"
            "v_or_b32 v113, 3, v113\n"
            "v_or_b32 v114, 3, v114\n"
            "s_sub_u32 s75, s75, 1\n"
            "s_waitcnt lgkmcnt(6)\n"
            "v_lshlrev_b32 v92, 16, v93\n"
            "v_lshlrev_b32 v94, 16, v95\n"
            "v_lshlrev_b32 v96, 16, v97\n"
            "v_pk_fma_f32 v[92:93], v[92:93], v[108:109], v[86:87] op_sel:[0,0,0] op_sel_hi:[1,0,1]\n"
            "v_pk_fma_f32 v[94:95], v[94:95], v[108:109], v[86:87] op_sel:[0,1,0] op_sel_hi:[1,1,1]\n"
            "v_pk_fma_f32 v[96:97], v[96:97], v[110:111], v[86:87] op_sel:[0,0,0] op_sel_hi:[1,0,1]\n"
            "v_lshl_add_u32 v92, v93, 16, v92\n"
            "v_lshl_add_u32 v94, v95, 16, v94\n"
            "v_lshl_add_u32 v96, v97, 16, v96\n"
            "v_sub_u32 v92, v92, v86\n"
            "v_mad_i32_i16 v88, v93, v112, v88\n"
            "ds_add_u32 v112, v92 offset:65533\n"
            "v_sub_u32 v94, v94, v86\n"
            "v_mad_i32_i16 v88, v95, v113, v88\n"
            "ds_add_u32 v113, v94 offset:65533\n"
            "v_sub_u32 v96, v96, v86\n"
            "v_mad_i32_i16 v88, v97, v114, v88\n"
            "ds_add_u32 v114, v96 offset:65533\n"
            "Ls1_%=:\n"
            "s_cmp_gt_i32 s75, s74\n"
            "s_cbranch_scc1 1f\n"
            "s_waitcnt vmcnt(6)\n"
            "1:\n"
            "s_waitcnt vmcnt(11)\n"
            "s_cmp_eq_u32 s75, s79\n"
            "s_cbranch_scc1 Le1_%=\n"
            "Lc1_%=:\n"
            "v_and_b32 v104, 0x1fffc, v16\n"
            "v_and_b32 v105, 0x1fffc, v17\n"
            "v_and_b32 v106, 0x1fffc, v18\n"
            "ds_read_b32 v93, v104\n"
            "ds_read_b32 v95, v105\n"
            "ds_read_b32 v97, v106\n"
            "v_add_u32_dpp v111, v81, v82 row_newbcast:1 row_mask:0xf bank_mask:0x3\n"
            "v_add_u32_dpp v111, v81, v82 row_newbcast:9 row_mask:0xf bank_mask:0xc\n"
            "v_and_b32 v108, 0x7fe, v15\n"
            "v_and_b32 v109, 0x1ff800, v15\n"
            "v_lshrrev_b32 v110, 22, v15\n"
            "v_lshrrev_b32 v112, 15, v12\n"
            "v_lshrrev_b32 v113, 15, v13\n"
            "v_lshrrev_b32 v114, 15, v14\n"
            "v_and_b32 v111, " IDXMASK ", v111\n"
            "v_min_u32 v111, v111, v83\n"
            "buffer_load_dwordx4 v[12:15], v111, %[rs], 0 idxen " RAUX "\n"
            "v_fmamk_f32 v108, v108, 0x7e800000, v84\n"
            "v_fmamk_f32 v109, v109, 0x79800000, v84\n"
            "v_fmamk_f32 v110, v110, 0x7f000000, v84\n"
            "v_or_b32 v112, 3, v112\n"
            "v_or_b32 v113, 3, v113\n"
            "v_or_b32 v114, 3, v114\n"
            "s_sub_u32 s75, s75, 1\n"
            "s_waitcnt lgkmcnt(6)\n"
            "v_lshlrev_b32 v98, 16, v99\n"
            "v_lshlrev_b32 v100, 16, v101\n"
            "v_lshlrev_b32 v102, 16, v103\n"
            "v_pk_fma_f32 v[98:99], v[98:99], v[108:109], v[86:87] op_sel:[0,0,0] op_sel_hi:[1,0,1]\n"
            "v_pk_fma_f32 v[100:101], v[100:101], v[108:109], v[86:87] op_sel:[0,1,0] op_sel_hi:[1,1,1]\n"
            "v_pk_fma_f32 v[102:103], v[102:103], v[110:111], v[86:87] op_sel:[0,0,0] op_sel_hi:[1,0,1]\n"
            "v_lshl_add_u32 v98, v99, 16, v98\n"
            "v_lshl_add_u32 v100, v101, 16, v100\n"
            "v_lshl_add_u32 v102, v103, 16, v102\n"
            "v_sub_u32 v98, v98, v86\n"
            "v_mad_i32_i16 v88, v99, v112, v88\n"
            "ds_add_u32 v112, v98 offset:65533\n"
            "v_sub_u32 v100, v100, v86\n"
            "v_mad_i32_i16 v88, v101, v113, v88\n"
            "ds_add_u32 v113, v100 offset:65533\n"
            "v_sub_u32 v102, v102, v86\n"
            "v_mad_i32_i16 v88, v103, v114, v88\n"
            "ds_add_u32 v114, v102 offset:65533\n"
            "Ls2_%=:\n"
            "s_cmp_gt_i32 s75, s74\n"
            "s_cbranch_scc1 1f\n"
            "s_waitcnt vmcnt(6)\n"
            "1:\n"
            "s_waitcnt vmcnt(11)\n"
            "s_cmp_eq_u32 s75, s79\n"
            "s_cbranch_scc1 Le2_%=\n"
            "Lc2_%=:\n"
            "v_and_b32 v104, 0x1fffc, v20\n"
            "v_and_b32 v105, 0x1fffc, v21\n"
            "v_and_b32 v106, 0x1fffc, v22\n"
            "ds_read_b32 v99, v104\n"
            "ds_read_b32 v101, v105\n"
            "ds_read_b32 v103, v106\n"
            "v_add_u32_dpp v111, v81, v82 row_newbcast:2 row_mask:0xf bank_mask:0x3\n"
            "v_add_u32_dpp v111, v81, v82 row_newbcast:10 row_mask:0xf bank_mask:0xc\n"
            "v_and_b32 v108, 0x7fe, v19\n"
            "v_and_b32 v109, 0x1ff800, v19\n"
            "v_lshrrev_b32 v110, 22, v19\n"
            "v_lshrrev_b32 v112, 15, v16\n"
            "v_lshrrev_b32 v113, 15, v17\n"
            "v_lshrrev_b32 v114, 15, v18\n"
            "v_and_b32 v111, " IDXMASK ", v111\n"
            "v_min_u32 v111, v111, v83\n"
            "buffer_load_dwordx4 v[16:19], v111, %[rs], 0 idxen " RAUX "\n"
            "v_fmamk_f32 v108, v108, 0x7e800000, v84\n"
            "v_fmamk_f32 v109, v109, 0x79800000, v84\n"
            "v_fmamk_f32 v110, v110, 0x7f000000, v84\n"
            "v_or_b32 v112, 3, v112\n"
            "v_or_b32 v113, 3, v113\n"
            "v_or_b32 v114, 3, v114\n"
            "s_sub_u32 s75, s75, 1\n"
            "s_waitcnt lgkmcnt(6)\n"
            "v_lshlrev_b32 v92, 16, v93\n"
            "v_lshlrev_b32 v94, 16, v95\n"
            "v_lshlrev_b32 v96, 16, v97\n"
            "v_pk_fma_f32 v[92:93], v[92:93], v[108:109], v[86:87] op_sel:[0,0,0] op_sel_hi:[1,0,1]\n"
            "v_pk_fma_f32 v[94:95], v[94:95], v[108:109], v[86:87] op_sel:[0,1,0] op_sel_hi:[1,1,1]\n"
            "v_pk_fma_f32 v[96:97], v[96:97], v[110:111], v[86:87] op_sel:[0,0,0] op_sel_hi:[1,0,1]\n"
            "v_lshl_add_u32 v92, v93, 16, v92\n"
            "v_lshl_add_u32 v94, v95, 16, v94\n"
            "v_lshl_add_u32 v96, v97, 16, v96\n"
            "v_sub_u32 v92, v92, v86\n"
            "v_mad_i32_i16 v88, v93, v112, v88\n"
            "ds_add_u32 v112, v92 offset:65533\n"
            "v_sub_u32 v94, v94, v86\n"
            "v_mad_i32_i16 v88, v95, v113, v88\n"
            "ds_add_u32 v113, v94 offset:65533\n"
            "v_sub_u32 v96, v96, v86\n"
            "v_mad_i32_i16 v88, v97, v114, v88\n"
            "ds_add_u32 v114, v96 offset:65533\n"
            "Ls3_%=:\n"
            "s_cmp_gt_i32 s75, s74\n"
            "s_cbranch_scc1 1f\n"
            "s_waitcnt vmcnt(6)\n"
            "1:\n"
            "s_waitcnt vmcnt(11)\n"
            "s_cmp_eq_u32 s75, s79\n"
            "s_cbranch_scc1 Le3_%=\n"
            "Lc3_%=:\n"
            "v_and_b32 v104, 0x1fffc, v24\n"
            "v_and_b32 v105, 0x1fffc, v25\n"
            "v_and_b32 v106, 0x1fffc, v26\n"
            "ds_read_b32 v93, v104\n"
            "ds_read_b32 v95, v105\n"
            "ds_read_b32 v97, v106\n"
            "v_add_u32_dpp v111, v81, v82 row_newbcast:3 row_mask:0xf bank_mask:0x3\n"
            "v_add_u32_dpp v111, v81, v82 row_newbcast:11 row_mask:0xf bank_mask:0xc\n"
            "v_and_b32 v108, 0x7fe, v23\n"
            "v_and_b32 v109, 0x1ff800, v23\n"
            "v_lshrrev_b32 v110, 22, v23\n"
            "v_lshrrev_b32 v112, 15, v20\n"
            "v_lshrrev_b32 v113, 15, v21\n"
            "v_lshrrev_b32 v114, 15, v22\n"
            "v_and_b32 v111, " IDXMASK ", v111\n"
            "v_min_u32 v111, v111, v83\n"
            "buffer_load_dwordx4 v[20:23], v111, %[rs], 0 idxen " RAUX "\n"
            "v_fmamk_f32 v108, v108, 0x7e800000, v84\n"
            "v_fmamk_f32 v109, v109, 0x79800000, v84\n"
            "v_fmamk_f32 v110, v110, 0x7f000000, v84\n"
            "v_or_b32 v112, 3, v112\n"
            "v_or_b32 v113, 3, v113\n"
            "v_or_b32 v114, 3, v114\n"
            "s_sub_u32 s75, s75, 1\n"
            "s_waitcnt lgkmcnt(6)\n"
            "v_lshlrev_b32 v98, 16, v99\n"
            "v_lshlrev_b32 v100, 16, v101\n"
            "v_lshlrev_b32 v102, 16, v103\n"
            "v_pk_fma_f32 v[98:99], v[98:99], v[108:109], v[86:87] op_sel:[0,0,0] op_sel_hi:[1,0,1]\n"
            "v_pk_fma_f32 v[100:101], v[100:101], v[108:109], v[86:87] op_sel:[0,1,0] op_sel_hi:[1,1,1]\n"
            "v_pk_fma_f32 v[102:103], v[102:103], v[110:111], v[86:87] op_sel:[0,0,0] op_sel_hi:[1,0,1]\n"
            "v_lshl_add_u32 v98, v99, 16, v98\n"
            "v_lshl_add_u32 v100, v101, 16, v100\n"
            "v_lshl_add_u32 v102, v103, 16, v102\n"
            "v_sub_u32 v98, v98, v86\n"
            "v_mad_i32_i16 v88, v99, v112, v88\n"
            "ds_add_u32 v112, v98 offset:65533\n"
            "v_sub_u32 v100, v100, v86\n"
            "v_mad_i32_i16 v88, v101, v113, v88\n"
            "ds_add_u32 v113, v100 offset:65533\n"
            "v_sub_u32 v102, v102, v86\n"
            "v_mad_i32_i16 v88, v103, v114, v88\n"
            "ds_add_u32 v114, v102 offset:65533\n"
            "Ls4_%=:\n"
            "s_cmp_gt_i32 s75, s74\n"
            "s_cbranch_scc1 1f\n"
            "s_waitcnt vmcnt(6)\n"
            "1:\n"
            "s_waitcnt vmcnt(11)\n"
            "s_cmp_eq_u32 s75, s79\n"
            "s_cbranch_scc1 Le4_%=\n"
            "Lc4_%=:\n"
            "v_and_b32 v104, 0x1fffc, v28\n"
            "v_and_b32 v105, 0x1fffc, v29\n"
            "v_and_b32 v106, 0x1fffc, v30\n"
            "ds_read_b32 v99, v104\n"
            "ds_read_b32 v101, v105\n"
            "ds_read_b32 v103, v106\n"
            "v_add_u32_dpp v111, v81, v82 row_newbcast:4 row_mask:0xf bank_mask:0x3\n"
            "v_add_u32_dpp v111, v81, v82 row_newbcast:12 row_mask:0xf bank_mask:0xc\n"
            "v_and_b32 v108, 0x7fe, v27\n"
            "v_and_b32 v109, 0x1ff800, v27\n"
            "v_lshrrev_b32 v110, 22, v27\n"
            "v_lshrrev_b32 v112, 15, v24\n"
            "v_lshrrev_b32 v113, 15, v25\n"
            "v_lshrrev_b32 v114, 15, v26\n"
            "v_and_b32 v111, " IDXMASK ", v111\n"
            "v_min_u32 v111, v111, v83\n"
            "buffer_load_dwordx4 v[24:27], v111, %[rs], 0 idxen " RAUX "\n"
            "v_fmamk_f32 v108, v108, 0x7e800000, v84\n"
            "v_fmamk_f32 v109, v109, 0x79800000, v84\n"
            "v_fmamk_f32 v110, v110, 0x7f000000, v84\n"
            "v_or_b32 v112, 3, v112\n"
            "v_or_b32 v113, 3, v113\n"
            "v_or_b32 v114, 3, v114\n"
            "s_sub_u32 s75, s75, 1\n"
            "s_waitcnt lgkmcnt(6)\n"
            "v_lshlrev_b32 v92, 16, v93\n"
            "v_lshlrev_b32 v94, 16, v95\n"
            "v_lshlrev_b32 v96, 16, v97\n"
            "v_pk_fma_f32 v[92:93], v[92:93], v[108:109], v[86:87] op_sel:[0,0,0] op_sel_hi:[1,0,1]\n"
            "v_pk_fma_f32 v[94:95], v[94:95], v[108:109], v[86:87] op_sel:[0,1,0] op_sel_hi:[1,1,1]\n"
            "v_pk_fma_f32 v[96:97], v[96:97], v[110:111], v[86:87] op_sel:[0,0,0] op_sel_hi:[1,0,1]\n"
            "v_lshl_add_u32 v92, v93, 16, v92\n"
            "v_lshl_add_u32 v94, v95, 16, v94\n"
            "v_lshl_add_u32 v96, v97, 16, v96\n"
            "v_sub_u32 v92, v92, v86\n"
            "v_mad_i32_i16 v88, v93, v112, v88\n"
            "ds_add_u32 v112, v92 offset:65533\n"
            "v_sub_u32 v94, v94, v86\n"
            "v_mad_i32_i16 v88, v95, v113, v88\n"
            "ds_add_u32 v113, v94 offset:65533\n"
            "v_sub_u32 v96, v96, v86\n"
            "v_mad_i32_i16 v88, v97, v114, v88\n"
            "ds_add_u32 v114, v96 offset:65533\n"
            "Ls5_%=:\n"
            "s_cmp_gt_i32 s75, s74\n"
            "s_cbranch_scc1 1f\n"
            "s_waitcnt vmcnt(6)\n"
            "1:\n"
            "s_waitcnt vmcnt(11)\n"
            "s_cmp_eq_u32 s75, s79\n"
            "s_cbranch_scc1 Le5_%=\n"
            "Lc5_%=:\n"
            "v_and_b32 v104, 0x1fffc, v32\n"
            "v_and_b32 v105, 0x1fffc, v33\n"
            "v_and_b32 v106, 0x1fffc, v34\n"
            "ds_read_b32 v93, v104\n"
            "ds_read_b32 v95, v105\n"
            "ds_read_b32 v97, v106\n"
            "v_add_u32_dpp v111, v81, v82 row_newbcast:5 row_mask:0xf bank_mask:0x3\n"
            "v_add_u32_dpp v111, v81, v82 row_newbcast:13 row_mask:0xf bank_mask:0xc\n"
            "v_and_b32 v108, 0x7fe, v31\n"
            "v_and_b32 v109, 0x1ff800, v31\n"
            "v_lshrrev_b32 v110, 22, v31\n"
            "v_lshrrev_b32 v112, 15, v28\n"
            "v_lshrrev_b32 v113, 15, v29\n"
            "v_lshrrev_b32 v114, 15, v30\n"
            "v_and_b32 v111, " IDXMASK ", v111\n"
            "v_min_u32 v111, v111, v83\n"
            "buffer_load_dwordx4 v[28:31], v111, %[rs], 0 idxen " RAUX "\n"
            "v_fmamk_f32 v108, v108, 0x7e800000, v84\n"
            "v_fmamk_f32 v109, v109, 0x79800000, v84\n"
            "v_fmamk_f32 v110, v110, 0x7f000000, v84\n"
            "v_or_b32 v112, 3, v112\n"
            "v_or_b32 v113, 3, v113\n"
            "v_or_b32 v114, 3, v114\n"
            "s_sub_u32 s75, s75, 1\n"
            "s_waitcnt lgkmcnt(6)\n"
            "v_lshlrev_b32 v98, 16, v99\n"
            "v_lshlrev_b32 v100, 16, v101\n"
            "v_lshlrev_b32 v102, 16, v103\n"
            "v_pk_fma_f32 v[98:99], v[98:99], v[108:109], v[86:87] op_sel:[0,0,0] op_sel_hi:[1,0,1]\n"
            "v_pk_fma_f32 v[100:101], v[100:101], v[108:109], v[86:87] op_sel:[0,1,0] op_sel_hi:[1,1,1]\n"
            "v_pk_fma_f32 v[102:103], v[102:103], v[110:111], v[86:87] op_sel:[0,0,0] op_sel_hi:[1,0,1]\n"
            "v_lshl_add_u32 v98, v99, 16, v98\n"
            "v_lshl_add_u32 v100, v101, 16, v100\n"
            "v_lshl_add_u32 v102, v103, 16, v102\n"
            "v_sub_u32 v98, v98, v86\n"
            "v_mad_i32_i16 v88, v99, v112, v88\n"
            "ds_add_u32 v112, v98 offset:65533\n"
            "v_sub_u32 v100, v100, v86\n"
            "v_mad_i32_i16 v88, v101, v113, v88\n"
            "ds_add_u32 v113, v100 offset:65533\n"
            "v_sub_u32 v102, v102, v86\n"
            "v_mad_i32_i16 v88, v103, v114, v88\n"
            "ds_add_u32 v114, v102 offset:65533\n"
            "Ls6_%=:\n"
            "s_cmp_gt_i32 s75, s74\n"
            "s_cbranch_scc1 1f\n"
            "s_waitcnt vmcnt(6)\n"
            "1:\n"
            "s_waitcnt vmcnt(11)\n"
            "s_cmp_eq_u32 s75, s79\n"
            "s_cbranch_scc1 Le6_%=\n"
            "Lc6_%=:\n"
            "v_and_b32 v104, 0x1fffc, v36\n"
            "v_and_b32 v105, 0x1fffc, v37\n"
            "v_and_b32 v106, 0x1fffc, v38\n"
            "ds_read_b32 v99, v104\n"
            "ds_read_b32 v101, v105\n"
            "ds_read_b32 v103, v106\n"
            "v_add_u32_dpp v111, v81, v82 row_newbcast:6 row_mask:0xf bank_mask:0x3\n"
            "v_add_u32_dpp v111, v81, v82 row_newbcast:14 row_mask:0xf bank_mask:0xc\n"
            "v_and_b32 v108, 0x7fe, v35\n"
            "v_and_b32 v109, 0x1ff800, v35\n"
            "v_lshrrev_b32 v110, 22, v35\n"
            "v_lshrrev_b32 v112, 15, v32\n"
            "v_lshrrev_b32 v113, 15, v33\n"
            "v_lshrrev_b32 v114, 15, v34\n"
            "v_and_b32 v111, " IDXMASK ", v111\n"
            "v_min_u32 v111, v111, v83\n"
            "buffer_load_dwordx4 v[32:35], v111, %[rs], 0 idxen " RAUX "\n"
            "v_fmamk_f32 v108, v108, 0x7e800000, v84\n"
            "v_fmamk_f32 v109, v109, 0x79800000, v84\n"
            "v_fmamk_f32 v110, v110, 0x7f000000, v84\n"
            "v_or_b32 v112, 3, v112\n"
            "v_or_b32 v113, 3, v113\n"
            "v_or_b32 v114, 3, v114\n"
            "s_sub_u32 s75, s75, 1\n"
            "s_waitcnt lgkmcnt(6)\n"
            "v_lshlrev_b32 v92, 16, v93\n"
            "v_lshlrev_b32 v94, 16, v95\n"
            "v_lshlrev_b32 v96, 16, v97\n"
            "v_pk_fma_f32 v[92:93], v[92:93], v[108:109], v[86:87] op_sel:[0,0,0] op_sel_hi:[1,0,1]\n"
            "v_pk_fma_f32 v[94:95], v[94:95], v[108:109], v[86:87] op_sel:[0,1,0] op_sel_hi:[1,1,1]\n"
            "v_pk_fma_f32 v[96:97], v[96:97], v[110:111], v[86:87] op_sel:[0,0,0] op_sel_hi:[1,0,1]\n"
            "v_lshl_add_u32 v92, v93, 16, v92\n"
            "v_lshl_add_u32 v94, v95, 16, v94\n"
            "v_lshl_add_u32 v96, v97, 16, v96\n"
            "v_sub_u32 v92, v92, v86\n"
            "v_mad_i32_i16 v88, v93, v112, v88\n"
            "ds_add_u32 v112, v92 offset:65533\n"
            "v_sub_u32 v94, v94, v86\n"
            "v_mad_i32_i16 v88, v95, v113, v88\n"
            "ds_add_u32 v113, v94 offset:65533\n"
            "v_sub_u32 v96, v96, v86\n"
            "v_mad_i32_i16 v88, v97, v114, v88\n"
            "ds_add_u32 v114, v96 offset:65533\n"
            "Ls7_%=:\n"
            "s_cmp_gt_i32 s75, s74\n"
            "s_cbranch_scc1 1f\n"
            "s_waitcnt vmcnt(6)\n"
            "1:\n"
            "s_waitcnt vmcnt(11)\n"
            "s_cmp_eq_u32 s75, s79\n"
            "s_cbranch_scc1 Le7_%=\n"
            "Lc7_%=:\n"
            "v_and_b32 v104, 0x1fffc, v8\n"
            "v_and_b32 v105, 0x1fffc, v9\n"
            "v_and_b32 v106, 0x1fffc, v10\n"
            "ds_read_b32 v93, v104\n"
            "ds_read_b32 v95, v105\n"
            "ds_read_b32 v97, v106\n"
            "v_add_u32_dpp v111, v81, v82 row_newbcast:7 row_mask:0xf bank_mask:0x3\n"
            "v_add_u32_dpp v111, v81, v82 row_newbcast:15 row_mask:0xf bank_mask:0xc\n"
            "v_and_b32 v108, 0x7fe, v39\n"
            "v_and_b32 v109, 0x1ff800, v39\n"
            "v_lshrrev_b32 v110, 22, v39\n"
            "v_lshrrev_b32 v112, 15, v36\n"
            "v_lshrrev_b32 v113, 15, v37\n"
            "v_lshrrev_b32 v114, 15, v38\n"
            "v_and_b32 v111, " IDXMASK ", v111\n"
            "v_min_u32 v111, v111, v83\n"
            "buffer_load_dwordx4 v[36:39], v111, %[rs], 0 idxen " RAUX "\n"
            "s_min_u32 s82, s82, 19\n"
            "s_add_u32 s82, s82, 1\n"
            "s_set_gpr_idx_on s82, gpr_idx(SRC0)\n"
            "s_nop 0\n"
            "v_mov_b32 v81, v60\n"
            "s_set_gpr_idx_off\n"
            "v_fmamk_f32 v108, v108, 0x7e800000, v84\n"
            "v_fmamk_f32 v109, v109, 0x79800000, v84\n"
            "v_fmamk_f32 v110, v110, 0x7f000000, v84\n"
            "v_or_b32 v112, 3, v112\n"
            "v_or_b32 v113, 3, v113\n"
            "v_or_b32 v114, 3, v114\n"
            "s_sub_u32 s75, s75, 1\n"
            "s_waitcnt lgkmcnt(6)\n"
            "v_lshlrev_b32 v98, 16, v99\n"
            "v_lshlrev_b32 v100, 16, v101\n"
            "v_lshlrev_b32 v102, 16, v103\n"
            "v_pk_fma_f32 v[98:99], v[98:99], v[108:109], v[86:87] op_sel:[0,0,0] op_sel_hi:[1,0,1]\n"
            "v_pk_fma_f32 v[100:101], v[100:101], v[108:109], v[86:87] op_sel:[0,1,0] op_sel_hi:[1,1,1]\n"
            "v_pk_fma_f32 v[102:103], v[102:103], v[110:111], v[86:87] op_sel:[0,0,0] op_sel_hi:[1,0,1]\n"
            "v_lshl_add_u32 v98, v99, 16, v98\n"
            "v_lshl_add_u32 v100, v101, 16, v100\n"
            "v_lshl_add_u32 v102, v103, 16, v102\n"
            "v_sub_u32 v98, v98, v86\n"
            "v_mad_i32_i16 v88, v99, v112, v88\n"
            "ds_add_u32 v112, v98 offset:65533\n"
            "v_sub_u32 v100, v100, v86\n"
            "v_mad_i32_i16 v88, v101, v113, v88\n"
            "ds_add_u32 v113, v100 offset:65533\n"
            "v_sub_u32 v102, v102, v86\n"
            "v_mad_i32_i16 v88, v103, v114, v88\n"
            "ds_add_u32 v114, v102 offset:65533\n"
            "s_branch Ls0_%=\n"
            "Le0_%=:\n"
            "s_cmp_lt_u32 s75, 8\n"
            "s_cbranch_scc1 1f\n"
            "s_swappc_b64 s[80:81], s[66:67]\n"
            "s_branch Lc0_%=\n"
            "1:\n"
            "s_cmp_lg_u32 s75, 7\n"
            "s_cbranch_scc1 1f\n"
            "s_setprio 2\n"
            "s_mov_b32 s79, 3\n"
            "s_branch Lc0_%=\n"
            "1:\n"
            "s_cmp_lg_u32 s75, 3\n"
            "s_cbranch_scc1 1f\n"
            "s_setprio 1\n"
            "s_mov_b32 s79, 1\n"
            "s_branch Lc0_%=\n"
            "1:\n"
            "s_setprio 0\n"
            "v_add_u32_dpp v111, v81, v82 row_newbcast:0 row_mask:0xf bank_mask:0x3\n"
            "v_add_u32_dpp v111, v81, v82 row_newbcast:8 row_mask:0xf bank_mask:0xc\n"
            "v_and_b32 v108, 0x7fe, v11\n"
            "v_and_b32 v109, 0x1ff800, v11\n"
            "v_lshrrev_b32 v110, 22, v11\n"
            "v_lshrrev_b32 v112, 15, v8\n"
            "v_lshrrev_b32 v113, 15, v9\n"
            "v_lshrrev_b32 v114, 15, v10\n"
            "v_and_b32 v111, " IDXMASK ", v111\n"
            "v_min_u32 v111, v111, v83\n"
            "buffer_load_dwordx4 v[8:11], v111, %[rs], 0 idxen " RAUX "\n"
            "v_fmamk_f32 v108, v108, 0x7e800000, v84\n"
            "v_fmamk_f32 v109, v109, 0x79800000, v84\n"
            "v_fmamk_f32 v110, v110, 0x7f000000, v84\n"
            "v_or_b32 v112, 3, v112\n"
            "v_or_b32 v113, 3, v113\n"
            "v_or_b32 v114, 3, v114\n"
            "s_waitcnt lgkmcnt(3)\n"
            "s_swappc_b64 s[80:81], s[70:71]\n"
            "v_lshlrev_b32 v92, 16, v93\n"
            "v_lshlrev_b32 v94, 16, v95\n"
            "v_lshlrev_b32 v96, 16, v97\n"
            "v_pk_fma_f32 v[92:93], v[92:93], v[108:109], v[86:87] op_sel:[0,0,0] op_sel_hi:[1,0,1]\n"
            "v_pk_fma_f32 v[94:95], v[94:95], v[108:109], v[86:87] op_sel:[0,1,0] op_sel_hi:[1,1,1]\n"
            "v_pk_fma_f32 v[96:97], v[96:97], v[110:111], v[86:87] op_sel:[0,0,0] op_sel_hi:[1,0,1]\n"
            "v_lshl_add_u32 v92, v93, 16, v92\n"
            "v_lshl_add_u32 v94, v95, 16, v94\n"
            "v_lshl_add_u32 v96, v97, 16, v96\n"
            "v_sub_u32 v92, v92, v86\n"
            "v_mad_i32_i16 v88, v93, v112, v88\n"
            "ds_add_u32 v112, v92 offset:65533\n"
            "v_sub_u32 v94, v94, v86\n"
            "v_mad_i32_i16 v88, v95, v113, v88\n"
            "ds_add_u32 v113, v94 offset:65533\n"
            "v_sub_u32 v96, v96, v86\n"
            "v_mad_i32_i16 v88, v97, v114, v88\n"
            "ds_add_u32 v114, v96 offset:65533\n"
            "v_and_b32 v104, 0x1fffc, v12\n"
            "v_and_b32 v105, 0x1fffc, v13\n"
            "v_and_b32 v106, 0x1fffc, v14\n"
            "s_swappc_b64 s[80:81], s[72:73]\n"
            "ds_read_b32 v99, v104\n"
            "ds_read_b32 v101, v105\n"
            "ds_read_b32 v103, v106\n"
            "s_waitcnt lgkmcnt(0)\n"
            "s_branch Ls1_%=\n"
            "Le1_%=:\n"
            "s_cmp_lt_u32 s75, 8\n"
            "s_cbranch_scc1 1f\n"
            "s_swappc_b64 s[80:81], s[66:67]\n"
            "s_branch Lc1_%=\n"
            "1:\n"
            "s_cmp_lg_u32 s75, 7\n"
            "s_cbranch_scc1 1f\n"
            "s_setprio 2\n"
            "s_mov_b32 s79, 3\n"
            "s_branch Lc1_%=\n"
            "1:\n"
            "s_cmp_lg_u32 s75, 3\n"
            "s_cbranch_scc1 1f\n"
            "s_setprio 1\n"
            "s_mov_b32 s79, 1\n"
            "s_branch Lc1_%=\n"
            "1:\n"
            "s_setprio 0\n"
            "v_add_u32_dpp v111, v81, v82 row_newbcast:1 row_mask:0xf bank_mask:0x3\n"
            "v_add_u32_dpp v111, v81, v82 row_newbcast:9 row_mask:0xf bank_mask:0xc\n"
            "v_and_b32 v108, 0x7fe, v15\n"
            "v_and_b32 v109, 0x1ff800, v15\n"
            "v_lshrrev_b32 v110, 22, v15\n"
            "v_lshrrev_b32 v112, 15, v12\n"
            "v_lshrrev_b32 v113, 15, v13\n"
            "v_lshrrev_b32 v114, 15, v14\n"
            "v_and_b32 v111, " IDXMASK ", v111\n"
            "v_min_u32 v111, v111, v83\n"
            "buffer_load_dwordx4 v[12:15], v111, %[rs], 0 idxen " RAUX "\n"
            "v_fmamk_f32 v108, v108, 0x7e800000, v84\n"
            "v_fmamk_f32 v109, v109, 0x79800000, v84\n"
            "v_fmamk_f32 v110, v110, 0x7f000000, v84\n"
            "v_or_b32 v112, 3, v112\n"
            "v_or_b32 v113, 3, v113\n"
            "v_or_b32 v114, 3, v114\n"
            "s_waitcnt lgkmcnt(3)\n"
            "s_swappc_b64 s[80:81], s[70:71]\n"
            "v_lshlrev_b32 v98, 16, v99\n"
            "v_lshlrev_b32 v100, 16, v101\n"
            "v_lshlrev_b32 v102, 16, v103\n"
            "v_pk_fma_f32 v[98:99], v[98:99], v[108:109], v[86:87] op_sel:[0,0,0] op_sel_hi:[1,0,1]\n"
            "v_pk_fma_f32 v[100:101], v[100:101], v[108:109], v[86:87] op_sel:[0,1,0] op_sel_hi:[1,1,1]\n"
            "v_pk_fma_f32 v[102:103], v[102:103], v[110:111], v[86:87] op_sel:[0,0,0] op_sel_hi:[1,0,1]\n"
            "v_lshl_add_u32 v98, v99, 16, v98\n"
            "v_lshl_add_u32 v100, v101, 16, v100\n"
            "v_lshl_add_u32 v102, v103, 16, v102\n"
            "v_sub_u32 v98, v98, v86\n"
            "v_mad_i32_i16 v88, v99, v112, v88\n"
            "ds_add_u32 v112, v98 offset:65533\n"
            "v_sub_u32 v100, v100, v86\n"
            "v_mad_i32_i16 v88, v101, v113, v88\n"
            "ds_add_u32 v113, v100 offset:65533\n"
            "v_sub_u32 v102, v102, v86\n"
            "v_mad_i32_i16 v88, v103, v114, v88\n"
            "ds_add_u32 v114, v102 offset:65533\n"
            "v_and_b32 v104, 0x1fffc, v16\n"
            "v_and_b32 v105, 0x1fffc, v17\n"
            "v_and_b32 v106, 0x1fffc, v18\n"
            "s_swappc_b64 s[80:81], s[72:73]\n"
            "ds_read_b32 v93, v104\n"
            "ds_read_b32 v95, v105\n"
            "ds_read_b32 v97, v106\n"
            "s_waitcnt lgkmcnt(0)\n"
            "s_branch Ls2_%=\n"
            "Le2_%=:\n"
            "s_cmp_lt_u32 s75, 8\n"
            "s_cbranch_scc1 1f\n"
            "s_swappc_b64 s[80:81], s[66:67]\n"
            "s_branch Lc2_%=\n"
            "1:\n"
            "s_cmp_lg_u32 s75, 7\n"
            "s_cbranch_scc1 1f\n"
            "s_setprio 2\n"
            "s_mov_b32 s79, 3\n"
            "s_branch Lc2_%=\n"
            "1:\n"
            "s_cmp_lg_u32 s75, 3\n"
            "s_cbranch_scc1 1f\n"
            "s_setprio 1\n"
            "s_mov_b32 s79, 1\n"
            "s_branch Lc2_%=\n"
            "1:\n"
            "s_setprio 0\n"
            "v_add_u32_dpp v111, v81, v82 row_newbcast:2 row_mask:0xf bank_mask:0x3\n"
            "v_add_u32_dpp v111, v81, v82 row_newbcast:10 row_mask:0xf bank_mask:0xc\n"
            "v_and_b32 v108, 0x7fe, v19\n"
            "v_and_b32 v109, 0x1ff800, v19\n"
            "v_lshrrev_b32 v110, 22, v19\n"
            "v_lshrrev_b32 v112, 15, v16\n"
            "v_lshrrev_b32 v113, 15, v17\n"
            "v_lshrrev_b32 v114, 15, v18\n"
            "v_and_b32 v111, " IDXMASK ", v111\n"
            "v_min_u32 v111, v111, v83\n"
            "buffer_load_dwordx4 v[16:19], v111, %[rs], 0 idxen " RAUX "\n"
            "v_fmamk_f32 v108, v108, 0x7e800000, v84\n"
            "v_fmamk_f32 v109, v109, 0x79800000, v84\n"
            "v_fmamk_f32 v110, v110, 0x7f000000, v84\n"
            "v_or_b32 v112, 3, v112\n"
            "v_or_b32 v113, 3, v113\n"
            "v_or_b32 v114, 3, v114\n"
            "s_waitcnt lgkmcnt(3)\n"
            "s_swappc_b64 s[80:81], s[70:71]\n"
            "v_lshlrev_b32 v92, 16, v93\n"
            "v_lshlrev_b32 v94, 16, v95\n"
            "v_lshlrev_b32 v96, 16, v97\n"
            "v_pk_fma_f32 v[92:93], v[92:93], v[108:109], v[86:87] op_sel:[0,0,0] op_sel_hi:[1,0,1]\n"
            "v_pk_fma_f32 v[94:95], v[94:95], v[108:109], v[86:87] op_sel:[0,1,0] op_sel_hi:[1,1,1]\n"
            "v_pk_fma_f32 v[96:97], v[96:97], v[110:111], v[86:87] op_sel:[0,0,0] op_sel_hi:[1,0,1]\n"
            "v_lshl_add_u32 v92, v93, 16, v92\n"
            "v_lshl_add_u32 v94, v95, 16, v94\n"
            "v_lshl_add_u32 v96, v97, 16, v96\n"
            "v_sub_u32 v92, v92, v86\n"
            "v_mad_i32_i16 v88, v93, v112, v88\n"
            "ds_add_u32 v112, v92 offset:65533\n"
            "v_sub_u32 v94, v94, v86\n"
            "v_mad_i32_i16 v88, v95, v113, v88\n"
            "ds_add_u32 v113, v94 offset:65533\n"
            "v_sub_u32 v96, v96, v86\n"
            "v_mad_i32_i16 v88, v97, v114, v88\n"
            "ds_add_u32 v114, v96 offset:65533\n"
            "v_and_b32 v104, 0x1fffc, v20\n"
            "v_and_b32 v105, 0x1fffc, v21\n"
            "v_and_b32 v106, 0x1fffc, v22\n"
            "s_swappc_b64 s[80:81], s[72:73]\n"
            "ds_read_b32 v99, v104\n"
            "ds_read_b32 v101, v105\n"
            "ds_read_b32 v103, v106\n"
            "s_waitcnt lgkmcnt(0)\n"
            "s_branch Ls3_%=\n"
            "Le3_%=:\n"
            "s_cmp_lt_u32 s75, 8\n"
            "s_cbranch_scc1 1f\n"
            "s_swappc_b64 s[80:81], s[66:67]\n"
            "s_branch Lc3_%=\n"
            "1:\n"
            "s_cmp_lg_u32 s75, 7\n"
            "s_cbranch_scc1 1f\n"
            "s_setprio 2\n"
            "s_mov_b32 s79, 3\n"
            "s_branch Lc3_%=\n"
            "1:\n"
            "s_cmp_lg_u32 s75, 3\n"
            "s_cbranch_scc1 1f\n"
            "s_setprio 1\n"
            "s_mov_b32 s79, 1\n"
            "s_branch Lc3_%=\n"
            "1:\n"
            "s_setprio 0\n"
            "v_add_u32_dpp v111, v81, v82 row_newbcast:3 row_mask:0xf bank_mask:0x3\n"
            "v_add_u32_dpp v111, v81, v82 row_newbcast:11 row_mask:0xf bank_mask:0xc\n"
            "v_and_b32 v108, 0x7fe, v23\n"
            "v_and_b32 v109, 0x1ff800, v23\n"
            "v_lshrrev_b32 v110, 22, v23\n"
            "v_lshrrev_b32 v112, 15, v20\n"
            "v_lshrrev_b32 v113, 15, v21\n"
            "v_lshrrev_b32 v114, 15, v22\n"
            "v_and_b32 v111, " IDXMASK ", v111\n"
            "v_min_u32 v111, v111, v83\n"
            "buffer_load_dwordx4 v[20:23], v111, %[rs], 0 idxen " RAUX "\n"
            "v_fmamk_f32 v108, v108, 0x7e800000, v84\n"
            "v_fmamk_f32 v109, v109, 0x79800000, v84\n"
            "v_fmamk_f32 v110, v110, 0x7f000000, v84\n"
            "v_or_b32 v112, 3, v112\n"
            "v_or_b32 v113, 3, v113\n"
            "v_or_b32 v114, 3, v114\n"
            "s_waitcnt lgkmcnt(3)\n"
            "s_swappc_b64 s[80:81], s[70:71]\n"
            "v_lshlrev_b32 v98, 16, v99\n"
            "v_lshlrev_b32 v100, 16, v101\n"
            "v_lshlrev_b32 v102, 16, v103\n"
            "v_pk_fma_f32 v[98:99], v[98:99], v[108:109], v[86:87] op_sel:[0,0,0] op_sel_hi:[1,0,1]\n"
            "v_pk_fma_f32 v[100:101], v[100:101], v[108:109], v[86:87] op_sel:[0,1,0] op_sel_hi:[1,1,1]\n"
            "v_pk_fma_f32 v[102:103], v[102:103], v[110:111], v[86:87] op_sel:[0,0,0] op_sel_hi:[1,0,1]\n"
            "v_lshl_add_u32 v98, v99, 16, v98\n"
            "v_lshl_add_u32 v100, v101, 16, v100\n"
            "v_lshl_add_u32 v102, v103, 16, v102\n"
            "v_sub_u32 v98, v98, v86\n"
            "v_mad_i32_i16 v88, v99, v112, v88\n"
            "ds_add_u32 v112, v98 offset:65533\n"
            "v_sub_u32 v100, v100, v86\n"
            "v_mad_i32_i16 v88, v101, v113, v88\n"
            "ds_add_u32 v113, v100 offset:65533\n"
            "v_sub_u32 v102, v102, v86\n"
            "v_mad_i32_i16 v88, v103, v114, v88\n"
            "ds_add_u32 v114, v102 offset:65533\n"
            "v_and_b32 v104, 0x1fffc, v24\n"
            "v_and_b32 v105, 0x1fffc, v25\n"
            "v_and_b32 v106, 0x1fffc, v26\n"
            "s_swappc_b64 s[80:81], s[72:73]\n"
            "ds_read_b32 v93, v104\n"
            "ds_read_b32 v95, v105\n"
            "ds_read_b32 v97, v106\n"
            "s_waitcnt lgkmcnt(0)\n"
            "s_branch Ls4_%=\n"
            "Le4_%=:\n"
            "s_cmp_lt_u32 s75, 8\n"
            "s_cbranch_scc1 1f\n"
            "s_swappc_b64 s[80:81], s[66:67]\n"
            "s_branch Lc4_%=\n"
            "1:\n"
            "s_cmp_lg_u32 s75, 7\n"
            "s_cbranch_scc1 1f\n"
            "s_setprio 2\n"
            "s_mov_b32 s79, 3\n"
            "s_branch Lc4_%=\n"
            "1:\n"
            "s_cmp_lg_u32 s75, 3\n"
            "s_cbranch_scc1 1f\n"
            "s_setprio 1\n"
            "s_mov_b32 s79, 1\n"
            "s_branch Lc4_%=\n"
            "1:\n"
            "s_setprio 0\n"
            "v_add_u32_dpp v111, v81, v82 row_newbcast:4 row_mask:0xf bank_mask:0x3\n"
            "v_add_u32_dpp v111, v81, v82 row_newbcast:12 row_mask:0xf bank_mask:0xc\n"
            "v_and_b32 v108, 0x7fe, v27\n"
            "v_and_b32 v109, 0x1ff800, v27\n"
            "v_lshrrev_b32 v110, 22, v27\n"
            "v_lshrrev_b32 v112, 15, v24\n"
            "v_lshrrev_b32 v113, 15, v25\n"
            "v_lshrrev_b32 v114, 15, v26\n"
            "v_and_b32 v111, " IDXMASK ", v111\n"
            "v_min_u32 v111, v111, v83\n"
            "buffer_load_dwordx4 v[24:27], v111, %[rs], 0 idxen " RAUX "\n"
            "v_fmamk_f32 v108, v108, 0x7e800000, v84\n"
            "v_fmamk_f32 v109, v109, 0x79800000, v84\n"
            "v_fmamk_f32 v110, v110, 0x7f000000, v84\n"
            "v_or_b32 v112, 3, v112\n"
            "v_or_b32 v113, 3, v113\n"
            "v_or_b32 v114, 3, v114\n"
            "s_waitcnt lgkmcnt(3)\n"
            "s_swappc_b64 s[80:81], s[70:71]\n"
            "v_lshlrev_b32 v92, 16, v93\n"
            "v_lshlrev_b32 v94, 16, v95\n"
            "v_lshlrev_b32 v96, 16, v97\n"
            "v_pk_fma_f32 v[92:93], v[92:93], v[108:109], v[86:87] op_sel:[0,0,0] op_sel_hi:[1,0,1]\n"
            "v_pk_fma_f32 v[94:95], v[94:95], v[108:109], v[86:87] op_sel:[0,1,0] op_sel_hi:[1,1,1]\n"
            "v_pk_fma_f32 v[96:97], v[96:97], v[110:111], v[86:87] op_sel:[0,0,0] op_sel_hi:[1,0,1]\n"
            "v_lshl_add_u32 v92, v93, 16, v92\n"
            "v_lshl_add_u32 v94, v95, 16, v94\n"
            "v_lshl_add_u32 v96, v97, 16, v96\n"
            "v_sub_u32 v92, v92, v86\n"
            "v_mad_i32_i16 v88, v93, v112, v88\n"
            "ds_add_u32 v112, v92 offset:65533\n"
            "v_sub_u32 v94, v94, v86\n"
            "v_mad_i32_i16 v88, v95, v113, v88\n"
            "ds_add_u32 v113, v94 offset:65533\n"
            "v_sub_u32 v96, v96, v86\n"
            "v_mad_i32_i16 v88, v97, v114, v88\n"
            "ds_add_u32 v114, v96 offset:65533\n"
            "v_and_b32 v104, 0x1fffc, v28\n"
            "v_and_b32 v105, 0x1fffc, v29\n"
            "v_and_b32 v106, 0x1fffc, v30\n"
            "s_swappc_b64 s[80:81], s[72:73]\n"
            "ds_read_b32 v99, v104\n"
            "ds_read_b32 v101, v105\n"
            "ds_read_b32 v103, v106\n"
            "s_waitcnt lgkmcnt(0)\n"
            "s_branch Ls5_%=\n"
            "Le5_%=:\n"
            "s_cmp_lt_u32 s75, 8\n"
            "s_cbranch_scc1 1f\n"
            "s_swappc_b64 s[80:81], s[66:67]\n"
            "s_branch Lc5_%=\n"
            "1:\n"
            "s_cmp_lg_u32 s75, 7\n"
            "s_cbranch_scc1 1f\n"
            "s_setprio 2\n"
            "s_mov_b32 s79, 3\n"
            "s_branch Lc5_%=\n"
            "1:\n"
            "s_cmp_lg_u32 s75, 3\n"
            "s_cbranch_scc1 1f\n"
            "s_setprio 1\n"
            "s_mov_b32 s79, 1\n"
            "s_branch Lc5_%=\n"
            "1:\n"
            "s_setprio 0\n"
            "v_add_u32_dpp v111, v81, v82 row_newbcast:5 row_mask:0xf bank_mask:0x3\n"
            "v_add_u32_dpp v111, v81, v82 row_newbcast:13 row_mask:0xf bank_mask:0xc\n"
            "v_and_b32 v108, 0x7fe, v31\n"
            "v_and_b32 v109, 0x1ff800, v31\n"
            "v_lshrrev_b32 v110, 22, v31\n"
            "v_lshrrev_b32 v112, 15, v28\n"
            "v_lshrrev_b32 v113, 15, v29\n"
            "v_lshrrev_b32 v114, 15, v30\n"
            "v_and_b32 v111, " IDXMASK ", v111\n"
            "v_min_u32 v111, v111, v83\n"
            "buffer_load_dwordx4 v[28:31], v111, %[rs], 0 idxen " RAUX "\n"
            "v_fmamk_f32 v108, v108, 0x7e800000, v84\n"
            "v_fmamk_f32 v109, v109, 0x79800000, v84\n"
            "v_fmamk_f32 v110, v110, 0x7f000000, v84\n"
            "v_or_b32 v112, 3, v112\n"
            "v_or_b32 v113, 3, v113\n"
            "v_or_b32 v114, 3, v114\n"
            "s_waitcnt lgkmcnt(3)\n"
            "s_swappc_b64 s[80:81], s[70:71]\n"
            "v_lshlrev_b32 v98, 16, v99\n"
            "v_lshlrev_b32 v100, 16, v101\n"
            "v_lshlrev_b32 v102, 16, v103\n"
            "v_pk_fma_f32 v[98:99], v[98:99], v[108:109], v[86:87] op_sel:[0,0,0] op_sel_hi:[1,0,1]\n"
            "v_pk_fma_f32 v[100:101], v[100:101], v[108:109], v[86:87] op_sel:[0,1,0] op_sel_hi:[1,1,1]\n"
            "v_pk_fma_f32 v[102:103], v[102:103], v[110:111], v[86:87] op_sel:[0,0,0] op_sel_hi:[1,0,1]\n"
            "v_lshl_add_u32 v98, v99, 16, v98\n"
            "v_lshl_add_u32 v100, v101, 16, v100\n"
            "v_lshl_add_u32 v102, v103, 16, v102\n"
            "v_sub_u32 v98, v98, v86\n"
            "v_mad_i32_i16 v88, v99, v112, v88\n"
            "ds_add_u32 v112, v98 offset:65533\n"
            "v_sub_u32 v100, v100, v86\n"
            "v_mad_i32_i16 v88, v101, v113, v88\n"
            "ds_add_u32 v113, v100 offset:65533\n"
            "v_sub_u32 v102, v102, v86\n"
            "v_mad_i32_i16 v88, v103, v114, v88\n"
            "ds_add_u32 v114, v102 offset:65533\n"
            "v_and_b32 v104, 0x1fffc, v32\n"
            "v_and_b32 v105, 0x1fffc, v33\n"
            "v_and_b32 v106, 0x1fffc, v34\n"
            "s_swappc_b64 s[80:81], s[72:73]\n"
            "ds_read_b32 v93, v104\n"
            "ds_read_b32 v95, v105\n"
            "ds_read_b32 v97, v106\n"
            "s_waitcnt lgkmcnt(0)\n"
            "s_branch Ls6_%=\n"
            "Le6_%=:\n"
            "s_cmp_lt_u32 s75, 8\n"
            "s_cbranch_scc1 1f\n"
            "s_swappc_b64 s[80:81], s[66:67]\n"
            "s_branch Lc6_%=\n"
            "1:\n"
            "s_cmp_lg_u32 s75, 7\n"
            "s_cbranch_scc1 1f\n"
            "s_setprio 2\n"
            "s_mov_b32 s79, 3\n"
            "s_branch Lc6_%=\n"
            "1:\n"
            "s_cmp_lg_u32 s75, 3\n"
            "s_cbranch_scc1 1f\n"
            "s_setprio 1\n"
            "s_mov_b32 s79, 1\n"
            "s_branch Lc6_%=\n"
            "1:\n"
            "s_setprio 0\n"
            "v_add_u32_dpp v111, v81, v82 row_newbcast:6 row_mask:0xf bank_mask:0x3\n"
            "v_add_u32_dpp v111, v81, v82 row_newbcast:14 row_mask:0xf bank_mask:0xc\n"
            "v_and_b32 v108, 0x7fe, v35\n"
            "v_and_b32 v109, 0x1ff800, v35\n"
            "v_lshrrev_b32 v110, 22, v35\n"
            "v_lshrrev_b32 v112, 15, v32\n"
            "v_lshrrev_b32 v113, 15, v33\n"
            "v_lshrrev_b32 v114, 15, v34\n"
            "v_and_b32 v111, " IDXMASK ", v111\n"
            "v_min_u32 v111, v111, v83\n"
            "buffer_load_dwordx4 v[32:35], v111, %[rs], 0 idxen " RAUX "\n"
            "v_fmamk_f32 v108, v108, 0x7e800000, v84\n"
            "v_fmamk_f32 v109, v109, 0x79800000, v84\n"
            "v_fmamk_f32 v110, v110, 0x7f000000, v84\n"
            "v_or_b32 v112, 3, v112\n"
            "v_or_b32 v113, 3, v113\n"
            "v_or_b32 v114, 3, v114\n"
            "s_waitcnt lgkmcnt(3)\n"
            "s_swappc_b64 s[80:81], s[70:71]\n"
            "v_lshlrev_b32 v92, 16, v93\n"
            "v_lshlrev_b32 v94, 16, v95\n"
            "v_lshlrev_b32 v96, 16, v97\n"
            "v_pk_fma_f32 v[92:93], v[92:93], v[108:109], v[86:87] op_sel:[0,0,0] op_sel_hi:[1,0,1]\n"
            "v_pk_fma_f32 v[94:95], v[94:95], v[108:109], v[86:87] op_sel:[0,1,0] op_sel_hi:[1,1,1]\n"
            "v_pk_fma_f32 v[96:97], v[96:97], v[110:111], v[86:87] op_sel:[0,0,0] op_sel_hi:[1,0,1]\n"
            "v_lshl_add_u32 v92, v93, 16, v92\n"
            "v_lshl_add_u32 v94, v95, 16, v94\n"
            "v_lshl_add_u32 v96, v97, 16, v96\n"
            "v_sub_u32 v92, v92, v86\n"
            "v_mad_i32_i16 v88, v93, v112, v88\n"
            "ds_add_u32 v112, v92 offset:65533\n"
            "v_sub_u32 v94, v94, v86\n"
            "v_mad_i32_i16 v88, v95, v113, v88\n"
            "ds_add_u32 v113, v94 offset:65533\n"
            "v_sub_u32 v96, v96, v86\n"
            "v_mad_i32_i16 v88, v97, v114, v88\n"
            "ds_add_u32 v114, v96 offset:65533\n"
            "v_and_b32 v104, 0x1fffc, v36\n"
            "v_and_b32 v105, 0x1fffc, v37\n"
            "v_and_b32 v106, 0x1fffc, v38\n"
            "s_swappc_b64 s[80:81], s[72:73]\n"
            "ds_read_b32 v99, v104\n"
            "ds_read_b32 v101, v105\n"
            "ds_read_b32 v103, v106\n"
            "s_waitcnt lgkmcnt(0)\n"
            "s_branch Ls7_%=\n"
            "Le7_%=:\n"
            "s_cmp_lt_u32 s75, 8\n"
            "s_cbranch_scc1 1f\n"
            "s_swappc_b64 s[80:81], s[66:67]\n"
            "s_branch Lc7_%=\n"
            "1:\n"
            "s_cmp_lg_u32 s75, 7\n"
            "s_cbranch_scc1 1f\n"
            "s_setprio 2\n"
            "s_mov_b32 s79, 3\n"
            "s_branch Lc7_%=\n"
            "1:\n"
            "s_cmp_lg_u32 s75, 3\n"
            "s_cbranch_scc1 1f\n"
            "s_setprio 1\n"
            "s_mov_b32 s79, 1\n"
            "s_branch Lc7_%=\n"
            "1:\n"
            "s_setprio 0\n"
            "v_add_u32_dpp v111, v81, v82 row_newbcast:7 row_mask:0xf bank_mask:0x3\n"
            "v_add_u32_dpp v111, v81, v82 row_newbcast:15 row_mask:0xf bank_mask:0xc\n"
            "v_and_b32 v108, 0x7fe, v39\n"
            "v_and_b32 v109, 0x1ff800, v39\n"
            "v_lshrrev_b32 v110, 22, v39\n"
            "v_lshrrev_b32 v112, 15, v36\n"
            "v_lshrrev_b32 v113, 15, v37\n"
            "v_lshrrev_b32 v114, 15, v38\n"
            "v_and_b32 v111, " IDXMASK ", v111\n"
            "v_min_u32 v111, v111, v83\n"
            "buffer_load_dwordx4 v[36:39], v111, %[rs], 0 idxen " RAUX "\n"
            "s_min_u32 s82, s82, 19\n"
            "s_add_u32 s82, s82, 1\n"
            "s_set_gpr_idx_on s82, gpr_idx(SRC0)\n"
            "s_nop 0\n"
            "v_mov_b32 v81, v60\n"
            "s_set_gpr_idx_off\n"
            "v_fmamk_f32 v108, v108, 0x7e800000, v84\n"
            "v_fmamk_f32 v109, v109, 0x79800000, v84\n"
            "v_fmamk_f32 v110, v110, 0x7f000000, v84\n"
            "v_or_b32 v112, 3, v112\n"
            "v_or_b32 v113, 3, v113\n"
            "v_or_b32 v114, 3, v114\n"
            "s_waitcnt lgkmcnt(3)\n"
            "s_swappc_b64 s[80:81], s[70:71]\n"
            "v_lshlrev_b32 v98, 16, v99\n"
            "v_lshlrev_b32 v100, 16, v101\n"
            "v_lshlrev_b32 v102, 16, v103\n"
            "v_pk_fma_f32 v[98:99], v[98:99], v[108:109], v[86:87] op_sel:[0,0,0] op_sel_hi:[1,0,1]\n"
            "v_pk_fma_f32 v[100:101], v[100:101], v[108:109], v[86:87] op_sel:[0,1,0] op_sel_hi:[1,1,1]\n"
            "v_pk_fma_f32 v[102:103], v[102:103], v[110:111], v[86:87] op_sel:[0,0,0] op_sel_hi:[1,0,1]\n"
            "v_lshl_add_u32 v98, v99, 16, v98\n"
            "v_lshl_add_u32 v100, v101, 16, v100\n"
            "v_lshl_add_u32 v102, v103, 16, v102\n"
            "v_sub_u32 v98, v98, v86\n"
            "v_mad_i32_i16 v88, v99, v112, v88\n"
            "ds_add_u32 v112, v98 offset:65533\n"
            "v_sub_u32 v100, v100, v86\n"
            "v_mad_i32_i16 v88, v101, v113, v88\n"
            "ds_add_u32 v113, v100 offset:65533\n"
            "v_sub_u32 v102, v102, v86\n"
            "v_mad_i32_i16 v88, v103, v114, v88\n"
            "ds_add_u32 v114, v102 offset:65533\n"
            "v_and_b32 v104, 0x1fffc, v8\n"
            "v_and_b32 v105, 0x1fffc, v9\n"
            "v_and_b32 v106, 0x1fffc, v10\n"
            "s_swappc_b64 s[80:81], s[72:73]\n"
            "ds_read_b32 v93, v104\n"
            "ds_read_b32 v95, v105\n"
            "ds_read_b32 v97, v106\n"
            "s_waitcnt lgkmcnt(0)\n"
            "s_branch Ls0_%=\n"
            "Lchg1%=:\n"
            "s_add_u32 s92, s92, 1\n"
            "s_cmp_eq_u32 s92, 8\n"
            "s_cbranch_scc1 1f\n"
            "s_barrier\n"
            "s_mov_b32 m0, %[wb]\n"
            "s_nop 0\n"
            "ds_write_addtid_b32 v40 offset:0\n"
            "ds_write_addtid_b32 v41 offset:256\n"
            "ds_write_addtid_b32 v42 offset:512\n"
            "ds_write_addtid_b32 v43 offset:768\n"
            "ds_write_addtid_b32 v44 offset:16384\n"
            "ds_write_addtid_b32 v45 offset:16640\n"
            "ds_write_addtid_b32 v46 offset:16896\n"
            "ds_write_addtid_b32 v47 offset:17152\n"
            "ds_write_addtid_b32 v48 offset:32768\n"
            "ds_write_addtid_b32 v49 offset:33024\n"
            "ds_write_addtid_b32 v50 offset:33280\n"
            "ds_write_addtid_b32 v51 offset:33536\n"
            "ds_write_addtid_b32 v52 offset:49152\n"
            "ds_write_addtid_b32 v53 offset:49408\n"
            "ds_write_addtid_b32 v54 offset:49664\n"
            "ds_write_addtid_b32 v55 offset:49920\n"
            "s_add_u32 m0, %[wb], 16384\n"
            "s_nop 0\n"
            "ds_write_addtid_b32 v56 offset:49152\n"
            "ds_write_addtid_b32 v57 offset:49408\n"
            "ds_write_addtid_b32 v58 offset:49664\n"
            "ds_write_addtid_b32 v59 offset:49920\n"
            "1:\n"
            "s_setpc_b64 s[80:81]\n"
            "Lchg2%=:\n"
            "s_cmp_eq_u32 s92, 8\n"
            "s_cbranch_scc1 Lfin%=\n"
            "s_lshr_b64 s[76:77], s[76:77], 8\n"
            "s_and_b32 s75, s76, 255\n"
            "s_movk_i32 s74, 1000\n"
            "s_sub_i32 s79, s75, %[j0]\n"
            "s_max_i32 s79, s79, 8\n"
            "s_cmp_eq_u32 s92, 7\n s_cselect_b32 s79, 8, s79\n"
            "s_setprio 3\n"
            "s_waitcnt lgkmcnt(3)\n"
            "s_barrier\n"
            "s_setpc_b64 s[80:81]\n"
            "Lchg3%=:\n"
            "s_sub_u32 s74, s75, 7\n"
            "s_mov_b32 s79, 7\n"
            "s_cmp_eq_u32 s92, 7\n"
            "s_cbranch_scc1 1f\n"
            "s_add_u32 s94, s94, %[i4]\n"
            "buffer_load_dwordx4 v[40:43], %[tid16], %[rm], s94 offen\n"
            "s_add_u32 s96, s94, 16384\n"
            "buffer_load_dwordx4 v[44:47], %[tid16], %[rm], s96 offen\n"
            "s_add_u32 s96, s94, 32768\n"
            "buffer_load_dwordx4 v[48:51], %[tid16], %[rm], s96 offen\n"
            "s_add_u32 s96, s94, 49152\n"
            "buffer_load_dwordx4 v[52:55], %[tid16], %[rm], s96 offen\n"
            "s_add_u32 s96, s94, 65536\n"
            "buffer_load_dwordx4 v[56:59], %[tid16], %[rm], s96 offen\n"
            "s_branch 2f\n"
            "1:\n"
            "buffer_load_ushort v48, %[ed], %[rd], 0 offen " NTH "\n"
            "buffer_load_ushort v49, %[ed], %[rd], 0 offen offset:2 " NTH "\n"
            "buffer_load_ushort v50, %[ed], %[rd], %[e1] offen " NTH "\n"
            "buffer_load_ushort v51, %[ed], %[rd], %[e1] offen offset:2 " NTH "\n"
            "v_lshlrev_b32 v52, 2, %[ed]\n"
            "s_lshl_b32 s96, %[e1], 2\n"
            "buffer_load_dwordx4 v[40:43], v52, %[rmm], 0 offen " NTH "\n"
            "buffer_load_dwordx4 v[44:47], v52, %[rmm], s96 offen " NTH "\n"
            "2:\n"
            "s_setpc_b64 s[80:81]\n"
            "Lfin%=:\n"
            "s_waitcnt vmcnt(0) lgkmcnt(0)\n"
            "s_barrier\n"
            "ds_read_b128 v[8:11], v89 offset:0\n"
            "ds_read_b128 v[12:15], v89 offset:16384\n"
            "ds_read_b128 v[16:19], v89 offset:32768\n"
            "ds_read_b128 v[20:23], v89 offset:49152\n"
            "ds_read_b128 v[24:27], v90\n"
            "v_subrev_u32 v117, 65533, v89\n"
            "v_mov_b32 v118, 0\n"
            "v_bfe_u32 v116, %[tid16], 9, 3\n"
            "v_cmp_eq_u32 vcc, %[q], v116\n"
            "v_bfrev_b32 v115, 1\n"
            "s_nop 0\n"
            "v_cndmask_b32 v115, %[tid16], v115, vcc\n"
            "s_mov_b32 s96, 16384\n s_mov_b32 s97, 32768\n s_mov_b32 s92, 49152\n s_mov_b32 s93, 65536\n"
            "s_waitcnt lgkmcnt(4)\n"
            "buffer_store_dwordx4 v[8:11], v115, %[rp0], 0 offen sc1\n"
            "s_waitcnt lgkmcnt(3)\n"
            "buffer_store_dwordx4 v[12:15], v115, %[rp0], s96 offen sc1\n"
            "s_waitcnt lgkmcnt(2)\n"
            "buffer_store_dwordx4 v[16:19], v115, %[rp0], s97 offen sc1\n"
            "s_waitcnt lgkmcnt(1)\n"
            "buffer_store_dwordx4 v[20:23], v115, %[rp0], s92 offen sc1\n"
            "s_waitcnt lgkmcnt(0)\n"
            "buffer_store_dwordx4 v[24:27], v115, %[rp0], s93 offen sc1\n"
            "v_add_u32 v112, 0x8000, v8\n"
            "v_add_u32 v116, 0, v117\n"
            "v_mad_i32_i16 v118, v112, v116, v118 op_sel:[1,0,0,0]\n"
            "v_add_u32 v112, 0x8000, v9\n"
            "v_add_u32 v116, 4, v117\n"
            "v_mad_i32_i16 v118, v112, v116, v118 op_sel:[1,0,0,0]\n"
            "v_add_u32 v112, 0x8000, v10\n"
            "v_add_u32 v116, 8, v117\n"
            "v_mad_i32_i16 v118, v112, v116, v118 op_sel:[1,0,0,0]\n"
            "v_add_u32 v112, 0x8000, v11\n"
            "v_add_u32 v116, 12, v117\n"
            "v_mad_i32_i16 v118, v112, v116, v118 op_sel:[1,0,0,0]\n"
            "v_add_u32 v117, 16384, v117\n"
            "v_add_u32 v112, 0x8000, v12\n"
            "v_add_u32 v116, 0, v117\n"
            "v_mad_i32_i16 v118, v112, v116, v118 op_sel:[1,0,0,0]\n"
            "v_add_u32 v112, 0x8000, v13\n"
            "v_add_u32 v116, 4, v117\n"
            "v_mad_i32_i16 v118, v112, v116, v118 op_sel:[1,0,0,0]\n"
            "v_add_u32 v112, 0x8000, v14\n"
            "v_add_u32 v116, 8, v117\n"
            "v_mad_i32_i16 v118, v112, v116, v118 op_sel:[1,0,0,0]\n"
            "v_add_u32 v112, 0x8000, v15\n"
            "v_add_u32 v116, 12, v117\n"
            "v_mad_i32_i16 v118, v112, v116, v118 op_sel:[1,0,0,0]\n"
            "v_add_u32 v117, 16384, v117\n"
            "v_add_u32 v112, 0x8000, v16\n"
            "v_add_u32 v116, 0, v117\n"
            "v_mad_i32_i16 v118, v112, v116, v118 op_sel:[1,0,0,0]\n"
            "v_add_u32 v112, 0x8000, v17\n"
            "v_add_u32 v116, 4, v117\n"
            "v_mad_i32_i16 v118, v112, v116, v118 op_sel:[1,0,0,0]\n"
            "v_add_u32 v112, 0x8000, v18\n"
            "v_add_u32 v116, 8, v117\n"
            "v_mad_i32_i16 v118, v112, v116, v118 op_sel:[1,0,0,0]\n"
            "v_add_u32 v112, 0x8000, v19\n"
            "v_add_u32 v116, 12, v117\n"
            "v_mad_i32_i16 v118, v112, v116, v118 op_sel:[1,0,0,0]\n"
            "v_add_u32 v117, 16384, v117\n"
            "v_add_u32 v112, 0x8000, v20\n"
            "v_add_u32 v116, 0, v117\n"
            "v_mad_i32_i16 v118, v112, v116, v118 op_sel:[1,0,0,0]\n"
            "v_add_u32 v112, 0x8000, v21\n"
            "v_add_u32 v116, 4, v117\n"
            "v_mad_i32_i16 v118, v112, v116, v118 op_sel:[1,0,0,0]\n"
            "v_add_u32 v112, 0x8000, v22\n"
            "v_add_u32 v116, 8, v117\n"
            "v_mad_i32_i16 v118, v112, v116, v118 op_sel:[1,0,0,0]\n"
            "v_add_u32 v112, 0x8000, v23\n"
            "v_add_u32 v116, 12, v117\n"
            "v_mad_i32_i16 v118, v112, v116, v118 op_sel:[1,0,0,0]\n"
            "v_add_u32 v117, 16384, v117\n"
            "v_add_u32 v112, 0x8000, v24\n"
            "v_add_u32 v116, 0, v117\n"
            "v_mad_i32_i16 v118, v112, v116, v118 op_sel:[1,0,0,0]\n"
            "v_add_u32 v112, 0x8000, v25\n"
            "v_add_u32 v116, 4, v117\n"
            "v_mad_i32_i16 v118, v112, v116, v118 op_sel:[1,0,0,0]\n"
            "v_add_u32 v112, 0x8000, v26\n"
            "v_add_u32 v116, 8, v117\n"
            "v_mad_i32_i16 v118, v112, v116, v118 op_sel:[1,0,0,0]\n"
            "v_add_u32 v112, 0x8000, v27\n"
            "v_add_u32 v116, 12, v117\n"
            "v_mad_i32_i16 v118, v112, v116, v118 op_sel:[1,0,0,0]\n"
            "v_add_u32 v117, 16384, v117\n"
            "v_sub_u32 v118, v88, v118\n"
            "v_mov_b32 v116, %[wb]\n"
            "v_mov_b32 v117, %[bad]\n"
            "v_add_u32_dpp v118, v118, v118 row_shr:1 row_mask:0xf bank_mask:0xf bound_ctrl:1\n"
            "s_nop 1\n"
            "v_add_u32_dpp v118, v118, v118 row_shr:2 row_mask:0xf bank_mask:0xf bound_ctrl:1\n"
            "s_nop 1\n"
            "v_add_u32_dpp v118, v118, v118 row_shr:4 row_mask:0xf bank_mask:0xf bound_ctrl:1\n"
            "s_nop 1\n"
            "v_add_u32_dpp v118, v118, v118 row_shr:8 row_mask:0xf bank_mask:0xf bound_ctrl:1\n"
            "s_nop 1\n"
            "v_add_u32_dpp v118, v118, v118 row_bcast:15 row_mask:0xa bank_mask:0xf\n"
            "s_nop 1\n"
            "v_add_u32_dpp v118, v118, v118 row_bcast:31 row_mask:0xc bank_mask:0xf\n"
            "s_nop 1\n"
            "v_lshrrev_b32 v116, 7, v116\n"
            "s_mov_b32 exec_lo, 0\n s_mov_b32 exec_hi, 0x80000000\n"
            "ds_write2_b32 v116, v118, v117 offset1:1\n"
            "s_mov_b64 exec, -1\n"
            "s_branch Lend2%=\n"
            "Lend%=:\n"
            "s_waitcnt vmcnt(0)\n"
            "Lend2%=:\n"
            "s_setprio 0\n"
            "s_waitcnt lgkmcnt(0)\n"

            : [mode] "=&s"(mode), [bad] "=&s"(bad), [exs] "=&v"(exs), [tid16] "+v"(t16), "+v"(tc2), "=&{v[40:43]}"(mm[0]), "=&{v[44:47]}"(mm[1]), "=&{v[48:51]}"(dd)
            : [rs] "s"(rs), [rm] "s"(rm), [rp0] "s"(rp0), [rt] "s"(rt), [i4] "s"(LMAX * 4), [c4] "s"(a_.c4), [pb] "s"(81920 + wave * 5120), [wb] "s"(wave * 1024), [q] "s"(q), [j0] "s"(J0),
              [rmm] "s"(rmm), [rd] "s"(rd), [e1] "s"(128 * 32 * 128 * 2), [ed] "v"((((wave * 8 + q) * 32 + b) * 128 + lane * 2) * 2),
              [fof] "s"((NT * NWS * 3 + cp * (P1_GRID * 2)) * 4), [dumu] "s"(ly.dumu),
              [tof] "v"((w * NT + t0) * 12), [fb] "v"(frag_unit(w, ly)), [xb] "v"((blk_line(w) + grp_line(qt)) * 8), [hm] "v"(slot_line((lane >> 4) * 64 + b) * 8)
            : "memory", "vcc", "scc", "m0", "s66", "s67", "s70", "s71", "s72", "s73", "s74", "s75", "s76", "s77", "s79", "s80", "s81", "s82",
              "s84", "s85", "s86", "s87", "s88", "s89", "s90", "s91", "s92", "s93", "s94", "s96", "s97",
              "v8", "v9", "v10", "v11", "v12", "v13", "v14", "v15", "v16", "v17", "v18", "v19", "v20", "v21", "v22", "v23", "v24", "v25", "v26", "v27", "v28", "v29", "v30", "v31", "v32", "v33", "v34", "v35", "v36", "v37", "v38", "v39", "v52", "v53", "v54", "v55", "v56", "v57", "v58", "v59", "v60", "v61", "v62", "v63", "v64", "v65", "v66", "v67", "v68", "v69", "v70", "v71", "v72", "v73", "v74", "v75", "v76", "v77", "v78", "v79", "v80", "v81", "v82", "v83", "v84", "v85", "v86", "v87", "v88", "v89", "v90", "v91", "v92", "v93", "v94", "v95", "v96", "v97", "v98", "v99", "v100", "v101", "v102", "v103", "v104", "v105", "v106", "v107", "v108", "v109", "v110", "v111", "v112", "v113", "v114", "v115", "v116", "v117", "v118");
    }
    const int tid2 = (int)(t16 >> 4);
    kaptr ka = (kaptr)__builtin_amdgcn_kernarg_segment_ptr();
    asm volatile("" : "+s"(ka));
    unsigned long long* wm = (unsigned long long*)(ka->wgmode + b * 32);
    const int E = ka->E;
    const unsigned nblk = ka->O >> 7;
    const int ln = tid2 & 63, wv = __builtin_amdgcn_readfirstlane(tid2 >> 6);
    const bool two = (16 + wv) * 8 + q < nblk;
    float g[2][2][4], h[2][2][4];
    auto gh = [&]() {
#pragma unroll
        for (int p = 0; p < 2; p++) {
            if (p == 0 || two) {
                edge_gh(lo_bf(p ? dd.z : dd.x), ka->centers, ka->w_sbf, ka->w_rbf, g[p][0], h[p][0]);
                edge_gh(lo_bf(p ? dd.w : dd.y), ka->centers, ka->w_sbf, ka->w_rbf, g[p][1], h[p][1]);
            }
        }
    };
    v2u own[2] = {{0, 0}, {0, 0}};
    if (mode == 0) {
        gh();
        own[0] = *(const v2u*)(lds + OBASE + (wv * 8 + q) * 128 + ln * 2);
        if (two) own[1] = *(const v2u*)(lds + OBASE + ((16 + wv) * 8 + q) * 128 + ln * 2);
        asm volatile("s_waitcnt vmcnt(0)" : "+v"(g[0][0][0]), "+v"(h[0][1][3]) :: "memory");
        __syncthreads();
        const v2u cs = *(const v2u*)(lds + (ln & 15) * 2);
        if (__builtin_amdgcn_readlane(row_scan_add(cs.x), 15) != 0 || __ballot(cs.y != 0)) mode = 1;
    }
    if (mode != 0) {
        acc_fallback(lds, mode, *ka, tid2);
        asm volatile("s_waitcnt vmcnt(0)" ::: "memory");
        __syncthreads();
        if (tid2 == P2_THREADS - 1) { __builtin_amdgcn_fence(__ATOMIC_RELEASE, "agent"); asm volatile("s_waitcnt vmcnt(0)" ::: "memory"); }
        const __amdgpu_buffer_rsrc_t rmm = __builtin_amdgcn_make_buffer_rsrc((void*)ka->m, (short)0, E * 8, 0x00020000);
        const __amdgpu_buffer_rsrc_t rdd = __builtin_amdgcn_make_buffer_rsrc((void*)ka->dist, (short)0, E * 2, 0x00020000);
        const unsigned e = ((wv * 8 + q) * 32 + b) * 128 + ln * 2, e1 = e + 128 * 32 * 128;
        mm[0] = __builtin_amdgcn_raw_buffer_load_b128(rmm, e * 8, 0, 0); mm[1] = __builtin_amdgcn_raw_buffer_load_b128(rmm, e1 * 8, 0, 0);
        dd.x = __builtin_amdgcn_raw_buffer_load_b16(rdd, e * 2, 0, 0); dd.y = __builtin_amdgcn_raw_buffer_load_b16(rdd, e * 2 + 2, 0, 0);
        dd.z = __builtin_amdgcn_raw_buffer_load_b16(rdd, e1 * 2, 0, 0); dd.w = __builtin_amdgcn_raw_buffer_load_b16(rdd, e1 * 2 + 2, 0, 0);
        gh();
        own[0].x = own[0].y = own[1].x = own[1].y = 0;
    }
    const unsigned* prb = ka->praw + (size_t)b * ka->O; unsigned short* outp = ka->out;
    int EP = ka->EP;
    unsigned wu[8];
#pragma unroll
    for (int i = 0; i < 8; i++) wu[i] = ((cptr)ka->w_up)[i];
    if (tid2 == P2_THREADS - 1) {
        const unsigned long long add = (1u | (mode ? 256u << q : 0u)) | (qt == 0 ? (unsigned long long)(__builtin_amdgcn_readfirstlane(exs) & 0xFFFF) << (32 + 16 * cp) : 0ull);
        unsigned long long v = __hip_atomic_fetch_add(wm, add, __ATOMIC_RELAXED, __HIP_MEMORY_SCOPE_AGENT) + add;
        for (int spin = 0; spin < (1 << 22) && (v & 255) != 8; spin++) {
            v = __hip_atomic_load(wm, __ATOMIC_RELAXED, __HIP_MEMORY_SCOPE_AGENT);
            if ((v & 255) != 8) __builtin_amdgcn_s_sleep(2);
        }
        if (v & 0xFF00) { __builtin_amdgcn_fence(__ATOMIC_ACQUIRE, "agent"); asm volatile("s_waitcnt vmcnt(0)" ::: "memory"); }
        lds[64] = ((unsigned)v >> 8) & 255; lds[65] = (unsigned)(v >> 32);
    }
    asm volatile("" : "+s"(prb), "+s"(outp), "+s"(EP), "+s"(wu[0]), "+s"(wu[1]), "+s"(wu[2]), "+s"(wu[3]), "+s"(wu[4]), "+s"(wu[5]), "+s"(wu[6]), "+s"(wu[7]));
    const __amdgpu_buffer_rsrc_t rpr = __builtin_amdgcn_make_buffer_rsrc((void*)prb, (short)0, (7 * EP + (int)(nblk << 7)) * 4, 0x00020000);
    const __amdgpu_buffer_rsrc_t rou = __builtin_amdgcn_make_buffer_rsrc((void*)outp, (short)0, E * 8, 0x00020000);
    __syncthreads();
    v2u w[2][8];
#pragma unroll
    for (int p = 0; p < 2; p++) {
        if (p == 0 || two) {
#pragma unroll
            for (int j = 0; j < 8; j++)
                w[p][j] = __builtin_amdgcn_raw_buffer_load_b64(rpr, (((p * 16 + wv) * 8 + q) * 128 + ln * 2) * 4, j == q ? (int)OOB : j * EP * 4, PR_AUX);
        }
    }
    const unsigned fbm = __builtin_amdgcn_readfirstlane(lds[64]), exw = __builtin_amdgcn_readfirstlane(lds[65]);
    float inv[4];
#pragma unroll
    for (int k = 0; k < 4; k++) inv[k] = __uint_as_float(((exw >> (8 * k)) & 255) << 23) * (1.0f / 511.0f);
#pragma unroll
    for (int p = 0; p < 2; p++) {
        const unsigned blk = (p * 16 + wv) * 8 + q;
        if (p == 0 || two) {
            int s[2][4];
            {
                const int l0 = (int)(short)(own[p].x & 0xFFFF), h0 = (int)(own[p].x + 0x8000u) >> 16, l1 = (int)(short)(own[p].y & 0xFFFF), h1 = (int)(own[p].y + 0x8000u) >> 16;
                s[0][0] = cp ? 0 : l0; s[0][1] = cp ? 0 : h0; s[0][2] = cp ? l0 : 0; s[0][3] = cp ? h0 : 0;
                s[1][0] = cp ? 0 : l1; s[1][1] = cp ? 0 : h1; s[1][2] = cp ? l1 : 0; s[1][3] = cp ? h1 : 0;
            }
#pragma unroll
            for (int j = 0; j < 8; j++) {
                s[0][(j & 1) * 2] += (int)(short)(w[p][j].x & 0xFFFF); s[0][(j & 1) * 2 + 1] += (int)(w[p][j].x + 0x8000u) >> 16;
                s[1][(j & 1) * 2] += (int)(short)(w[p][j].y & 0xFFFF); s[1][(j & 1) * 2 + 1] += (int)(w[p][j].y + 0x8000u) >> 16;
            }
            float a[2][4];
#pragma unroll
            for (int i = 0; i < 2; i++)
#pragma unroll
                for (int k = 0; k < 4; k++) a[i][k] = s[i][k] ? (float)s[i][k] * inv[k] : 0.f;
            if (fbm && blk < nblk) {
                const float* part = ka->part + (size_t)b * ka->O + blk * 128 + ln * 2;
#pragma unroll 1
                for (int j = 0; j < 8; j++)
                    if ((fbm >> j) & 1) {
#pragma unroll
                        for (int i = 0; i < 2; i++) {
                            const float x = part[(size_t)(j * 2) * EP + i], y = part[(size_t)(j * 2 + 1) * EP + i];
                            if (j & 1) { a[i][2] += x; a[i][3] += y; } else { a[i][0] += x; a[i][1] += y; }
                        }
                    }
            }
            const v2u r0 = edge_out(a[0], g[p][0], h[p][0], mm[p].x, mm[p].y, wu), r1 = edge_out(a[1], g[p][1], h[p][1], mm[p].z, mm[p].w, wu);
            const v4u r = {r0.x, r0.y, r1.x, r1.y};
            __builtin_amdgcn_raw_buffer_store_b128(r, rou, ((blk * 32 + b) * 128 + ln * 2) * 8, 0, OUT_AUX);
        }
    }
}

extern "C" __global__ __launch_bounds__(256) void k_naive(const int* __restrict__ idx_in, const int* __restrict__ idx_out,
                                               const unsigned short* __restrict__ angle, const unsigned short* __restrict__ ang_freq,
                                               const unsigned short* __restrict__ m, const unsigned short* __restrict__ w_down,
                                               float* __restrict__ part, int T, int EP) {
    const float freq = bf2f(ang_freq[0]);
    for (long long t = (long long)blockIdx.x * 256 + threadIdx.x; t < T; t += (long long)gridDim.x * 256) {
        v2u mv = ((const v2u*)m)[idx_in[t]];
        float a[4] = {lo_bf(mv.x), hi_bf(mv.x), lo_bf(mv.y), hi_bf(mv.y)};
        float c = __cosf(freq * bf2f(angle[t]));
        int eo = idx_out[t];
#pragma unroll
        for (int d = 0; d < 4; d++) {
            float s = 0.f;
#pragma unroll
            for (int k = 0; k < 4; k++) s += a[k] * bf2f(w_down[k * 4 + d]);
            atomicAdd(&part[(size_t)d * EP + eo], s * c);
        }
    }
}

__global__ __launch_bounds__(256) void k_final(const unsigned short* __restrict__ m, const unsigned short* __restrict__ dist,
                                               const unsigned short* __restrict__ centers, const unsigned short* __restrict__ w_rbf,
                                               const unsigned short* __restrict__ w_sbf, const unsigned short* __restrict__ w_up,
                                               const float* __restrict__ part, unsigned short* __restrict__ out, int E, int EP) {
    const int e = blockIdx.x * 256 + threadIdx.x;
    if (e >= E) return;
    float a[4], g[4], h[4];
#pragma unroll
    for (int k = 0; k < 4; k++) a[k] = part[(size_t)k * EP + e];
    edge_gh(bf2f(dist[e]), centers, w_sbf, w_rbf, g, h);
    const v2u mv = ((const v2u*)m)[e];
    unsigned wu[8];
#pragma unroll
    for (int i = 0; i < 8; i++) wu[i] = ((cptr)w_up)[i];
    ((v2u*)out)[e] = edge_out(a, g, h, mv.x, mv.y, wu);
}

extern "C" void kernel_launch(void* const* d_in, const int* in_sizes, int n_in,
                              void* d_out, int out_size, void* d_ws, size_t ws_size,
                              hipStream_t stream) {
    const unsigned short* m = (const unsigned short*)d_in[0];
    const int* idx_in = (const int*)d_in[1];
    const int* idx_out = (const int*)d_in[2];
    const unsigned short* dist = (const unsigned short*)d_in[3];
    const unsigned short* angle = (const unsigned short*)d_in[4];
    const unsigned short* centers = (const unsigned short*)d_in[5];
    const unsigned short* ang_freq = (const unsigned short*)d_in[6];
    const unsigned short* w_rbf = (const unsigned short*)d_in[7];
    const unsigned short* w_sbf = (const unsigned short*)d_in[8];
    const unsigned short* w_down = (const unsigned short*)d_in[9];
    const unsigned short* w_up = (const unsigned short*)d_in[10];
    const int E = in_sizes[0] / 4;
    const int T = in_sizes[1];
    const int NR = T / (P1_GRID * CH);
    const int per = ((T - NR * (P1_GRID * CH) + P1_GRID - 1) / P1_GRID + 3) / 4 * 4;
    const unsigned O = (unsigned)(((E + 127) / 128 + 31) / 32) * 128;
    const unsigned I = (unsigned)(E + 31) / 32;
    const int EP = (int)O * 32;
    Lay ly;
    const unsigned meanr = (unsigned)(((size_t)NR * CH + per) / NT);
    ly.K0 = meanr >= 30 ? (meanr - 30) / 24 : 0;
    ly.KX = ly.K0 + ly.K0 * 2 / 3 + 4;
#define FCAP 4928
    ly.fcap = meanr >= 100 ? FCAP : 8192;
    const size_t lines = (size_t)PLANE * (ly.KX + 1);
    char* ws = (char*)d_ws;
    size_t o = 0;
    unsigned* praw = (unsigned*)(ws + o); o += (size_t)8 * EP * 4; o = (o + 255) / 256 * 256;
    unsigned* wgmode = (unsigned*)(ws + o); o += 16384;
    unsigned* mdp = (unsigned*)(ws + o); o += (size_t)2 * 32 * LMAX * 4;
    unsigned* tab = (unsigned*)(ws + o); o += (size_t)NT * NWS * 12;
    unsigned* flags = (unsigned*)(ws + o); o += FL_SIZE * 4; o = (o + 255) / 256 * 256;
    o += (size_t)NWS * ly.fcap * 16;
    char* rec = ws + o;
    ly.dumu = (unsigned)(lines * 8);
    o += 0x200000 + ((size_t)ly.dumu + 64) * 16 - (size_t)PLANE * 128;
    float* part = (float*)(ws + o); o += (size_t)16 * EP * 4;
    const bool ok = E >= 16384 && O <= LMAX && I <= LMAX && T > 0 && T < (1 << 29) && ly.KX <= 30 && o <= ws_size;
    if (!ok) {
        part = (float*)ws;
        const int EQ = (E + 3) / 4 * 4;
        (void)hipMemsetAsync(part, 0, (size_t)4 * EQ * 4, stream);
        if (T > 0) k_naive<<<2048, 256, 0, stream>>>(idx_in, idx_out, angle, ang_freq, m, w_down, part, T, EQ);
        k_final<<<(E + 255) / 256, 256, 0, stream>>>(m, dist, centers, w_rbf, w_sbf, w_up, part, (unsigned short*)d_out, E, EQ);
        return;
    }
    k_bin<<<P1_GRID, P1_THREADS, 0, stream>>>(idx_in, idx_out, angle, ang_freq, m, w_down, mdp, E, T, rec, tab, flags, NR, per, ly, wgmode);
    AccArgs aa;
    aa.rec = rec; aa.tab = tab; aa.flags = flags; aa.mdp = mdp; aa.part = part; aa.praw = praw; aa.wgmode = wgmode;
    aa.idx_in = idx_in; aa.idx_out = idx_out; aa.angle = angle; aa.ang_freq = ang_freq; aa.m = m; aa.w_down = w_down;
    aa.EP = EP; aa.O = O; aa.T = T; aa.c4 = (float)((double)E / 144.0);
    aa.dist = dist; aa.centers = centers; aa.w_rbf = w_rbf; aa.w_sbf = w_sbf; aa.w_up = w_up; aa.out = (unsigned short*)d_out; aa.E = E; aa.unused = 0;
    aa.ly = ly;
    k_acc<<<256, P2_THREADS, 0, stream>>>(aa);
}
```

```cpp
#include <hip/hip_runtime.h>
#include <hip/hip_bf16.h>
#include <stdint.h>

typedef int v4i __attribute__((ext_vector_type(4)));
typedef unsigned v4u __attribute__((ext_vector_type(4)));
typedef unsigned v2u __attribute__((ext_vector_type(2)));
typedef int v4i_rs __attribute__((ext_vector_type(4)));
typedef __attribute__((address_space(3))) unsigned lds_u32;
typedef __attribute__((address_space(3))) v4u lds_v4u;

static __device__ __forceinline__ float bf2f(unsigned short v) { return __uint_as_float(((unsigned)v) << 16); }
static __device__ __forceinline__ unsigned f2bf_bits(float f) {
    unsigned u = __float_as_uint(f);
    u += 0x7FFF + ((u >> 16) & 1);
    return u >> 16;
}
static __device__ __forceinline__ float lo_bf(unsigned u) { return __uint_as_float(u << 16); }
static __device__ __forceinline__ float hi_bf(unsigned u) { return __uint_as_float(u & 0xFFFF0000u); }
static __device__ __forceinline__ v4i_rs mk_rsrc(const void* p, unsigned bytes) {
    unsigned long long a = (unsigned long long)p;
    v4i_rs r = {(int)(unsigned)a, (int)(unsigned)((a >> 32) & 0xFFFF), (int)bytes, 0x00020000};
    return r;
}
static __device__ __forceinline__ lds_u32* ldsp(unsigned byte) { return (lds_u32*)(size_t)byte; }

#define P1_THREADS 1024
#define P1_GRID 256
#define NWS 256
#define CH (P1_THREADS * 4)
#define NT 1024
#define LMAX 20480
#define P2_THREADS 1024
#define OOB 0x80000000u
#define OBASE 20480
#define IDXB 4096
#define ST_AUX 16
#define LB 4096
#define LS 132
#define TINC 683
#define WL_OFF (LB + NT * LS)
#define MS_OFF (WL_OFF + 16 * 1024)
#define TB_OFF (MS_OFF + 128 + 320)
#define PC_OFF (TB_OFF + 32 * 33 * 4)
#define BIN_LDS (PC_OFF + 128)
#define SINC ((1u << 5) | (1u << 15) | (1u << 24))
#define SW0 (((tid & 3) << 6) | ((run & 16) << 1))
#define NSTAT 14
#define WM_CTR 1024
#define WM_BOX (WM_CTR + 128)
#define WM_LOC (WM_BOX + 256)
#define NTH "nt"
#define OUT_AUX 16
#define PR_AUX 18
#define TCH 7
#define CZERO ((512u << 1) | (512u << 11) | (512u << 22))
#define FL_SIZE (2 * P1_GRID * 2)

#define CB(c) ((((c) >> 3) << 15) | (((c) & 7) << 10))
#define TO(t) ((((t) & 15) * 4) | (((t) >> 6) << 6) | ((((t) >> 4) & 3) << 13))
#define HO 512
struct Grp { v4i io; v4i ii; v2u an; };
static __device__ __forceinline__ void load_grp(Grp& r, v4i_rs ro, v4i_rs ri, v4i_rs ra, unsigned c, unsigned vo16, unsigned vo8) {
    asm volatile("buffer_load_dwordx4 %0, %1, %2, %3 offen nt" : "=v"(r.io) : "v"(vo16), "s"(ro), "s"(c * 2));
    asm volatile("buffer_load_dwordx4 %0, %1, %2, %3 offen nt" : "=v"(r.ii) : "v"(vo16), "s"(ri), "s"(c * 2));
    asm volatile("buffer_load_dwordx2 %0, %1, %2, %3 offen nt" : "=v"(r.an) : "v"(vo8), "s"(ra), "s"(c));
}
#define TELLC ((rd & 6) == 0)

static __device__ __forceinline__ unsigned wave_scan_max(unsigned x) {
    x = max(x, (unsigned)__builtin_amdgcn_update_dpp(0, (int)x, 0x111, 0xF, 0xF, false));
    x = max(x, (unsigned)__builtin_amdgcn_update_dpp(0, (int)x, 0x112, 0xF, 0xF, false));
    x = max(x, (unsigned)__builtin_amdgcn_update_dpp(0, (int)x, 0x114, 0xF, 0xF, false));
    x = max(x, (unsigned)__builtin_amdgcn_update_dpp(0, (int)x, 0x118, 0xF, 0xF, false));
    x = max(x, (unsigned)__builtin_amdgcn_update_dpp(0, (int)x, 0x142, 0xA, 0xF, false));
    x = max(x, (unsigned)__builtin_amdgcn_update_dpp(0, (int)x, 0x143, 0xC, 0xF, false));
    return x;
}
static __device__ __forceinline__ unsigned row_scan_add(unsigned x) {
    x += (unsigned)__builtin_amdgcn_update_dpp(0, (int)x, 0x111, 0xF, 0xF, false);
    x += (unsigned)__builtin_amdgcn_update_dpp(0, (int)x, 0x112, 0xF, 0xF, false);
    x += (unsigned)__builtin_amdgcn_update_dpp(0, (int)x, 0x114, 0xF, 0xF, false);
    x += (unsigned)__builtin_amdgcn_update_dpp(0, (int)x, 0x118, 0xF, 0xF, false);
    return x;
}
static __device__ __forceinline__ unsigned wave_scan_add(unsigned x) {
    x = row_scan_add(x);
    x += (unsigned)__builtin_amdgcn_update_dpp(0, (int)x, 0x142, 0xA, 0xF, false);
    x += (unsigned)__builtin_amdgcn_update_dpp(0, (int)x, 0x143, 0xC, 0xF, false);
    return x;
}

static __device__ __forceinline__ float wave_scan_addf(float x) {
    x += __int_as_float(__builtin_amdgcn_update_dpp(0, __float_as_int(x), 0x111, 0xF, 0xF, false));
    x += __int_as_float(__builtin_amdgcn_update_dpp(0, __float_as_int(x), 0x112, 0xF, 0xF, false));
    x += __int_as_float(__builtin_amdgcn_update_dpp(0, __float_as_int(x), 0x114, 0xF, 0xF, false));
    x += __int_as_float(__builtin_amdgcn_update_dpp(0, __float_as_int(x), 0x118, 0xF, 0xF, false));
    x += __int_as_float(__builtin_amdgcn_update_dpp(0, __float_as_int(x), 0x142, 0xA, 0xF, false));
    x += __int_as_float(__builtin_amdgcn_update_dpp(0, __float_as_int(x), 0x143, 0xC, 0xF, false));
    return x;
}

struct Lay { unsigned K0, KX, dumu, fcap; };
#define PLANE (NWS * NT)
static __device__ __forceinline__ unsigned blk_line(unsigned w) { return (((w & 3) | ((w >> 3) << 2)) << 11) | ((w & 4) << 4); }
static __device__ __forceinline__ unsigned grp_line(unsigned q) { return ((q & 1) << 7) | ((q & 2) << 9); }
static __device__ __forceinline__ unsigned slot_line(unsigned s) { return (s >> 2) | ((s & 3) << 8); }
static __device__ __forceinline__ unsigned frag_unit(unsigned w, const Lay& ly) { return PLANE * 8 - (w + 1) * ly.fcap; }
static __device__ __forceinline__ char* rec_base(const char* p) { return (char*)p + ((int)(-(unsigned)(size_t)p & 0x1FFFFF) - PLANE * 128); }

static __device__ __forceinline__ void conv_tile(unsigned io, unsigned ii, unsigned& t4, unsigned& x) {
    unsigned y;
    asm("v_lshrrev_b32 %0, 5, %3\n v_and_b32 %1, 31, %4\n v_and_b32 %2, 0x7c, %0\n v_lshl_or_b32 %2, %1, 7, %2" : "=&v"(x), "=&v"(y), "=&v"(t4) : "v"(io), "v"(ii));
}
static __device__ __forceinline__ void conv_rec(unsigned io, unsigned ii, float ang, float frev, unsigned& A, unsigned& cq, unsigned& seen) {
    unsigned y;
    asm("v_lshrrev_b32 %1, 3, %3\n v_bfi_b32 %0, %4, %2, %0\n v_add_u32 %1, %5, %1\n v_lshl_add_u32 %0, %0, 17, %1"
        : "+v"(A), "=&v"(y) : "v"(io), "v"(ii), "s"(127u), "n"(IDXB << 17));
    float c = __builtin_amdgcn_cosf(__builtin_amdgcn_fractf(frev * ang));
    const unsigned cb = __float_as_uint(__builtin_fmaf(c, 511.0f, 12583424.0f));
    seen |= cb;
    cq = cb & 0x3FFu;
}
static __device__ __forceinline__ unsigned pad_rec(unsigned q) { return (0x7FFFu - (q & 511)) << 17; }

static __device__ __forceinline__ void prep_md(const v2u* mv, unsigned dst, const unsigned short* w_down, unsigned* mdp, unsigned plane, unsigned* mx, float* sq) {
    float wd[16];
#pragma unroll
    for (int i = 0; i < 8; i++) { unsigned u = ((const __attribute__((address_space(4))) unsigned*)w_down)[i]; wd[2 * i] = lo_bf(u); wd[2 * i + 1] = hi_bf(u); }
    unsigned o[2][4];
#pragma unroll
    for (int j = 0; j < 4; j++) {
        const float a[4] = {lo_bf(mv[j].x), hi_bf(mv[j].x), lo_bf(mv[j].y), hi_bf(mv[j].y)};
        float s[4];
#pragma unroll
        for (int d = 0; d < 4; d++) {
            s[d] = 0.f;
#pragma unroll
            for (int k = 0; k < 4; k++) s[d] += a[k] * wd[k * 4 + d];
            sq[d] += s[d] * s[d];
            mx[d] = max(mx[d], __float_as_uint(s[d]) & 0x7FFFFFFFu);
        }
#pragma unroll
        for (int p = 0; p < 2; p++) {
            const unsigned lo = f2bf_bits(s[2 * p]) & 0xFFFF;
            const unsigned sb = __float_as_uint(s[2 * p + 1]);
            const unsigned h = (unsigned)max((int)((sb & 0x7FFFFFFFu) - lo + 0x8000u), 0) & 0x7FFF0000u;
            o[p][j] = lo | h | (sb & 0x80000000u);
        }
    }
#pragma unroll
    for (int p = 0; p < 2; p++) {
        v4u v = {o[p][0], o[p][1], o[p][2], o[p][3]};
        *(v4u*)(mdp + (size_t)p * plane + dst) = v;
    }
}

static __device__ __forceinline__ unsigned touch_code(int tid) {
    unsigned alo, ahi, blo, r = 0;
    asm volatile("s_getpc_b64 s[96:97]\n s_add_u32 %0, s96, k_bin@rel32@lo+4\n s_addc_u32 %1, s97, k_bin@rel32@hi+12\n"
                 "s_getpc_b64 s[96:97]\n s_add_u32 %2, s96, k_naive@rel32@lo+4\n" : "=&s"(alo), "=&s"(ahi), "=&s"(blo) :: "scc", "s96", "s97");
    const unsigned size = blo - alo < 65536u ? blo - alo : 0u;
    const v4i_rs rc = {(int)alo, (int)(ahi & 0xFFFF), (int)size, 0x00020000};
    if (tid < 512) asm volatile("buffer_load_dword %0, %1, %2, 0 offen" : "=v"(r) : "v"(tid * 128), "s"(rc));
    return r;
}

struct Held { unsigned a[4], t[4], A[4], cd[4]; unsigned long long m[4]; };
struct Conv { unsigned A[4], t4[4], cq[4]; };
#define HB(k) "s_mov_b64 exec, %[m" #k "]\n ds_write_b32 %[a" #k "], %[A" #k "]\n ds_or_b32 %[t" #k "], %[c" #k "]\n"
static __device__ __forceinline__ void held_out(const Held& H) {
    asm volatile(HB(0) HB(1) HB(2) HB(3) "s_mov_b64 exec, -1\n"
                 :: [m0] "s"(H.m[0]), [m1] "s"(H.m[1]), [m2] "s"(H.m[2]), [m3] "s"(H.m[3]), [a0] "v"(H.a[0]), [a1] "v"(H.a[1]), [a2] "v"(H.a[2]), [a3] "v"(H.a[3]),
                   [t0] "v"(H.t[0]), [t1] "v"(H.t[1]), [t2] "v"(H.t[2]), [t3] "v"(H.t[3]),
                   [A0] "v"(H.A[0]), [A1] "v"(H.A[1]), [A2] "v"(H.A[2]), [A3] "v"(H.A[3]), [c0] "v"(H.cd[0]), [c1] "v"(H.cd[1]), [c2] "v"(H.cd[2]), [c3] "v"(H.cd[3])
                 : "memory");
}
static __device__ __forceinline__ void tickets(unsigned* v, const Conv& c, unsigned inc) {
    asm volatile("ds_add_rtn_u32 %0, %4, %8\n ds_add_rtn_u32 %1, %5, %8\n ds_add_rtn_u32 %2, %6, %8\n ds_add_rtn_u32 %3, %7, %8\n"
                 : "=&v"(v[0]), "=&v"(v[1]), "=&v"(v[2]), "=&v"(v[3]) : "v"(c.t4[0]), "v"(c.t4[1]), "v"(c.t4[2]), "v"(c.t4[3]), "v"(inc) : "memory");
}
static __device__ __forceinline__ void tickets_masked(unsigned* v, const Conv& c, unsigned inc, const unsigned long long* vm) {
#pragma unroll
    for (int k = 0; k < 4; k++) {
        v[k] = 0;
        asm volatile("s_mov_b64 exec, %3\n ds_add_rtn_u32 %0, %1, %2\n s_mov_b64 exec, -1" : "+v"(v[k]) : "v"(c.t4[k]), "v"(inc), "s"(vm[k]) : "memory");
    }
}
static __device__ __forceinline__ void place(Held& H, unsigned* v, Conv& c, const unsigned long long* vm, unsigned lb) {
    unsigned bt[4];
#pragma unroll
    for (int k = 0; k < 4; k++) asm("v_mad_u32_u24 %0, %1, %2, %3" : "=v"(bt[k]) : "v"(c.t4[k]), "n"(LS / 4), "v"(lb));
    asm volatile("s_waitcnt lgkmcnt(0)" : "+v"(v[0]), "+v"(v[1]), "+v"(v[2]), "+v"(v[3]));
#pragma unroll
    for (int k = 0; k < 4; k++) {
        H.m[k] = __ballot(v[k] >= 16384u);
        unsigned sh;
        asm("v_lshrrev_b32 %0, 7, %3\n v_lshrrev_b32 %2, 6, %3\n v_and_b32 %0, 0x7c, %0\n v_or_b32 %1, 12, %0\n v_add_u32 %0, %0, %4\n v_add_u32 %1, %1, %4"
            : "=&v"(H.a[k]), "=&v"(H.t[k]), "=&v"(sh) : "v"(v[k]), "v"(bt[k]));
        H.A[k] = c.A[k];
        asm("v_lshlrev_b32 %0, %1, %2" : "=v"(H.cd[k]) : "v"(sh), "v"(c.cq[k]));
        asm volatile("s_andn2_b64 exec, %[w], %[m]\n ds_write_b32 %[a], %[A]\n ds_or_b32 %[t], %[c]\n s_mov_b64 exec, -1\n"
                     :: [w] "s"(vm[k]), [m] "s"(H.m[k]), [a] "v"(H.a[k]), [A] "v"(H.A[k]), [t] "v"(H.t[k]), [c] "v"(H.cd[k]) : "memory", "scc");
    }
}
struct Own { v2u e; unsigned pc, fm, ovf, c0, c1; };
static __device__ __forceinline__ v4u read_unit(unsigned la) {
    const lds_u32* p = ldsp(la);
    v4u v = {p[0], p[1], p[2], p[3]};
    return v;
}
typedef __attribute__((address_space(3))) v2u lds_v2u;
static __device__ __forceinline__ unsigned long long low_lanes(unsigned groups) { return groups >= 8 ? ~0ull : (1ull << (groups * 8)) - 1; }
static __device__ __forceinline__ void flush_pass(v2u e, v4u v, unsigned long long act, unsigned k16, __amdgpu_buffer_rsrc_t rr) {
    unsigned vo;
    asm volatile("s_mov_b64 exec, %1\n ds_write_b32 %2, %3 offset:12\n s_mov_b64 exec, -1\n v_cndmask_b32 %0, %4, %5, %1"
                 : "=v"(vo) : "s"(act), "v"(e.x), "v"(0u), "v"(OOB), "v"(e.y + k16) : "memory");
    __builtin_amdgcn_raw_buffer_store_b128(v, rr, vo, 0, ST_AUX);
}
static __device__ __forceinline__ void flush(Own& o, unsigned& cnt, int tid, unsigned sinc, __amdgpu_buffer_rsrc_t rr) {
    const bool full = cnt >= 16384u;
    const unsigned long long bal = __ballot(full);
    const unsigned nfull = __popcll(bal);
    const unsigned wl = WL_OFF + (tid >> 6) * 1024;
    if (full) {
        unsigned sl;
        asm volatile("ds_add_rtn_u32 %0, %1, %2" : "=v"(sl) : "v"(o.pc), "v"(sinc) : "memory");
        const unsigned pos = __builtin_amdgcn_mbcnt_hi((unsigned)(bal >> 32), __builtin_amdgcn_mbcnt_lo((unsigned)bal, 0));
        cnt -= 24 * TINC;
        *ldsp(tid * 4) = cnt;
        o.ovf |= cnt;
        v2u e = o.e;
        o.fm >>= 1;
        o.e.y = min((o.e.y + PLANE * 128) ^ (o.fm & 8192u), OOB);
        const bool late = o.pc >= PC_OFF + 128;
        o.pc += 16;
        asm volatile("s_waitcnt lgkmcnt(0)" : "+v"(sl) :: "memory");
        sl ^= (sl << 3) & 0x18000300u;
        if (__builtin_expect(__ballot(late) != 0, 0)) {
            if (late) sl = (tid & 255) * SINC;
            else { o.c1 = __builtin_amdgcn_alignbit(o.c1, o.c0, 24); o.c0 = __builtin_amdgcn_alignbit(o.c0, sl, 24); }
        } else { o.c1 = __builtin_amdgcn_alignbit(o.c1, o.c0, 24); o.c0 = __builtin_amdgcn_alignbit(o.c0, sl, 24); }
        e.y |= sl & 0x19F80u;
        *(lds_v2u*)(size_t)(wl + pos * 8) = e;
    }
    const unsigned g = (tid >> 3) & 7, k16 = (tid & 7) * 16;
    v2u e0 = *(lds_v2u*)(size_t)(wl + g * 8), e1 = *(lds_v2u*)(size_t)(wl + g * 8 + 64);
    e0.x += k16; e1.x += k16;
    const v4u v0 = read_unit(e0.x), v1 = read_unit(e1.x);
    asm volatile("" ::: "memory");
    flush_pass(e0, v0, low_lanes(nfull), k16, rr);
    flush_pass(e1, v1, nfull > 8 ? low_lanes(nfull - 8) : 0ull, k16, rr);
    for (unsigned p = 16; p < nfull; p += 8) {
        v2u e = *(lds_v2u*)(size_t)(wl + (p + g) * 8);
        e.x += k16;
        const v4u v = read_unit(e.x);
        asm volatile("" ::: "memory");
        flush_pass(e, v, low_lanes(nfull - p), k16, rr);
    }
    asm volatile("s_waitcnt lgkmcnt(0)" ::: "memory");
}

extern "C" __global__ __launch_bounds__(P1_THREADS) void k_bin(const int* __restrict__ idx_in, const int* __restrict__ idx_out,
                                                    const unsigned short* __restrict__ angle, const unsigned short* __restrict__ ang_freq,
                                                    const unsigned short* __restrict__ m, const unsigned short* __restrict__ w_down,
                                                    unsigned* __restrict__ mdp, int E, int T,
                                                    char* __restrict__ rec_end, unsigned* __restrict__ tab, unsigned* __restrict__ flags,
                                                    int NR, int per, Lay ly, unsigned* __restrict__ wgmode,
                                                    const unsigned short* __restrict__ centers, const unsigned short* __restrict__ w_sbf,
                                                    const unsigned short* __restrict__ w_rbf, const unsigned short* __restrict__ w_up) {
    __shared__ __attribute__((aligned(16))) unsigned L[BIN_LDS / 4];
    const int tid = threadIdx.x;
    const unsigned run = blockIdx.x;
    char* const rec = rec_base(rec_end);
    float freq;
    const v4i_rs ro = mk_rsrc(idx_out, (unsigned)T * 4), ri = mk_rsrc(idx_in, (unsigned)T * 4), ra = mk_rsrc(angle, (unsigned)T * 2);
    const __amdgpu_buffer_rsrc_t rr = __builtin_amdgcn_make_buffer_rsrc((void*)rec, (short)0,
        (int)(ly.dumu * 16),
        0x00020000);
    const unsigned vo16 = TO(tid) * 4, vo8 = TO(tid) * 2;
    const unsigned shard = ((run >> 3) ^ (run >> 5) ^ run) & 3;
    const bool leader = run == shard * 8;
    const v4i_rs rw = mk_rsrc(wgmode + WM_CTR, (128 + 256 + 2048) * 4);
    const int wvi = __builtin_amdgcn_readfirstlane(tid >> 6), wave0 = wvi == 0;
    const unsigned ctr_o = shard * 128, box_o = (128 + shard * 64 + ((run & 7) | ((run >> 5) << 3))) * 4;
#define PLO(j) (((j) & 1) * 8192 + ((j) & 2) * 2048 + ((j) >> 2) * 16384 + ((j) >> 1) * 1280)
    const bool prober = ((run ^ (run >> 2)) & 8) == 0;
    const unsigned loc_o = (tid & 63) - 1u < 8u ? (128 + 256 + (run >> 4) * 128 + ((tid & 63) - 1) * 16 + (run & 15)) * 4 : OOB;
    const unsigned ppg = (((run & 7) + 1) * PLANE + (run >> 4) * 16384) * 128;
    if (wave0) {
        const v4u z = {0, 0, 0, 0};
        asm volatile("buffer_store_dwordx4 %0, %1, %2, 0 offen sc0 sc1" :: "v"(z), "v"(prober ? ppg + PLO(tid >> 3) + (tid & 7) * 16 : OOB), "s"(mk_rsrc(rec, ly.dumu * 16)) : "memory");
        asm volatile("buffer_store_dword %0, %1, %2, 0 offen sc1" :: "v"(0u), "v"(tid == 0 ? (leader ? ctr_o : box_o) : loc_o), "s"(rw) : "memory");
    }
    unsigned tc0 = touch_code(tid);
    unsigned fbits;
    asm volatile("buffer_load_ushort %0, off, %1, 0" : "=v"(fbits) : "s"(mk_rsrc(ang_freq, 2)));
    v2u mv[4];
    const unsigned gp = run * 80 + (tid >> 5) * 4;
    {
        const v4i_rs rm = mk_rsrc(m, (unsigned)E * 8);
        const unsigned o = tid < 640 ? (((gp & ~255u) | ((gp >> 2) & 63)) * 32 + (tid & 31)) * 8 : OOB;
#pragma unroll
        for (int j = 0; j < 4; j++) asm volatile("buffer_load_dwordx2 %0, %1, %2, %3 offen " NTH : "=v"(mv[j]) : "v"(o), "s"(rm), "s"(j * 64 * 32 * 8));
    }
    Grp tg; bool tv[4];
    const unsigned tbase = (unsigned)NR * (P1_GRID * CH) + run * per;
    {
        const bool in = tid * 4 < per;
#pragma unroll
        for (int k = 0; k < 4; k++) tv[k] = in && tbase + tid * 4 + k < (unsigned)T;
        const unsigned o16 = in ? tid * 16 : OOB, o8 = in ? tid * 8 : OOB;
        asm volatile("buffer_load_dwordx4 %0, %1, %2, %3 offen nt" : "=v"(tg.io) : "v"(o16), "s"(ro), "s"(tbase * 4));
        asm volatile("buffer_load_dwordx4 %0, %1, %2, %3 offen nt" : "=v"(tg.ii) : "v"(o16), "s"(ri), "s"(tbase * 4));
        asm volatile("buffer_load_dwordx2 %0, %1, %2, %3 offen nt" : "=v"(tg.an) : "v"(o8), "s"(ra), "s"(tbase * 2));
    }
    Grp cur[2];
    load_grp(cur[0], ro, ri, ra, NR > 0 ? CB(run) * 2 : 0x7F000000u, vo16, vo8);
    load_grp(cur[1], ro, ri, ra, NR > 1 ? CB(run + P1_GRID) * 2 : 0x7F000000u, vo16, vo8);
    Own own;
    own.e.x = LB + tid * LS; own.pc = PC_OFF + (tid >> 8) * 4; own.c0 = own.c1 = 0;
    own.ovf = (unsigned)(size_t)L << 14;
    own.fm = 0; own.e.y = OOB;
    *ldsp(tid * 4) = 64;
#pragma unroll
    for (int i = 0; i < 8; i++) *ldsp(LB + tid * LS + i * 16 + 12) = 0;
    { const v2u z = {0, 0}; *(lds_v2u*)(size_t)(WL_OFF + (tid >> 6) * 1024 + (tid & 63) * 8) = z; }
    if (tid < 32) *ldsp(MS_OFF + tid * 4) = 0;
    if (tid < 32) *ldsp(PC_OFF + tid * 4) = (SW0 & 0xFC) * SINC;
    if (run == 0 && tid < 64) wgmode[(tid >> 1) * 32 + (tid & 1)] = 0;
    if (run == 1 && tid < 64) {
        v4u v = {pad_rec(tid), pad_rec(tid + 64), pad_rec(tid + 128), CZERO};
        *(v4u*)(rec + ((size_t)ly.dumu + tid) * 16) = v;
    }
    if (run == 9 && tid >= 768 && (tid & 127) < 88) {
        const unsigned i = tid & 127, x = (tid >> 6) & 2, j = i - 8;
        const unsigned short* p = i < 8 ? centers + i : i >= 72 ? w_up + ((i - 72) ^ (x * 4)) : (j & 4) ? w_rbf + (j >> 3) * 4 + (j & 3) : w_sbf + (j >> 3) * 4 + ((j & 3) ^ x);
        __hip_atomic_store(flags + FL_SIZE + (tid - 768), (unsigned)*p << 16, __ATOMIC_RELAXED, __HIP_MEMORY_SCOPE_AGENT);
    }
    unsigned ans = 2;
    const unsigned tell_o = (tid & 63) < 16 ? (128 + 256 + (run >> 4) * 128 + (run & 7) * 16 + (tid & 63)) * 4 : OOB;
    if (wave0 && prober) {
        asm volatile("s_waitcnt vmcnt(16) lgkmcnt(0)" ::: "memory");
        if ((run & 7) < ly.KX) {
            unsigned long long t[7];
            unsigned x;
#define PRB1(i, j) "s_memtime %" #i "\n s_load_dword %7, %8, %" #j "\n s_waitcnt lgkmcnt(0)\n"
            asm volatile(PRB1(0, 9) PRB1(1, 10) PRB1(2, 11) PRB1(3, 12) PRB1(4, 13) PRB1(5, 14) "s_memtime %6\n s_waitcnt lgkmcnt(0)"
                         : "=&s"(t[0]), "=&s"(t[1]), "=&s"(t[2]), "=&s"(t[3]), "=&s"(t[4]), "=&s"(t[5]), "=&s"(t[6]), "=&s"(x)
                         : "s"(rec + ppg), "n"(PLO(0)), "n"(PLO(1)), "n"(PLO(2)), "n"(PLO(3)), "n"(PLO(4)), "n"(PLO(5)) : "memory");
            unsigned d[6];
#pragma unroll
            for (int i = 0; i < 6; i++) d[i] = (unsigned)t[i + 1] - (unsigned)t[i];
            ans = 2 | (((min(min(d[0], d[2]), d[4]) > min(min(d[1], d[3]), d[5]) ? 1 : 0) ^ (run >> 2)) & 1);
        }
        asm volatile("buffer_store_dword %0, %1, %2, 0 offen sc1" :: "v"(ans), "v"(tell_o), "s"(rw) : "memory");
    }
    unsigned seen = 0;
    auto convert1 = [&](Grp& gp, Conv& o) {
        conv_tile(gp.io.x, gp.ii.x, o.t4[0], o.A[0]); conv_tile(gp.io.y, gp.ii.y, o.t4[1], o.A[1]);
        conv_tile(gp.io.z, gp.ii.z, o.t4[2], o.A[2]); conv_tile(gp.io.w, gp.ii.w, o.t4[3], o.A[3]);
    };
    auto convert2 = [&](Grp& gp, Conv& o) {
        asm volatile("" : "+v"(gp.an), "+v"(o.A[0]), "+v"(o.A[1]), "+v"(o.A[2]), "+v"(o.A[3]));
        conv_rec(gp.io.x, gp.ii.x, lo_bf(gp.an.x), freq, o.A[0], o.cq[0], seen); conv_rec(gp.io.y, gp.ii.y, hi_bf(gp.an.x), freq, o.A[1], o.cq[1], seen);
        conv_rec(gp.io.z, gp.ii.z, lo_bf(gp.an.y), freq, o.A[2], o.cq[2], seen); conv_rec(gp.io.w, gp.ii.w, hi_bf(gp.an.y), freq, o.A[3], o.cq[3], seen);
#pragma unroll
        for (int k = 0; k < 4; k++) asm volatile("" : "+v"(o.A[k]), "+v"(o.cq[k]));
    };
    const unsigned tinc = TINC, lb7 = LB, sinc = SINC;
    Held H;
    {
        asm volatile("s_waitcnt vmcnt(9)" : "+v"(mv[0]), "+v"(mv[1]), "+v"(mv[2]), "+v"(mv[3]));
        if (tid < 640) {
            unsigned mx[4] = {0, 0, 0, 0};
            float sq[4] = {0.f, 0.f, 0.f, 0.f};
            prep_md(mv, (tid & 31) * LMAX + gp, w_down, mdp, 32 * LMAX, mx, sq);
#pragma unroll
            for (int d = 0; d < 4; d++) {
                const unsigned v = wave_scan_max(mx[d]);
                const float s = wave_scan_addf(sq[d]);
                if ((tid & 63) == 63) { *ldsp(MS_OFF + 128 + (tid >> 6) * 32 + d * 4) = v; *ldsp(MS_OFF + 128 + (tid >> 6) * 32 + 16 + d * 4) = __float_as_uint(s); }
            }
        }
        asm volatile("s_waitcnt vmcnt(3)" : "+v"(tg.io), "+v"(tg.ii), "+v"(tg.an), "+v"(fbits));
        asm volatile("" :: "v"(tc0));
        freq = lo_bf(fbits) * 0.15915494309189535f;
        if (T & 1) {
            const unsigned rel = (unsigned)T - 1 - (tbase + tid * 4);
            if (tv[0] && rel < 4) { const unsigned av = angle[T - 1]; if (rel == 0) tg.an.x = av; else tg.an.y = av; }
        }
        __syncthreads();
        if (tid >= P1_THREADS - 64) {
            const unsigned sa = MS_OFF + 128 + (tid & 15) * 32 + ((tid >> 4) & 3) * 4;
            unsigned mx = (tid & 15) < 10 ? *ldsp(sa) : 0u;
            float sq = (tid & 15) < 10 ? __uint_as_float(*ldsp(sa + 16)) : 0.f;
#pragma unroll
            for (int i = 0; i < 4; i++) {
                mx = max(mx, (unsigned)(i == 0 ? __builtin_amdgcn_update_dpp(0, (int)mx, 0x111, 0xF, 0xF, false) : i == 1 ? __builtin_amdgcn_update_dpp(0, (int)mx, 0x112, 0xF, 0xF, false)
                                        : i == 2 ? __builtin_amdgcn_update_dpp(0, (int)mx, 0x114, 0xF, 0xF, false) : __builtin_amdgcn_update_dpp(0, (int)mx, 0x118, 0xF, 0xF, false)));
                const int si = __float_as_int(sq);
                sq += __int_as_float(i == 0 ? __builtin_amdgcn_update_dpp(0, si, 0x111, 0xF, 0xF, false) : i == 1 ? __builtin_amdgcn_update_dpp(0, si, 0x112, 0xF, 0xF, false)
                                     : i == 2 ? __builtin_amdgcn_update_dpp(0, si, 0x114, 0xF, 0xF, false) : __builtin_amdgcn_update_dpp(0, si, 0x118, 0xF, 0xF, false));
            }
            mx = min(mx ? (mx >> 16) + 2 : 0u, 0x7F80u);
            const unsigned fx = (mx << 16) | (unsigned)__builtin_amdgcn_update_dpp(0, (int)mx, 0x142, 0xA, 0xF, false);
            const unsigned fy = (__float_as_uint(sq) & 0xFFFF0000u) | ((unsigned)__builtin_amdgcn_update_dpp(0, __float_as_int(sq), 0x142, 0xA, 0xF, false) >> 16);
            if ((tid & 31) == 31) __hip_atomic_store((unsigned long long*)(flags + (((tid >> 5) & 1) * P1_GRID + run) * 2), ((unsigned long long)fy << 32) | fx, __ATOMIC_RELAXED, __HIP_MEMORY_SCOPE_AGENT);
        }
        unsigned long long vm[4];
#pragma unroll
        for (int k = 0; k < 4; k++) vm[k] = __ballot(tv[k]);
        if (vm[0]) {
            unsigned v[4];
            Conv cv;
            convert1(tg, cv);
            tickets_masked(v, cv, tinc, vm);
            convert2(tg, cv);
            place(H, v, cv, vm, lb7);
            if ((H.m[0] & vm[0]) | (H.m[1] & vm[1]) | (H.m[2] & vm[2]) | (H.m[3] & vm[3])) own.ovf |= 16384u;
        }
#pragma unroll
        for (int k = 0; k < 4; k++) { H.m[k] = 0; H.a[k] = 0; H.t[k] = 0; H.A[k] = 0; H.cd[k] = 0; }
        if (NR < 2) asm volatile("s_waitcnt vmcnt(0)" ::: "memory");
    }
    const unsigned long long vm[4] = {~0ull, ~0ull, ~0ull, ~0ull};
    unsigned ca = NR > 1 ? CB(run + P1_GRID) * 2 : 16u;
    bool act = true;
#define HH 16
    const unsigned G = NR > NSTAT ? (NR - NSTAT) * (P1_GRID / 4) : 0, GH = G >= HH ? G - HH : G, GE = G >= HH ? G + HH : G;
    unsigned got = 0;
    unsigned dl = 0;
    int st = leader ? 1 : 0;
    auto answers = [&](bool need) {
        unsigned lc = __builtin_amdgcn_readfirstlane(*ldsp(MS_OFF + 108));
        if (!lc && need) {
            for (int i = 0; i < 4096 && !lc; i++) {
                unsigned x = 0;
                asm volatile("buffer_atomic_add %0, %1, %2, 0 offen sc0\n s_waitcnt vmcnt(0)" : "+v"(x) : "v"(loc_o), "s"(rw) : "memory");
                if ((__ballot(x != 0) & 510) == 510) lc = (unsigned)__ballot(x & 1) | 1;
            }
            if (!lc) { own.ovf |= 16384u; dl = 256; }
        }
        if (lc) {
            dl = (((lc >> 1) ^ lc ^ 1) & 255) | 256;
            own.fm = (dl & 255) << 13;
            own.e.y = ((PLANE + blk_line(run) + grp_line(tid >> 8)) * 128) ^ (own.fm & 8192u);
        }
    };
    unsigned cnt;
    if (NR == 0) { asm volatile("s_waitcnt lgkmcnt(0)" ::: "memory"); __syncthreads(); cnt = *ldsp(tid * 4); own.ovf |= cnt; }
    for (int rd = 0; NR > 0; rd += 2) {
        bool done = false;
#pragma unroll
        for (int r = 0; r < 2; r++) {
            unsigned v[4];
            Conv cv;
            const bool nact = wvi >= (ca & 31);
            const unsigned cl = nact ? ca & ~31u : 0x7F000000u;
            if (r || rd) load_grp(cur[r ^ 1], ro, ri, ra, cl, vo16, vo8);
            held_out(H);
            asm volatile("s_cmp_eq_u32 %3, 0\n s_cbranch_scc1 1f\n s_waitcnt vmcnt(5)\n 1:\n s_waitcnt vmcnt(8)" : "+v"(cur[r].io), "+v"(cur[r].ii), "+v"(cur[r].an) : "s"(wvi) : "scc");
            if (act) {
                convert1(cur[r], cv);
                tickets(v, cv, tinc);
                convert2(cur[r], cv);
                place(H, v, cv, vm, lb7);
            } else H.m[0] = H.m[1] = H.m[2] = H.m[3] = 0;
            act = nact;
            if (wave0) {
                asm volatile("s_waitcnt vmcnt(5)" : "+v"(got));
                const unsigned g = __builtin_amdgcn_readfirstlane(got);
                unsigned nx = rd + r + 2 < NR ? ca + P1_GRID * (CH * 2) : 16u;
                if (rd + r + 2 >= NSTAT) {
                    unsigned c = g, sub = 0;
                    if (g >= GH) { c = GH + ((g - GH) >> 1); sub = ((g - GH) & 1) * (HO * 2) - HO * 2 + 8; }
                    nx = st == 2 && g < GE ? CB(NSTAT * P1_GRID + c * 4 + shard) * 2 + sub : 16u;
                }
                if (st == 0 && g != 0) st = 1;
                if (st == 0 && rd + r + 3 == NSTAT) {
                    for (int i = 0; i < 128 && st == 0; i++) {
                        unsigned x = 0;
                        asm volatile("buffer_atomic_add %0, %1, %2, 0 offen sc0\n s_waitcnt vmcnt(0)" : "+v"(x) : "v"(tid == 0 ? box_o : OOB), "s"(rw) : "memory");
                        if (__builtin_amdgcn_readfirstlane(x) != 0) st = 1;
                    }
                }
                if (st == 1 && rd + r + 3 >= NSTAT) st = 2;
                if (ca == 16) nx = 16;
                if ((tid & 63) == 0) *ldsp(MS_OFF + 104) = nx;
                bool ask = !dl;
                if (ask && (__ballot(got != 0) & 510) == 510) { const unsigned lc = (unsigned)__ballot(got & 1) | 1; if (tid == 0) *ldsp(MS_OFF + 108) = lc; ask = false; }
                got = st == 2 && tid == 0 ? 1u : 0u;
                const bool tk = st != 1 && nx != 16;
                asm volatile("buffer_atomic_add %0, %1, %2, 0 offen sc0" : "+v"(got) : "v"(tid == 0 ? (tk ? (st == 2 ? ctr_o : box_o) : OOB) : ask ? loc_o : OOB), "s"(rw) : "memory");
                asm volatile("buffer_store_dword %0, %1, %2, 0 offen sc1" :: "v"(ans), "v"(prober && r == 0 && TELLC ? tell_o : OOB), "s"(rw) : "memory");
                asm volatile("buffer_store_dword %0, %1, %2, 0 offen sc1" :: "v"(1u), "v"(leader ? (128 + shard * 64 + tid) * 4 : OOB), "s"(rw) : "memory");
            }
            asm volatile("s_waitcnt lgkmcnt(0)" ::: "memory");
            __syncthreads();
            cnt = *ldsp(tid * 4);
            const unsigned nx = *ldsp(MS_OFF + 104);
            if (!dl) answers(__ballot(cnt >= 16384u) != 0);
            flush(own, cnt, tid, sinc, rr);
            if (ca == 16) { done = true; break; }
            ca = __builtin_amdgcn_readfirstlane(nx);
            __syncthreads();
        }
        if (done) break;
    }
    unsigned tc1 = touch_code(tid);
    {
        cnt &= 16383u;
        const unsigned nu = (cnt + 2 * TINC) >> 11;
        const unsigned inc = wave_scan_add(nu), off = inc - nu;
        const unsigned tot = ((unsigned)__builtin_amdgcn_readlane((int)inc, 63) + 7) & ~7u;
        unsigned fb = 0;
        if ((tid & 63) == 0) fb = atomicAdd(L + MS_OFF / 4, tot);
        const unsigned wl = WL_OFF + (tid >> 6) * 1024;
        if (nu) {
            const unsigned la = wl + off * 2, src = (tid & 63) * LS;
#pragma unroll
            for (int k = 7; k >= 0; k--) asm volatile("ds_write_b16 %0, %1 offset:%2" :: "v"(la), "v"(src + k * 16), "n"(k * 2) : "memory");
        }
        if ((cnt >> 11) < nu) {
            const unsigned c2 = cnt + TINC, ua = own.e.x + ((cnt >> 7) & 0x70);
            const bool two = (c2 >> 11) < nu;
            *ldsp(ua + 8) = pad_rec(run);
            if (two) *ldsp(ua + 4) = pad_rec(run + 256);
            asm volatile("ds_or_b32 %0, %1 offset:12" :: "v"(ua), "v"(two ? (512u << 11) | (512u << 22) : 512u << 22) : "memory");
        }
        fb = __builtin_amdgcn_readfirstlane(fb);
        const unsigned nl = (own.pc - PC_OFF) >> 4, tp = ((tid & 31) << 5) | ((tid ^ (tid >> 5)) & 31), tq = tid ^ (tid >> 5);
        const unsigned long long cc = (((unsigned long long)own.c1 << 32) | own.c0) << ((64 - 8 * min(nl, 8u)) & 63);
        *ldsp(TB_OFF + ((tid & 31) * 33 + (tid >> 5)) * 4) = min(nl, 31u) | (nu << 5) | ((dl & 255) << 9) | ((fb + off) << 18);
        *ldsp(LB + 128 + tp * LS) = (unsigned)(cc >> 32);
        if (own.ovf >= 16384u || nl > ly.KX || (seen & 0x30000000u) || fb + tot > ly.fcap) {
            __hip_atomic_store(flags + run * 2, 0x7F807F80u, __ATOMIC_RELAXED, __HIP_MEMORY_SCOPE_AGENT);
            __hip_atomic_store(flags + (P1_GRID + run) * 2, 0x7F807F80u, __ATOMIC_RELAXED, __HIP_MEMORY_SCOPE_AGENT);
        }
        typedef __attribute__((address_space(3))) unsigned short lds_u16;
        const unsigned lb = LB + (tid >> 6) * (64 * LS), ln = tid & 63;
        unsigned sa[5];
#pragma unroll
        for (int i = 0; i < 5; i++) sa[i] = lb + (*(lds_u16*)(size_t)(wl + i * 128 + ln * 2) & 0x3FFCu);
        asm volatile("s_waitcnt lgkmcnt(0)" ::: "memory");
        __syncthreads();
        held_out(H);
        *ldsp(tp * 4) = (unsigned)cc;
        typedef unsigned v3u __attribute__((ext_vector_type(3)));
        v3u tw;
        tw.x = *ldsp(TB_OFF + (tid + (tid >> 5)) * 4); tw.y = *ldsp(LB + 128 + tq * LS);
        asm volatile("s_waitcnt lgkmcnt(0)" ::: "memory");
        __syncthreads();
        const __amdgpu_buffer_rsrc_t rf = __builtin_amdgcn_make_buffer_rsrc((void*)(rec + (size_t)(frag_unit(run, ly) + fb) * 16), (short)0, fb + tot > ly.fcap ? 0 : tot * 16, 0x00020000);
        v4u fv[5];
#pragma unroll
        for (int i = 0; i < 5; i++) fv[i] = read_unit(sa[i]);
        tw.z = *ldsp(tq * 4);
        asm volatile("buffer_store_dwordx3 %0, %1, %2, 0 offen sc1" :: "v"(tw), "v"(tid * 12), "s"(mk_rsrc(tab + run * (NT * 3), NT * 12)) : "memory");
#pragma unroll
        for (int i = 0; i < 5; i++) __builtin_amdgcn_raw_buffer_store_b128(fv[i], rf, ln * 16 + i * 1024, 0, 16);
        for (unsigned p = 320; p < tot; p += 64) {
            const v4u v = read_unit(lb + (*(lds_u16*)(size_t)(wl + (p + ln) * 2) & 0x3FFCu));
            __builtin_amdgcn_raw_buffer_store_b128(v, rf, (p + ln) * 16, 0, 16);
        }
        asm volatile("s_waitcnt vmcnt(0)" :: "v"(tc1));
    }
    asm volatile("s_waitcnt vmcnt(0)" :: "v"(cur[0].io), "v"(cur[0].ii), "v"(cur[0].an), "v"(cur[1].io), "v"(cur[1].ii), "v"(cur[1].an), "v"(got));
}

struct AccArgs {
    const char* rec; const unsigned* tab; const unsigned* flags; const unsigned* mdp; float* part; unsigned* praw; unsigned* wgmode;
    const int* idx_in; const int* idx_out; const unsigned short* angle; const unsigned short* ang_freq; const unsigned short* m; const unsigned short* w_down;
    const unsigned short* dist; const unsigned short* centers; const unsigned short* w_rbf; const unsigned short* w_sbf; const unsigned short* w_up;
    unsigned short* out;
    int EP; unsigned O; int T; float c4; int E; int unused;
    Lay ly;
};
typedef char AccArgs_has_no_padding[sizeof(AccArgs) == 19 * 8 + 10 * 4 ? 1 : -1];

typedef const __attribute__((address_space(4))) unsigned* cptr;
static __device__ __forceinline__ void edge_gh(float d, const unsigned short* centers, const unsigned short* w_sbf, const unsigned short* w_rbf, float* g, float* h) {
#pragma unroll
    for (int k = 0; k < 4; k++) g[k] = h[k] = 0.f;
#pragma unroll
    for (int r = 0; r < 8; r++) {
        const unsigned cu = ((cptr)centers)[r >> 1];
        const float df = d - ((r & 1) ? hi_bf(cu) : lo_bf(cu));
        const float rb = __expf(-10.f * df * df);
#pragma unroll
        for (int k = 0; k < 2; k++) {
            const unsigned u = ((cptr)w_sbf)[r * 2 + k], v = ((cptr)w_rbf)[r * 2 + k];
            g[2 * k] += rb * lo_bf(u); g[2 * k + 1] += rb * hi_bf(u); h[2 * k] += rb * lo_bf(v); h[2 * k + 1] += rb * hi_bf(v);
        }
    }
}
static __device__ __forceinline__ v2u edge_out(const float* agg, const float* g, const float* h, unsigned m0, unsigned m1, const unsigned* wu) {
    float a[4];
#pragma unroll
    for (int k = 0; k < 4; k++) a[k] = agg[k] * g[k];
    const float mm[4] = {lo_bf(m0), hi_bf(m0), lo_bf(m1), hi_bf(m1)};
    float o[4];
#pragma unroll
    for (int dd = 0; dd < 4; dd++) {
        float s = 0.f;
#pragma unroll
        for (int k = 0; k < 4; k++) { const unsigned u = wu[k * 2 + (dd >> 1)]; s += a[k] * ((dd & 1) ? hi_bf(u) : lo_bf(u)); }
        o[dd] = mm[dd] + s * h[dd];
    }
    v2u r;
    asm("v_cvt_pk_bf16_f32 %0, %1, %2" : "=v"(r.x) : "v"(o[0]), "v"(o[1]));
    asm("v_cvt_pk_bf16_f32 %0, %1, %2" : "=v"(r.y) : "v"(o[2]), "v"(o[3]));
    return r;
}

typedef const __attribute__((address_space(4))) AccArgs* kaptr;
static __device__ __forceinline__ void acc_fallback(unsigned* lds, unsigned mode, const __attribute__((address_space(4))) AccArgs& a_, int tid) {
    const char* rec = rec_base(a_.rec); const unsigned* flags = a_.flags; const unsigned* mdp = a_.mdp;
    const int EP = a_.EP, T = a_.T; const Lay ly = {a_.ly.K0, a_.ly.KX, a_.ly.dumu, a_.ly.fcap};
    const int lane = tid & 63;
    const int xcd = blockIdx.x & 7, jj = blockIdx.x >> 3;
    const int qt = xcd >> 1, cp = jj & 1, b = (xcd & 1) * 16 + (jj >> 1);
    const unsigned O = a_.O;
    float* pout0 = a_.part + (size_t)(qt * 4 + cp * 2) * EP + (size_t)b * O;
    const unsigned G = tid >> 3, k = tid & 7;
    {
        unsigned* praw = a_.praw + (size_t)(qt * 2 + cp) * EP + (size_t)b * O;
        for (unsigned i = tid; i < O; i += P2_THREADS) praw[i] = 0x8000u;
    }
    if (mode == 1) {
        unsigned* acc = lds + OBASE;
        const float Xm = 2139095040.0f / (float)(NWS * (ly.KX * 24 + 24)) - 2.0f;
#pragma unroll 1
        for (int c = 0; c < 2; c++) {
            __syncthreads();
            if (tid == 0) lds[0] = 0;
            __syncthreads();
            if (tid < NWS) { unsigned v = wave_scan_max((flags[(cp * P1_GRID + tid) * 2] >> (16 * c)) & 0xFFFF); if (lane == 63) atomicMax(&lds[0], v); }
            __syncthreads();
            unsigned mbits = __builtin_amdgcn_readfirstlane(lds[0]);
            if (mbits == 0) mbits = 0x3f80;
            const float M = __uint_as_float(mbits << 16);
            const float S = Xm / (511.0f * M), inv = M / Xm;
            float racc[20];
#pragma unroll
            for (int r = 0; r < 20; r++) racc[r] = 0.f;
#pragma unroll 1
            for (int s = 0; s < 8; s++) {
                __syncthreads();
                const unsigned* src = mdp + (size_t)cp * (32 * LMAX) + (size_t)(qt * 8 + s) * LMAX;
                for (unsigned i = tid; i < LMAX; i += P2_THREADS) {
                    unsigned w = src[i];
                    lds[(i & ~255u) | ((i & 3) << 6) | ((i >> 2) & 63)] = __float_as_uint((c ? __uint_as_float(w) : lo_bf(w)) * S);
                }
                for (int i = tid; i < OBASE; i += P2_THREADS) acc[i] = 0;
                __syncthreads();
                const unsigned t = b * 32 + qt * 8 + s;
                for (unsigned r = G; r < NWS; r += 128) {
                    const unsigned* te = a_.tab + (r * NT + t) * 3;
                    const unsigned tw = te[0], nl = tw & 31, nu = (tw >> 5) & 15;
                    for (unsigned l = 0; l <= nl; l++) {
                        unsigned u;
                        if (l < nl) u = (((l + 1) * PLANE + blk_line(r) + grp_line(qt) + slot_line(l < 8 ? (te[1 + (l >> 2)] >> (24 - 8 * (l & 3))) & 255 : s * 32 + b)) ^ ((__popc((tw >> 9) & 511 & ((2u << l) - 1)) & 1) << 6)) * 8 + k;
                        else { if (k >= nu) break; u = frag_unit(r, ly) + (tw >> 18) + k; }
                        v4u q = *(const v4u*)(rec + (size_t)u * 16);
                        unsigned a[3] = {q.x, q.y, q.z};
#pragma unroll
                        for (int j = 0; j < 3; j++) {
                            const int cq = (int)((q.w >> (j == 0 ? 1 : j == 1 ? 11 : 22)) & 1023) - 512;
                            const int x = __float2int_rn(__uint_as_float(lds[(a[j] >> 2) & 0x7FFF]) * (float)cq);
                            if ((a[j] >> 17) - IDXB < OBASE) atomicAdd(&acc[(a[j] >> 17) - IDXB], (unsigned)x);
                        }
                    }
                }
                __syncthreads();
#pragma unroll
                for (int r = 0; r < 20; r++) racc[r] += (float)(int)acc[r * P2_THREADS + tid] * inv;
            }
#pragma unroll
            for (int r = 0; r < 20; r++) if ((unsigned)(r * P2_THREADS + tid) < O) pout0[(size_t)c * EP + r * P2_THREADS + tid] = racc[r];
        }
    }
    if (mode == 2) {
        float* facc = (float*)lds;
        const float freq = bf2f(a_.ang_freq[0]);
        float wd[2][4];
#pragma unroll
        for (int kk = 0; kk < 4; kk++) { wd[0][kk] = bf2f(a_.w_down[kk * 4 + cp * 2]); wd[1][kk] = bf2f(a_.w_down[kk * 4 + cp * 2 + 1]); }
        __syncthreads();
        for (int i = tid; i < 2 * OBASE; i += P2_THREADS) facc[i] = 0.f;
        __syncthreads();
        for (int t = tid; t < T; t += P2_THREADS) {
            const unsigned io = a_.idx_out[t], ii = a_.idx_in[t];
            if (((io >> 7) & 31) == (unsigned)b && ((ii >> 3) & 3) == (unsigned)qt) {
                const v2u mv = ((const v2u*)a_.m)[ii];
                const float a[4] = {lo_bf(mv.x), hi_bf(mv.x), lo_bf(mv.y), hi_bf(mv.y)};
                const float cs = __cosf(freq * bf2f(a_.angle[t]));
                const unsigned ol = (io & 127u) | ((io >> 5) & ~127u);
#pragma unroll
                for (int c = 0; c < 2; c++)
                    atomicAdd(&facc[c * OBASE + ol], (a[0] * wd[c][0] + a[1] * wd[c][1] + a[2] * wd[c][2] + a[3] * wd[c][3]) * cs);
            }
        }
        __syncthreads();
        for (unsigned idx = tid; idx < O; idx += P2_THREADS) { pout0[idx] = facc[idx]; pout0[(size_t)EP + idx] = facc[OBASE + idx]; }
    }
}

#define SWAPX 2
#define WXOR ((wave & 1) << 2)
#define RAUX ""
#define J0 (wave >> 1)
#define IDXMASK "0x8fffffff"
#define STR2(x) #x
#define STR(x) STR2(x)
#define WT_OFF 3149824
#define PLAN_IT 160

extern "C" __global__ __launch_bounds__(P2_THREADS) void k_acc(AccArgs a_) {
    const Lay ly = a_.ly;
    __shared__ __attribute__((aligned(16))) unsigned lds[2 * OBASE];
    const int tid = threadIdx.x;
    const int wave = __builtin_amdgcn_readfirstlane(tid >> 6), lane = tid & 63;
    const int xcd = blockIdx.x & 7, jj = blockIdx.x >> 3;
    const int qt = xcd >> 1, cp = jj & 1, b = (xcd & 1) * 16 + (jj >> 1);
    const unsigned O = a_.O;
    unsigned stv = 0;
    unsigned tc2 = touch_code(tid);
    unsigned* praw = a_.praw + (size_t)(qt * 2 + cp) * a_.EP + (size_t)b * O;
    const unsigned* mds = a_.mdp + (size_t)cp * (32 * LMAX) + (size_t)(qt * 8) * LMAX;
    unsigned mode = (unsigned)(size_t)lds ? 2u : 0u;
    unsigned exs = 0, bad = 0;
    v4u mm[2], dd;
    const int q = qt * 2 + cp;
    unsigned t16 = tid * 16;
    if (mode == 0) {
        const unsigned w = wave * 16 + ((lane & 15) ^ (cp ? SWAPX : 0) ^ (WXOR)), t0 = b * 32 + qt * 8 + (lane >> 4) * 2;
        v4i_rs rs = mk_rsrc(rec_base(a_.rec), ly.dumu + 64);
        rs.y |= 16 << 16;
        const v4i_rs rm = mk_rsrc(mds, 8 * LMAX * 4);
        const v4i_rs rp0 = mk_rsrc(praw, O * 4);
        const v4i_rs rt = mk_rsrc(a_.tab, WT_OFF + 1024);
        const v4i_rs rmm = mk_rsrc(a_.m, a_.E * 8), rd = mk_rsrc(a_.dist, a_.E * 2);
        asm volatile(
            "s_setprio 3\n"
            "buffer_load_dwordx3 v[32:34], %[tof], %[rt], 0 offen\n"
            "buffer_load_dwordx3 v[36:38], %[tof], %[rt], 0 offen offset:12\n"
            "v_and_b32 v10, 1023, %[tid16]\n"
            "s_cmp_lg_u32 %[wb], 0x3c00\n s_cbranch_scc1 1f\n"
            "s_and_b32 s96, %[q], 1\n s_lshl_b32 s96, s96, 9\n s_add_u32 s96, s96, " STR(WT_OFF) "\n"
            "s_buffer_load_dword s97, %[rt], s96\n s_add_u32 s96, s96, 64\n"
            "s_buffer_load_dword s97, %[rt], s96\n s_add_u32 s96, s96, 64\n"
            "s_buffer_load_dword s97, %[rt], s96\n s_add_u32 s96, s96, 64\n"
            "s_buffer_load_dword s97, %[rt], s96\n s_add_u32 s96, s96, 64\n"
            "s_buffer_load_dword s97, %[rt], s96\n s_add_u32 s96, s96, 64\n"
            "s_buffer_load_dword s97, %[rt], s96\n s_add_u32 s96, s96, 64\n"
            "1:\n"
            "v_bfrev_b32 v12, 1\n"
            "v_add_u32 v11, %[pb], v10\n"
            "v_bfrev_b32 v13, 1\n"
            "v_bfrev_b32 v14, 1\n"
            "v_bfrev_b32 v15, 1\n"
            "ds_write_b128 v11, v[12:15] offset:0\n"
            "ds_write_b128 v11, v[12:15] offset:1024\n"
            "ds_write_b128 v11, v[12:15] offset:2048\n"
            "ds_write_b128 v11, v[12:15] offset:3072\n"
            "ds_write_b128 v11, v[12:15] offset:4096\n"
            "v_bfrev_b32 v80, 1\n"
            "v_mov_b32 v84, 0xb9000000\n"
            "v_mov_b32 v88, 0\n"
            "v_add_u32 v89, 81920, %[tid16]\n"
            "v_add_u32 v90, 65536, v89\n"
            "v_lshrrev_b32 v16, 4, v10\n"
            "v_add_u32 v83, %[dumu], v16\n"
            "v_and_b32 v17, 7, v16\n"
            "v_lshl_or_b32 v82, v17, 28, v17\n"
            "v_lshrrev_b32 v18, 3, v16\n"
            "v_lshl_add_u32 v17, v17, 3, v18\n"
            "v_lshlrev_b32 v17, 2, v17\n"
            "v_add_u32 v17, %[pb], v17\n"
            "s_getpc_b64 s[70:71]\n"
            "Lpc%=:\n"
            "s_add_u32 s72, s70, Lchg2%=-Lpc%=\n s_addc_u32 s73, s71, 0\n"
            "s_add_u32 s66, s70, Lchg3%=-Lpc%=\n s_addc_u32 s67, s71, 0\n"
            "s_add_u32 s70, s70, Lchg1%=-Lpc%=\n s_addc_u32 s71, s71, 0\n"
            "s_waitcnt vmcnt(0)\n"
            "buffer_load_dwordx4 v[92:95], v10, %[rt], %[fof] offen\n"
            "buffer_load_dwordx4 v[96:99], v10, %[rt], %[fof] offen offset:1024\n"
            "s_mov_b32 s94, 0\n"
            "buffer_load_dwordx4 v[40:43], %[tid16], %[rm], s94 offen\n"
            "s_add_u32 s96, s94, 16384\n"
            "buffer_load_dwordx4 v[44:47], %[tid16], %[rm], s96 offen\n"
            "s_add_u32 s96, s94, 32768\n"
            "buffer_load_dwordx4 v[48:51], %[tid16], %[rm], s96 offen\n"
            "s_add_u32 s96, s94, 49152\n"
            "buffer_load_dwordx4 v[52:55], %[tid16], %[rm], s96 offen\n"
            "s_add_u32 s96, s94, 65536\n"
            "buffer_load_dwordx4 v[56:59], %[tid16], %[rm], s96 offen\n"
            "v_and_b32 v20, 31, v32\n v_and_b32 v21, 31, v36\n v_bfe_u32 v22, v32, 5, 4\n v_bfe_u32 v23, v36, 5, 4\n"
            "v_min_u32 v24, 1, v22\n v_min_u32 v25, 1, v23\n v_add_u32 v24, v24, v20\n v_add_u32 v25, v25, v21\n"
            "v_lshl_or_b32 v24, v25, 16, v24\n"
            "v_and_b32 v19, 0x3fe00, v32\n v_lshrrev_b32 v8, 18, v32\n v_lshrrev_b32 v9, 18, v36\n"
            "v_add_u32_dpp v25, v24, v24 row_shr:1 row_mask:0xf bank_mask:0xf bound_ctrl:1\n"
            "v_add_u32 v8, %[fb], v8\n v_add_u32 v9, %[fb], v9\n"
            "v_add_u32_dpp v25, v25, v25 row_shr:2 row_mask:0xf bank_mask:0xf bound_ctrl:1\n"
            "v_sub_u32 v26, 8, v22\n v_sub_u32 v27, 8, v23\n"
            "v_add_u32_dpp v25, v25, v25 row_shr:4 row_mask:0xf bank_mask:0xf bound_ctrl:1\n"
            "v_lshl_or_b32 v8, v26, 28, v8\n v_lshl_or_b32 v9, v27, 28, v9\n"
            "v_add_u32_dpp v25, v25, v25 row_shr:8 row_mask:0xf bank_mask:0xf bound_ctrl:1\n"
            "s_nop 1\n"
            "v_readlane_b32 s84, v25, 15\n"
            "v_readlane_b32 s85, v25, 31\n"
            "v_readlane_b32 s86, v25, 47\n"
            "v_readlane_b32 s87, v25, 63\n"
            "v_sub_u32 v25, v25, v24\n"
            "s_mov_b32 s96, 0\n s_mov_b64 s[76:77], 0\n"
            "s_add_u32 s84, s84, 0x70007\n"
            "s_bfe_u32 s88, s84, 0xd0003\n s_lshr_b32 s89, s84, 19\n"
            "s_max_u32 s88, s88, 8\n s_max_u32 s89, s89, 8\n"
            "s_add_u32 s90, s96, s88\n"
            "s_pack_ll_b32_b16 s91, s96, s90\n"
            "s_add_u32 s96, s90, s89\n"
            "s_lshl_b32 s89, s89, 8\n s_or_b32 s88, s88, s89\n s_and_b32 s88, s88, 0xffff\n"
            "s_or_b32 s76, s76, s88\n"
            "s_lshl_b32 s91, s91, 3\n"
            "v_mov_b32 v26, s91\n"
            "s_add_u32 s85, s85, 0x70007\n"
            "s_bfe_u32 s88, s85, 0xd0003\n s_lshr_b32 s89, s85, 19\n"
            "s_max_u32 s88, s88, 8\n s_max_u32 s89, s89, 8\n"
            "s_add_u32 s90, s96, s88\n"
            "s_pack_ll_b32_b16 s91, s96, s90\n"
            "s_add_u32 s96, s90, s89\n"
            "s_lshl_b32 s89, s89, 8\n s_or_b32 s88, s88, s89\n s_and_b32 s88, s88, 0xffff\n"
            "s_lshl_b32 s88, s88, 16\n"
            "s_or_b32 s76, s76, s88\n"
            "s_lshl_b32 s91, s91, 3\n"
            "s_mov_b32 exec_lo, 0xffff0000\n s_mov_b32 exec_hi, 0xffffffff\n"
            "v_mov_b32 v26, s91\n"
            "s_add_u32 s86, s86, 0x70007\n"
            "s_bfe_u32 s88, s86, 0xd0003\n s_lshr_b32 s89, s86, 19\n"
            "s_max_u32 s88, s88, 8\n s_max_u32 s89, s89, 8\n"
            "s_add_u32 s90, s96, s88\n"
            "s_pack_ll_b32_b16 s91, s96, s90\n"
            "s_add_u32 s96, s90, s89\n"
            "s_lshl_b32 s89, s89, 8\n s_or_b32 s88, s88, s89\n s_and_b32 s88, s88, 0xffff\n"
            "s_or_b32 s77, s77, s88\n"
            "s_lshl_b32 s91, s91, 3\n"
            "s_mov_b32 exec_lo, 0x0\n s_mov_b32 exec_hi, 0xffffffff\n"
            "v_mov_b32 v26, s91\n"
            "s_add_u32 s87, s87, 0x70007\n"
            "s_bfe_u32 s88, s87, 0xd0003\n s_lshr_b32 s89, s87, 19\n"
            "s_max_u32 s88, s88, 8\n s_max_u32 s89, s89, 8\n"
            "s_add_u32 s90, s96, s88\n"
            "s_pack_ll_b32_b16 s91, s96, s90\n"
            "s_add_u32 s96, s90, s89\n"
            "s_lshl_b32 s89, s89, 8\n s_or_b32 s88, s88, s89\n s_and_b32 s88, s88, 0xffff\n"
            "s_lshl_b32 s88, s88, 16\n"
            "s_or_b32 s77, s77, s88\n"
            "s_lshl_b32 s91, s91, 3\n"
            "s_mov_b32 exec_lo, 0x0\n s_mov_b32 exec_hi, 0xffff0000\n"
            "v_mov_b32 v26, s91\n"
            "s_mov_b64 exec, -1\n"
            "s_mov_b32 %[bad], 0\n"
            "s_cmp_gt_u32 s96, " STR(PLAN_IT) "\n"
            "s_cbranch_scc0 1f\n"
            "s_mov_b32 %[bad], 1\n s_mov_b32 s76, 0x08080808\n s_mov_b32 s77, 0x08080808\n"
            "s_branch Lnoplan%=\n"
            "1:\n"
            "v_add_u32 v25, v25, v26\n"
            "v_and_b32 v26, 0xffff, v25\n v_lshrrev_b32 v27, 16, v25\n"
            "v_lshlrev_b32 v26, 2, v26\n v_lshlrev_b32 v27, 2, v27\n"
            "v_add_u32 v26, %[pb], v26\n v_add_u32 v27, %[pb], v27\n"
            "v_lshl_add_u32 v28, v20, 2, v26\n v_lshl_add_u32 v29, v21, 2, v27\n"
            "v_cmp_ne_u32 vcc, 0, v22\n v_cmp_ne_u32 s[88:89], 0, v23\n"
            "v_mov_b32 v18, %[xb]\n"
            "s_mov_b64 exec, vcc\n ds_write_b32 v28, v8\n s_mov_b64 exec, s[88:89]\n ds_write_b32 v29, v9\n s_mov_b64 exec, -1\n"
            "v_min_u32 v24, v20, v21\n"
            "v_mov_b32 v39, 0x802\n"
            "s_movk_i32 s90, 0x19f8\n"
            "v_cmp_gt_u32 vcc, 4, v24\n"
            "s_cmp_lg_u64 vcc, 0\n s_cbranch_scc0 4f\n"
            "v_cmp_lt_u32 vcc, 0, v20\n v_cmp_lt_u32 s[88:89], 0, v21\n"
            "v_and_b32 v35, 512, v19\n v_add_u32 v18, 2097152, v18\n v_lshrrev_b32 v19, 1, v19\n v_xor_b32 v18, v18, v35\n"
            "v_mul_u32_u24_sdwa v30, v33, v39 dst_sel:DWORD dst_unused:UNUSED_PAD src0_sel:BYTE_3 src1_sel:DWORD\n"
            "v_mul_u32_u24_sdwa v31, v37, v39 dst_sel:DWORD dst_unused:UNUSED_PAD src0_sel:BYTE_3 src1_sel:DWORD\n"
            "v_and_or_b32 v30, v30, s90, v18\n"
            "v_and_or_b32 v31, v31, s90, v18\n"
            "s_or_b64 s[84:85], vcc, s[88:89]\n"
            "s_cbranch_scc0 Lnoplan%=\n"
            "s_mov_b64 exec, vcc\n ds_write_b32 v26, v30 offset:0\n s_mov_b64 exec, s[88:89]\n ds_write_b32 v27, v31 offset:0\n s_mov_b64 exec, -1\n"
            "v_cmp_lt_u32 vcc, 1, v20\n v_cmp_lt_u32 s[88:89], 1, v21\n"
            "v_and_b32 v35, 512, v19\n v_add_u32 v18, 2097152, v18\n v_lshrrev_b32 v19, 1, v19\n v_xor_b32 v18, v18, v35\n"
            "v_mul_u32_u24_sdwa v30, v33, v39 dst_sel:DWORD dst_unused:UNUSED_PAD src0_sel:BYTE_2 src1_sel:DWORD\n"
            "v_mul_u32_u24_sdwa v31, v37, v39 dst_sel:DWORD dst_unused:UNUSED_PAD src0_sel:BYTE_2 src1_sel:DWORD\n"
            "v_and_or_b32 v30, v30, s90, v18\n"
            "v_and_or_b32 v31, v31, s90, v18\n"
            "s_or_b64 s[84:85], vcc, s[88:89]\n"
            "s_cbranch_scc0 Lnoplan%=\n"
            "s_mov_b64 exec, vcc\n ds_write_b32 v26, v30 offset:4\n s_mov_b64 exec, s[88:89]\n ds_write_b32 v27, v31 offset:4\n s_mov_b64 exec, -1\n"
            "v_cmp_lt_u32 vcc, 2, v20\n v_cmp_lt_u32 s[88:89], 2, v21\n"
            "v_and_b32 v35, 512, v19\n v_add_u32 v18, 2097152, v18\n v_lshrrev_b32 v19, 1, v19\n v_xor_b32 v18, v18, v35\n"
            "v_mul_u32_u24_sdwa v30, v33, v39 dst_sel:DWORD dst_unused:UNUSED_PAD src0_sel:BYTE_1 src1_sel:DWORD\n"
            "v_mul_u32_u24_sdwa v31, v37, v39 dst_sel:DWORD dst_unused:UNUSED_PAD src0_sel:BYTE_1 src1_sel:DWORD\n"
            "v_and_or_b32 v30, v30, s90, v18\n"
            "v_and_or_b32 v31, v31, s90, v18\n"
            "s_or_b64 s[84:85], vcc, s[88:89]\n"
            "s_cbranch_scc0 Lnoplan%=\n"
            "s_mov_b64 exec, vcc\n ds_write_b32 v26, v30 offset:8\n s_mov_b64 exec, s[88:89]\n ds_write_b32 v27, v31 offset:8\n s_mov_b64 exec, -1\n"
            "v_cmp_lt_u32 vcc, 3, v20\n v_cmp_lt_u32 s[88:89], 3, v21\n"
            "v_and_b32 v35, 512, v19\n v_add_u32 v18, 2097152, v18\n v_lshrrev_b32 v19, 1, v19\n v_xor_b32 v18, v18, v35\n"
            "v_mul_u32_u24_sdwa v30, v33, v39 dst_sel:DWORD dst_unused:UNUSED_PAD src0_sel:BYTE_0 src1_sel:DWORD\n"
            "v_mul_u32_u24_sdwa v31, v37, v39 dst_sel:DWORD dst_unused:UNUSED_PAD src0_sel:BYTE_0 src1_sel:DWORD\n"
            "v_and_or_b32 v30, v30, s90, v18\n"
            "v_and_or_b32 v31, v31, s90, v18\n"
            "s_or_b64 s[84:85], vcc, s[88:89]\n"
            "s_cbranch_scc0 Lnoplan%=\n"
            "s_mov_b64 exec, vcc\n ds_write_b32 v26, v30 offset:12\n s_mov_b64 exec, s[88:89]\n ds_write_b32 v27, v31 offset:12\n s_mov_b64 exec, -1\n"
            "s_branch 2f\n"
            "4:\n"
            "v_and_b32 v35, 512, v19\n v_add_u32 v18, 2097152, v18\n v_lshrrev_b32 v19, 1, v19\n v_xor_b32 v18, v18, v35\n"
            "v_mul_u32_u24_sdwa v30, v33, v39 dst_sel:DWORD dst_unused:UNUSED_PAD src0_sel:BYTE_3 src1_sel:DWORD\n"
            "v_mul_u32_u24_sdwa v31, v37, v39 dst_sel:DWORD dst_unused:UNUSED_PAD src0_sel:BYTE_3 src1_sel:DWORD\n"
            "v_and_or_b32 v30, v30, s90, v18\n"
            "v_and_or_b32 v31, v31, s90, v18\n"
            "v_and_b32 v35, 512, v19\n v_add_u32 v18, 2097152, v18\n v_lshrrev_b32 v19, 1, v19\n v_xor_b32 v18, v18, v35\n"
            "v_mul_u32_u24_sdwa v28, v33, v39 dst_sel:DWORD dst_unused:UNUSED_PAD src0_sel:BYTE_2 src1_sel:DWORD\n"
            "v_mul_u32_u24_sdwa v29, v37, v39 dst_sel:DWORD dst_unused:UNUSED_PAD src0_sel:BYTE_2 src1_sel:DWORD\n"
            "v_and_or_b32 v28, v28, s90, v18\n"
            "v_and_or_b32 v29, v29, s90, v18\n"
            "ds_write2_b32 v26, v30, v28 offset0:0 offset1:1\n ds_write2_b32 v27, v31, v29 offset0:0 offset1:1\n"
            "v_and_b32 v35, 512, v19\n v_add_u32 v18, 2097152, v18\n v_lshrrev_b32 v19, 1, v19\n v_xor_b32 v18, v18, v35\n"
            "v_mul_u32_u24_sdwa v30, v33, v39 dst_sel:DWORD dst_unused:UNUSED_PAD src0_sel:BYTE_1 src1_sel:DWORD\n"
            "v_mul_u32_u24_sdwa v31, v37, v39 dst_sel:DWORD dst_unused:UNUSED_PAD src0_sel:BYTE_1 src1_sel:DWORD\n"
            "v_and_or_b32 v30, v30, s90, v18\n"
            "v_and_or_b32 v31, v31, s90, v18\n"
            "v_and_b32 v35, 512, v19\n v_add_u32 v18, 2097152, v18\n v_lshrrev_b32 v19, 1, v19\n v_xor_b32 v18, v18, v35\n"
            "v_mul_u32_u24_sdwa v28, v33, v39 dst_sel:DWORD dst_unused:UNUSED_PAD src0_sel:BYTE_0 src1_sel:DWORD\n"
            "v_mul_u32_u24_sdwa v29, v37, v39 dst_sel:DWORD dst_unused:UNUSED_PAD src0_sel:BYTE_0 src1_sel:DWORD\n"
            "v_and_or_b32 v28, v28, s90, v18\n"
            "v_and_or_b32 v29, v29, s90, v18\n"
            "ds_write2_b32 v26, v30, v28 offset0:2 offset1:3\n ds_write2_b32 v27, v31, v29 offset0:2 offset1:3\n"
            "2:\n"
            "v_cmp_lt_u32 vcc, 4, v20\n v_cmp_lt_u32 s[88:89], 4, v21\n"
            "v_and_b32 v35, 512, v19\n v_add_u32 v18, 2097152, v18\n v_lshrrev_b32 v19, 1, v19\n v_xor_b32 v18, v18, v35\n"
            "v_mul_u32_u24_sdwa v30, v34, v39 dst_sel:DWORD dst_unused:UNUSED_PAD src0_sel:BYTE_3 src1_sel:DWORD\n"
            "v_mul_u32_u24_sdwa v31, v38, v39 dst_sel:DWORD dst_unused:UNUSED_PAD src0_sel:BYTE_3 src1_sel:DWORD\n"
            "v_and_or_b32 v30, v30, s90, v18\n"
            "v_and_or_b32 v31, v31, s90, v18\n"
            "s_or_b64 s[84:85], vcc, s[88:89]\n"
            "s_cbranch_scc0 Lnoplan%=\n"
            "s_mov_b64 exec, vcc\n ds_write_b32 v26, v30 offset:16\n s_mov_b64 exec, s[88:89]\n ds_write_b32 v27, v31 offset:16\n s_mov_b64 exec, -1\n"
            "v_cmp_lt_u32 vcc, 5, v20\n v_cmp_lt_u32 s[88:89], 5, v21\n"
            "v_and_b32 v35, 512, v19\n v_add_u32 v18, 2097152, v18\n v_lshrrev_b32 v19, 1, v19\n v_xor_b32 v18, v18, v35\n"
            "v_mul_u32_u24_sdwa v30, v34, v39 dst_sel:DWORD dst_unused:UNUSED_PAD src0_sel:BYTE_2 src1_sel:DWORD\n"
            "v_mul_u32_u24_sdwa v31, v38, v39 dst_sel:DWORD dst_unused:UNUSED_PAD src0_sel:BYTE_2 src1_sel:DWORD\n"
            "v_and_or_b32 v30, v30, s90, v18\n"
            "v_and_or_b32 v31, v31, s90, v18\n"
            "s_or_b64 s[84:85], vcc, s[88:89]\n"
            "s_cbranch_scc0 Lnoplan%=\n"
            "s_mov_b64 exec, vcc\n ds_write_b32 v26, v30 offset:20\n s_mov_b64 exec, s[88:89]\n ds_write_b32 v27, v31 offset:20\n s_mov_b64 exec, -1\n"
            "v_cmp_lt_u32 vcc, 6, v20\n v_cmp_lt_u32 s[88:89], 6, v21\n"
            "v_and_b32 v35, 512, v19\n v_add_u32 v18, 2097152, v18\n v_lshrrev_b32 v19, 1, v19\n v_xor_b32 v18, v18, v35\n"
            "v_mul_u32_u24_sdwa v30, v34, v39 dst_sel:DWORD dst_unused:UNUSED_PAD src0_sel:BYTE_1 src1_sel:DWORD\n"
            "v_mul_u32_u24_sdwa v31, v38, v39 dst_sel:DWORD dst_unused:UNUSED_PAD src0_sel:BYTE_1 src1_sel:DWORD\n"
            "v_and_or_b32 v30, v30, s90, v18\n"
            "v_and_or_b32 v31, v31, s90, v18\n"
            "s_or_b64 s[84:85], vcc, s[88:89]\n"
            "s_cbranch_scc0 Lnoplan%=\n"
            "s_mov_b64 exec, vcc\n ds_write_b32 v26, v30 offset:24\n s_mov_b64 exec, s[88:89]\n ds_write_b32 v27, v31 offset:24\n s_mov_b64 exec, -1\n"
            "v_cmp_lt_u32 vcc, 7, v20\n v_cmp_lt_u32 s[88:89], 7, v21\n"
            "v_and_b32 v35, 512, v19\n v_add_u32 v18, 2097152, v18\n v_lshrrev_b32 v19, 1, v19\n v_xor_b32 v18, v18, v35\n"
            "v_mul_u32_u24_sdwa v30, v34, v39 dst_sel:DWORD dst_unused:UNUSED_PAD src0_sel:BYTE_0 src1_sel:DWORD\n"
            "v_mul_u32_u24_sdwa v31, v38, v39 dst_sel:DWORD dst_unused:UNUSED_PAD src0_sel:BYTE_0 src1_sel:DWORD\n"
            "v_and_or_b32 v30, v30, s90, v18\n"
            "v_and_or_b32 v31, v31, s90, v18\n"
            "s_or_b64 s[84:85], vcc, s[88:89]\n"
            "s_cbranch_scc0 Lnoplan%=\n"
            "s_mov_b64 exec, vcc\n ds_write_b32 v26, v30 offset:28\n s_mov_b64 exec, s[88:89]\n ds_write_b32 v27, v31 offset:28\n s_mov_b64 exec, -1\n"
            "s_mov_b32 s90, 8\n"
            "v_or_b32 v18, v18, %[hm]\n"
            "3:\n"
            "v_cmp_lt_u32 vcc, s90, v20\n v_cmp_lt_u32 s[88:89], s90, v21\n"
            "v_add_u32 v18, 2097152, v18\n"
            "s_add_u32 s90, s90, 1\n"
            "v_xor_b32 v31, 64, v18\n"
            "s_or_b64 s[84:85], vcc, s[88:89]\n"
            "s_cbranch_scc0 Lnoplan%=\n"
            "s_mov_b64 exec, vcc\n ds_write_b32 v26, v18 offset:32\n s_mov_b64 exec, s[88:89]\n ds_write_b32 v27, v31 offset:32\n s_mov_b64 exec, -1\n"
            "v_add_u32 v26, 4, v26\n v_add_u32 v27, 4, v27\n"
            "s_branch 3b\n"
            "Lnoplan%=:\n"
            "ds_read_b32 v60, v17 offset:0\n"
            "ds_read_b32 v61, v17 offset:256\n"
            "ds_read_b32 v62, v17 offset:512\n"
            "ds_read_b32 v63, v17 offset:768\n"
            "ds_read_b32 v64, v17 offset:1024\n"
            "ds_read_b32 v65, v17 offset:1280\n"
            "ds_read_b32 v66, v17 offset:1536\n"
            "ds_read_b32 v67, v17 offset:1792\n"
            "ds_read_b32 v68, v17 offset:2048\n"
            "ds_read_b32 v69, v17 offset:2304\n"
            "ds_read_b32 v70, v17 offset:2560\n"
            "ds_read_b32 v71, v17 offset:2816\n"
            "ds_read_b32 v72, v17 offset:3072\n"
            "ds_read_b32 v73, v17 offset:3328\n"
            "ds_read_b32 v74, v17 offset:3584\n"
            "ds_read_b32 v75, v17 offset:3840\n"
            "ds_read_b32 v76, v17 offset:4096\n"
            "ds_read_b32 v77, v17 offset:4352\n"
            "ds_read_b32 v78, v17 offset:4608\n"
            "ds_read_b32 v79, v17 offset:4864\n"
            "v_mov_b32 v100, 0x8000\n"
            "v_mov_b32 v101, 0x8000\n"
            "v_mov_b32 v102, 0x8000\n"
            "v_mov_b32 v103, 0x8000\n"
            "v_mov_b32 v107, v11\n"
            "s_mov_b32 s82, 0\n"
            "s_waitcnt lgkmcnt(15)\n"
            "v_mov_b32 v81, v60\n"
            "s_nop 1\n"
            "v_add_u32_dpp v111, v81, v82 row_newbcast:0 row_mask:0xf bank_mask:0x3\n"
            "v_add_u32_dpp v111, v81, v82 row_newbcast:8 row_mask:0xf bank_mask:0xc\n"
            "v_and_b32 v111, " IDXMASK ", v111\n"
            "v_min_u32 v111, v111, v83\n"
            "buffer_load_dwordx4 v[8:11], v111, %[rs], 0 idxen " RAUX "\n"
            "v_add_u32_dpp v111, v81, v82 row_newbcast:1 row_mask:0xf bank_mask:0x3\n"
            "v_add_u32_dpp v111, v81, v82 row_newbcast:9 row_mask:0xf bank_mask:0xc\n"
            "v_and_b32 v111, " IDXMASK ", v111\n"
            "v_min_u32 v111, v111, v83\n"
            "buffer_load_dwordx4 v[12:15], v111, %[rs], 0 idxen " RAUX "\n"
            "v_add_u32_dpp v111, v81, v82 row_newbcast:2 row_mask:0xf bank_mask:0x3\n"
            "v_add_u32_dpp v111, v81, v82 row_newbcast:10 row_mask:0xf bank_mask:0xc\n"
            "v_and_b32 v111, " IDXMASK ", v111\n"
            "v_min_u32 v111, v111, v83\n"
            "buffer_load_dwordx4 v[16:19], v111, %[rs], 0 idxen " RAUX "\n"
            "v_add_u32_dpp v111, v81, v82 row_newbcast:3 row_mask:0xf bank_mask:0x3\n"
            "v_add_u32_dpp v111, v81, v82 row_newbcast:11 row_mask:0xf bank_mask:0xc\n"
            "v_and_b32 v111, " IDXMASK ", v111\n"
            "v_min_u32 v111, v111, v83\n"
            "buffer_load_dwordx4 v[20:23], v111, %[rs], 0 idxen " RAUX "\n"
            "v_add_u32_dpp v111, v81, v82 row_newbcast:4 row_mask:0xf bank_mask:0x3\n"
            "v_add_u32_dpp v111, v81, v82 row_newbcast:12 row_mask:0xf bank_mask:0xc\n"
            "v_and_b32 v111, " IDXMASK ", v111\n"
            "v_min_u32 v111, v111, v83\n"
            "buffer_load_dwordx4 v[24:27], v111, %[rs], 0 idxen " RAUX "\n"
            "v_add_u32_dpp v111, v81, v82 row_newbcast:5 row_mask:0xf bank_mask:0x3\n"
            "v_add_u32_dpp v111, v81, v82 row_newbcast:13 row_mask:0xf bank_mask:0xc\n"
            "v_and_b32 v111, " IDXMASK ", v111\n"
            "v_min_u32 v111, v111, v83\n"
            "buffer_load_dwordx4 v[28:31], v111, %[rs], 0 idxen " RAUX "\n"
            "v_add_u32_dpp v111, v81, v82 row_newbcast:6 row_mask:0xf bank_mask:0x3\n"
            "v_add_u32_dpp v111, v81, v82 row_newbcast:14 row_mask:0xf bank_mask:0xc\n"
            "v_and_b32 v111, " IDXMASK ", v111\n"
            "v_min_u32 v111, v111, v83\n"
            "buffer_load_dwordx4 v[32:35], v111, %[rs], 0 idxen " RAUX "\n"
            "v_add_u32_dpp v111, v81, v82 row_newbcast:7 row_mask:0xf bank_mask:0x3\n"
            "v_add_u32_dpp v111, v81, v82 row_newbcast:15 row_mask:0xf bank_mask:0xc\n"
            "v_and_b32 v111, " IDXMASK ", v111\n"
            "v_min_u32 v111, v111, v83\n"
            "buffer_load_dwordx4 v[36:39], v111, %[rs], 0 idxen " RAUX "\n"
            "s_min_u32 s82, s82, 19\n"
            "s_add_u32 s82, s82, 1\n"
            "s_set_gpr_idx_on s82, gpr_idx(SRC0)\n"
            "s_nop 0\n"
            "v_mov_b32 v81, v60\n"
            "s_set_gpr_idx_off\n"
            "s_setprio 0\n"
            "ds_write_b128 v107, v[100:103] offset:0\n"
            "ds_write_b128 v107, v[100:103] offset:1024\n"
            "ds_write_b128 v107, v[100:103] offset:2048\n"
            "ds_write_b128 v107, v[100:103] offset:3072\n"
            "ds_write_b128 v107, v[100:103] offset:4096\n"
            "s_waitcnt vmcnt(13)\n"
            "v_pk_max_u16 v92, v92, v94\n v_pk_max_u16 v96, v96, v98\n"
            "v_lshlrev_b32 v104, 16, v93\n v_lshlrev_b32 v105, 16, v95\n v_lshlrev_b32 v106, 16, v97\n v_lshlrev_b32 v108, 16, v99\n"
            "v_pk_max_u16 v92, v92, v96\n"
            "v_add_f32 v104, v104, v105\n v_add_f32 v106, v106, v108\n v_add_f32 v93, v93, v95\n v_add_f32 v97, v97, v99\n"
            "v_and_b32 v94, 0xffff, v92\n v_lshrrev_b32 v92, 16, v92\n"
            "v_add_f32 v104, v104, v106\n v_add_f32 v93, v93, v97\n"
            "s_nop 0\n"
            "v_max_u32_dpp v94, v94, v94 row_shr:1 row_mask:0xf bank_mask:0xf bound_ctrl:1\n"
            "v_max_u32_dpp v92, v92, v92 row_shr:1 row_mask:0xf bank_mask:0xf bound_ctrl:1\n"
            "v_add_f32_dpp v104, v104, v104 row_shr:1 row_mask:0xf bank_mask:0xf bound_ctrl:1\n"
            "v_add_f32_dpp v93, v93, v93 row_shr:1 row_mask:0xf bank_mask:0xf bound_ctrl:1\n"
            "v_max_u32_dpp v94, v94, v94 row_shr:2 row_mask:0xf bank_mask:0xf bound_ctrl:1\n"
            "v_max_u32_dpp v92, v92, v92 row_shr:2 row_mask:0xf bank_mask:0xf bound_ctrl:1\n"
            "v_add_f32_dpp v104, v104, v104 row_shr:2 row_mask:0xf bank_mask:0xf bound_ctrl:1\n"
            "v_add_f32_dpp v93, v93, v93 row_shr:2 row_mask:0xf bank_mask:0xf bound_ctrl:1\n"
            "v_max_u32_dpp v94, v94, v94 row_shr:4 row_mask:0xf bank_mask:0xf bound_ctrl:1\n"
            "v_max_u32_dpp v92, v92, v92 row_shr:4 row_mask:0xf bank_mask:0xf bound_ctrl:1\n"
            "v_add_f32_dpp v104, v104, v104 row_shr:4 row_mask:0xf bank_mask:0xf bound_ctrl:1\n"
            "v_add_f32_dpp v93, v93, v93 row_shr:4 row_mask:0xf bank_mask:0xf bound_ctrl:1\n"
            "v_max_u32_dpp v94, v94, v94 row_shr:8 row_mask:0xf bank_mask:0xf bound_ctrl:1\n"
            "v_max_u32_dpp v92, v92, v92 row_shr:8 row_mask:0xf bank_mask:0xf bound_ctrl:1\n"
            "v_add_f32_dpp v104, v104, v104 row_shr:8 row_mask:0xf bank_mask:0xf bound_ctrl:1\n"
            "v_add_f32_dpp v93, v93, v93 row_shr:8 row_mask:0xf bank_mask:0xf bound_ctrl:1\n"
            "v_max_u32_dpp v94, v94, v94 row_bcast:15 row_mask:0xa bank_mask:0xf\n"
            "v_max_u32_dpp v92, v92, v92 row_bcast:15 row_mask:0xa bank_mask:0xf\n"
            "v_add_f32_dpp v104, v104, v104 row_bcast:15 row_mask:0xa bank_mask:0xf\n"
            "v_add_f32_dpp v93, v93, v93 row_bcast:15 row_mask:0xa bank_mask:0xf\n"
            "v_max_u32_dpp v94, v94, v94 row_bcast:31 row_mask:0xc bank_mask:0xf\n"
            "v_max_u32_dpp v92, v92, v92 row_bcast:31 row_mask:0xc bank_mask:0xf\n"
            "v_add_f32_dpp v104, v104, v104 row_bcast:31 row_mask:0xc bank_mask:0xf\n"
            "v_add_f32_dpp v93, v93, v93 row_bcast:31 row_mask:0xc bank_mask:0xf\n"
            "s_nop 1\n"
            "v_readlane_b32 s84, v94, 63\n v_readlane_b32 s85, v92, 63\n v_readlane_b32 s86, v104, 63\n v_readlane_b32 s87, v93, 63\n"
            "s_mov_b32 %[mode], 0\n"
            "s_max_u32 s96, s84, s85\n s_cmp_ge_u32 s96, 0x7f00\n s_cselect_b32 %[mode], 2, 0\n"
            "s_cmp_eq_u32 s84, 0\n s_cselect_b32 s84, 0x3f80, s84\n s_cselect_b32 s86, 0x7f000000, s86\n"
            "s_cmp_eq_u32 s85, 0\n s_cselect_b32 s85, 0x3f80, s85\n s_cselect_b32 s87, 0x7f000000, s87\n"
            "s_min_u32 s96, s84, s85\n s_cmp_lt_u32 s96, 0x800\n s_cselect_b32 %[mode], 2, %[mode]\n"
            "s_cmp_lg_u32 %[mode], 0\n"
            "s_cbranch_scc1 Lend%=\n"
            "s_lshl_b32 s84, s84, 16\n s_lshl_b32 s85, s85, 16\n"
            "v_mov_b32 v104, s84\n v_mov_b32 v105, s85\n"
            "v_mul_f32 v106, 0x3e82d0e5, v104\n v_mul_f32 v108, 0x3e82d0e5, v105\n"
            "v_mul_f32 v104, v104, v104\n v_mul_f32 v105, v105, v105\n"
            "v_add_u32 v106, 0x7fffff, v106\n v_add_u32 v108, 0x7fffff, v108\n"
            "v_mul_f32 v104, %[c4], v104\n v_mul_f32 v105, %[c4], v105\n"
            "v_and_b32 v106, 0x7f800000, v106\n v_and_b32 v108, 0x7f800000, v108\n"
            "v_cmp_lt_f32 vcc, s86, v104\n v_cmp_lt_f32 s[88:89], s87, v105\n"
            "v_add_u32 v86, 0xc00000, v106\n v_add_u32 v87, 0xc00000, v108\n"
            "v_lshrrev_b32 v106, 23, v106\n v_lshrrev_b32 v108, 15, v108\n"
            "v_or_b32 %[exs], v106, v108\n"
            "s_or_b64 vcc, vcc, s[88:89]\n"
            "s_cselect_b32 %[mode], 1, 0\n"
            "s_cbranch_scc1 Lend%=\n"
            "s_waitcnt vmcnt(8)\n"
            "s_mov_b32 m0, %[wb]\n"
            "s_nop 0\n"
            "ds_write_addtid_b32 v40 offset:0\n"
            "ds_write_addtid_b32 v41 offset:256\n"
            "ds_write_addtid_b32 v42 offset:512\n"
            "ds_write_addtid_b32 v43 offset:768\n"
            "ds_write_addtid_b32 v44 offset:16384\n"
            "ds_write_addtid_b32 v45 offset:16640\n"
            "ds_write_addtid_b32 v46 offset:16896\n"
            "ds_write_addtid_b32 v47 offset:17152\n"
            "ds_write_addtid_b32 v48 offset:32768\n"
            "ds_write_addtid_b32 v49 offset:33024\n"
            "ds_write_addtid_b32 v50 offset:33280\n"
            "ds_write_addtid_b32 v51 offset:33536\n"
            "ds_write_addtid_b32 v52 offset:49152\n"
            "ds_write_addtid_b32 v53 offset:49408\n"
            "ds_write_addtid_b32 v54 offset:49664\n"
            "ds_write_addtid_b32 v55 offset:49920\n"
            "s_add_u32 m0, %[wb], 16384\n"
            "s_nop 0\n"
            "ds_write_addtid_b32 v56 offset:49152\n"
            "ds_write_addtid_b32 v57 offset:49408\n"
            "ds_write_addtid_b32 v58 offset:49664\n"
            "ds_write_addtid_b32 v59 offset:49920\n"
            "s_mov_b32 s92, 0\n"
            "s_and_b32 s75, s76, 255\n"
            "s_movk_i32 s74, 1000\n"
            "s_sub_i32 s79, s75, %[j0]\n"
            "s_max_i32 s79, s79, 8\n"
            "s_cmp_eq_u32 s92, 7\n s_cselect_b32 s79, 8, s79\n"
            "s_setprio 3\n"
            "s_waitcnt lgkmcnt(0)\n"
            "s_barrier\n"
            "s_waitcnt vmcnt(7)\n"
            "v_and_b32 v104, 0x1fffc, v8\n"
            "v_and_b32 v105, 0x1fffc, v9\n"
            "v_and_b32 v106, 0x1fffc, v10\n"
            "ds_read_b32 v93, v104\n"
            "ds_read_b32 v95, v105\n"
            "ds_read_b32 v97, v106\n"
            "s_waitcnt lgkmcnt(0)\n"
            "Ls0_%=:\n"
            "s_cmp_gt_i32 s75, s74\n"
            "s_cbranch_scc1 1f\n"
            "s_waitcnt vmcnt(6)\n"
            "1:\n"
            "s_waitcnt vmcnt(11)\n"
            "s_cmp_eq_u32 s75, s79\n"
            "s_cbranch_scc1 Le0_%=\n"
            "Lc0_%=:\n"
            "v_and_b32 v104, 0x1fffc, v12\n"
            "v_and_b32 v105, 0x1fffc, v13\n"
            "v_and_b32 v106, 0x1fffc, v14\n"
            "ds_read_b32 v99, v104\n"
            "ds_read_b32 v101, v105\n"
            "ds_read_b32 v103, v106\n"
            "v_add_u32_dpp v111, v81, v82 row_newbcast:0 row_mask:0xf bank_mask:0x3\n"
            "v_add_u32_dpp v111, v81, v82 row_newbcast:8 row_mask:0xf bank_mask:0xc\n"
            "v_and_b32 v108, 0x7fe, v11\n"
            "v_and_b32 v109, 0x1ff800, v11\n"
            "v_lshrrev_b32 v110, 22, v11\n"
            "v_lshrrev_b32 v112, 15, v8\n"
            "v_lshrrev_b32 v113, 15, v9\n"
            "v_lshrrev_b32 v114, 15, v10\n"
            "v_and_b32 v111, " IDXMASK ", v111\n"
            "v_min_u32 v111, v111, v83\n"
            "buffer_load_dwordx4 v[8:11], v111, %[rs], 0 idxen " RAUX "\n"
            "v_fmamk_f32 v108, v108, 0x7e800000, v84\n"
            "v_fmamk_f32 v109, v109, 0x79800000, v84\n"
            "v_fmamk_f32 v110, v110, 0x7f000000, v84\n"
            "v_or_b32 v112, 3, v112\n"
            "v_or_b32 v113, 3, v113\n"
            "v_or_b32 v114, 3, v114\n"
            "s_sub_u32 s75, s75, 1\n"
            "s_waitcnt lgkmcnt(6)\n"
            "v_lshlrev_b32 v92, 16, v93\n"
            "v_lshlrev_b32 v94, 16, v95\n"
            "v_lshlrev_b32 v96, 16, v97\n"
            "v_pk_fma_f32 v[92:93], v[92:93], v[108:109], v[86:87] op_sel:[0,0,0] op_sel_hi:[1,0,1]\n"
            "v_pk_fma_f32 v[94:95], v[94:95], v[108:109], v[86:87] op_sel:[0,1,0] op_sel_hi:[1,1,1]\n"
            "v_pk_fma_f32 v[96:97], v[96:97], v[110:111], v[86:87] op_sel:[0,0,0] op_sel_hi:[1,0,1]\n"
            "v_lshl_add_u32 v92, v93, 16, v92\n"
            "v_lshl_add_u32 v94, v95, 16, v94\n"
            "v_lshl_add_u32 v96, v97, 16, v96\n"
            "v_sub_u32 v92, v92, v86\n"
            "v_mad_i32_i16 v88, v93, v112, v88\n"
            "ds_add_u32 v112, v92 offset:65533\n"
            "v_sub_u32 v94, v94, v86\n"
            "v_mad_i32_i16 v88, v95, v113, v88\n"
            "ds_add_u32 v113, v94 offset:65533\n"
            "v_sub_u32 v96, v96, v86\n"
            "v_mad_i32_i16 v88, v97, v114, v88\n"
            "ds_add_u32 v114, v96 offset:65533\n"
            "Ls1_%=:\n"
            "s_cmp_gt_i32 s75, s74\n"
            "s_cbranch_scc1 1f\n"
            "s_waitcnt vmcnt(6)\n"
            "1:\n"
            "s_waitcnt vmcnt(11)\n"
            "s_cmp_eq_u32 s75, s79\n"
            "s_cbranch_scc1 Le1_%=\n"
            "Lc1_%=:\n"
            "v_and_b32 v104, 0x1fffc, v16\n"
            "v_and_b32 v105, 0x1fffc, v17\n"
            "v_and_b32 v106, 0x1fffc, v18\n"
            "ds_read_b32 v93, v104\n"
            "ds_read_b32 v95, v105\n"
            "ds_read_b32 v97, v106\n"
            "v_add_u32_dpp v111, v81, v82 row_newbcast:1 row_mask:0xf bank_mask:0x3\n"
            "v_add_u32_dpp v111, v81, v82 row_newbcast:9 row_mask:0xf bank_mask:0xc\n"
            "v_and_b32 v108, 0x7fe, v15\n"
            "v_and_b32 v109, 0x1ff800, v15\n"
            "v_lshrrev_b32 v110, 22, v15\n"
            "v_lshrrev_b32 v112, 15, v12\n"
            "v_lshrrev_b32 v113, 15, v13\n"
            "v_lshrrev_b32 v114, 15, v14\n"
            "v_and_b32 v111, " IDXMASK ", v111\n"
            "v_min_u32 v111, v111, v83\n"
            "buffer_load_dwordx4 v[12:15], v111, %[rs], 0 idxen " RAUX "\n"
            "v_fmamk_f32 v108, v108, 0x7e800000, v84\n"
            "v_fmamk_f32 v109, v109, 0x79800000, v84\n"
            "v_fmamk_f32 v110, v110, 0x7f000000, v84\n"
            "v_or_b32 v112, 3, v112\n"
            "v_or_b32 v113, 3, v113\n"
            "v_or_b32 v114, 3, v114\n"
            "s_sub_u32 s75, s75, 1\n"
            "s_waitcnt lgkmcnt(6)\n"
            "v_lshlrev_b32 v98, 16, v99\n"
            "v_lshlrev_b32 v100, 16, v101\n"
            "v_lshlrev_b32 v102, 16, v103\n"
            "v_pk_fma_f32 v[98:99], v[98:99], v[108:109], v[86:87] op_sel:[0,0,0] op_sel_hi:[1,0,1]\n"
            "v_pk_fma_f32 v[100:101], v[100:101], v[108:109], v[86:87] op_sel:[0,1,0] op_sel_hi:[1,1,1]\n"
            "v_pk_fma_f32 v[102:103], v[102:103], v[110:111], v[86:87] op_sel:[0,0,0] op_sel_hi:[1,0,1]\n"
            "v_lshl_add_u32 v98, v99, 16, v98\n"
            "v_lshl_add_u32 v100, v101, 16, v100\n"
            "v_lshl_add_u32 v102, v103, 16, v102\n"
            "v_sub_u32 v98, v98, v86\n"
            "v_mad_i32_i16 v88, v99, v112, v88\n"
            "ds_add_u32 v112, v98 offset:65533\n"
            "v_sub_u32 v100, v100, v86\n"
            "v_mad_i32_i16 v88, v101, v113, v88\n"
            "ds_add_u32 v113, v100 offset:65533\n"
            "v_sub_u32 v102, v102, v86\n"
            "v_mad_i32_i16 v88, v103, v114, v88\n"
            "ds_add_u32 v114, v102 offset:65533\n"
            "Ls2_%=:\n"
            "s_cmp_gt_i32 s75, s74\n"
            "s_cbranch_scc1 1f\n"
            "s_waitcnt vmcnt(6)\n"
            "1:\n"
            "s_waitcnt vmcnt(11)\n"
            "s_cmp_eq_u32 s75, s79\n"
            "s_cbranch_scc1 Le2_%=\n"
            "Lc2_%=:\n"
            "v_and_b32 v104, 0x1fffc, v20\n"
            "v_and_b32 v105, 0x1fffc, v21\n"
            "v_and_b32 v106, 0x1fffc, v22\n"
            "ds_read_b32 v99, v104\n"
            "ds_read_b32 v101, v105\n"
            "ds_read_b32 v103, v106\n"
            "v_add_u32_dpp v111, v81, v82 row_newbcast:2 row_mask:0xf bank_mask:0x3\n"
            "v_add_u32_dpp v111, v81, v82 row_newbcast:10 row_mask:0xf bank_mask:0xc\n"
            "v_and_b32 v108, 0x7fe, v19\n"
            "v_and_b32 v109, 0x1ff800, v19\n"
            "v_lshrrev_b32 v110, 22, v19\n"
            "v_lshrrev_b32 v112, 15, v16\n"
            "v_lshrrev_b32 v113, 15, v17\n"
            "v_lshrrev_b32 v114, 15, v18\n"
            "v_and_b32 v111, " IDXMASK ", v111\n"
            "v_min_u32 v111, v111, v83\n"
            "buffer_load_dwordx4 v[16:19], v111, %[rs], 0 idxen " RAUX "\n"
            "v_fmamk_f32 v108, v108, 0x7e800000, v84\n"
            "v_fmamk_f32 v109, v109, 0x79800000, v84\n"
            "v_fmamk_f32 v110, v110, 0x7f000000, v84\n"
            "v_or_b32 v112, 3, v112\n"
            "v_or_b32 v113, 3, v113\n"
            "v_or_b32 v114, 3, v114\n"
            "s_sub_u32 s75, s75, 1\n"
            "s_waitcnt lgkmcnt(6)\n"
            "v_lshlrev_b32 v92, 16, v93\n"
            "v_lshlrev_b32 v94, 16, v95\n"
            "v_lshlrev_b32 v96, 16, v97\n"
            "v_pk_fma_f32 v[92:93], v[92:93], v[108:109], v[86:87] op_sel:[0,0,0] op_sel_hi:[1,0,1]\n"
            "v_pk_fma_f32 v[94:95], v[94:95], v[108:109], v[86:87] op_sel:[0,1,0] op_sel_hi:[1,1,1]\n"
            "v_pk_fma_f32 v[96:97], v[96:97], v[110:111], v[86:87] op_sel:[0,0,0] op_sel_hi:[1,0,1]\n"
            "v_lshl_add_u32 v92, v93, 16, v92\n"
            "v_lshl_add_u32 v94, v95, 16, v94\n"
            "v_lshl_add_u32 v96, v97, 16, v96\n"
            "v_sub_u32 v92, v92, v86\n"
            "v_mad_i32_i16 v88, v93, v112, v88\n"
            "ds_add_u32 v112, v92 offset:65533\n"
            "v_sub_u32 v94, v94, v86\n"
            "v_mad_i32_i16 v88, v95, v113, v88\n"
            "ds_add_u32 v113, v94 offset:65533\n"
            "v_sub_u32 v96, v96, v86\n"
            "v_mad_i32_i16 v88, v97, v114, v88\n"
            "ds_add_u32 v114, v96 offset:65533\n"
            "Ls3_%=:\n"
            "s_cmp_gt_i32 s75, s74\n"
            "s_cbranch_scc1 1f\n"
            "s_waitcnt vmcnt(6)\n"
            "1:\n"
            "s_waitcnt vmcnt(11)\n"
            "s_cmp_eq_u32 s75, s79\n"
            "s_cbranch_scc1 Le3_%=\n"
            "Lc3_%=:\n"
            "v_and_b32 v104, 0x1fffc, v24\n"
            "v_and_b32 v105, 0x1fffc, v25\n"
            "v_and_b32 v106, 0x1fffc, v26\n"
            "ds_read_b32 v93, v104\n"
            "ds_read_b32 v95, v105\n"
            "ds_read_b32 v97, v106\n"
            "v_add_u32_dpp v111, v81, v82 row_newbcast:3 row_mask:0xf bank_mask:0x3\n"
            "v_add_u32_dpp v111, v81, v82 row_newbcast:11 row_mask:0xf bank_mask:0xc\n"
            "v_and_b32 v108, 0x7fe, v23\n"
            "v_and_b32 v109, 0x1ff800, v23\n"
            "v_lshrrev_b32 v110, 22, v23\n"
            "v_lshrrev_b32 v112, 15, v20\n"
            "v_lshrrev_b32 v113, 15, v21\n"
            "v_lshrrev_b32 v114, 15, v22\n"
            "v_and_b32 v111, " IDXMASK ", v111\n"
            "v_min_u32 v111, v111, v83\n"
            "buffer_load_dwordx4 v[20:23], v111, %[rs], 0 idxen " RAUX "\n"
            "v_fmamk_f32 v108, v108, 0x7e800000, v84\n"
            "v_fmamk_f32 v109, v109, 0x79800000, v84\n"
            "v_fmamk_f32 v110, v110, 0x7f000000, v84\n"
            "v_or_b32 v112, 3, v112\n"
            "v_or_b32 v113, 3, v113\n"
            "v_or_b32 v114, 3, v114\n"
            "s_sub_u32 s75, s75, 1\n"
            "s_waitcnt lgkmcnt(6)\n"
            "v_lshlrev_b32 v98, 16, v99\n"
            "v_lshlrev_b32 v100, 16, v101\n"
            "v_lshlrev_b32 v102, 16, v103\n"
            "v_pk_fma_f32 v[98:99], v[98:99], v[108:109], v[86:87] op_sel:[0,0,0] op_sel_hi:[1,0,1]\n"
            "v_pk_fma_f32 v[100:101], v[100:101], v[108:109], v[86:87] op_sel:[0,1,0] op_sel_hi:[1,1,1]\n"
            "v_pk_fma_f32 v[102:103], v[102:103], v[110:111], v[86:87] op_sel:[0,0,0] op_sel_hi:[1,0,1]\n"
            "v_lshl_add_u32 v98, v99, 16, v98\n"
            "v_lshl_add_u32 v100, v101, 16, v100\n"
            "v_lshl_add_u32 v102, v103, 16, v102\n"
            "v_sub_u32 v98, v98, v86\n"
            "v_mad_i32_i16 v88, v99, v112, v88\n"
            "ds_add_u32 v112, v98 offset:65533\n"
            "v_sub_u32 v100, v100, v86\n"
            "v_mad_i32_i16 v88, v101, v113, v88\n"
            "ds_add_u32 v113, v100 offset:65533\n"
            "v_sub_u32 v102, v102, v86\n"
            "v_mad_i32_i16 v88, v103, v114, v88\n"
            "ds_add_u32 v114, v102 offset:65533\n"
            "Ls4_%=:\n"
            "s_cmp_gt_i32 s75, s74\n"
            "s_cbranch_scc1 1f\n"
            "s_waitcnt vmcnt(6)\n"
            "1:\n"
            "s_waitcnt vmcnt(11)\n"
            "s_cmp_eq_u32 s75, s79\n"
            "s_cbranch_scc1 Le4_%=\n"
            "Lc4_%=:\n"
            "v_and_b32 v104, 0x1fffc, v28\n"
            "v_and_b32 v105, 0x1fffc, v29\n"
            "v_and_b32 v106, 0x1fffc, v30\n"
            "ds_read_b32 v99, v104\n"
            "ds_read_b32 v101, v105\n"
            "ds_read_b32 v103, v106\n"
            "v_add_u32_dpp v111, v81, v82 row_newbcast:4 row_mask:0xf bank_mask:0x3\n"
            "v_add_u32_dpp v111, v81, v82 row_newbcast:12 row_mask:0xf bank_mask:0xc\n"
            "v_and_b32 v108, 0x7fe, v27\n"
            "v_and_b32 v109, 0x1ff800, v27\n"
            "v_lshrrev_b32 v110, 22, v27\n"
            "v_lshrrev_b32 v112, 15, v24\n"
            "v_lshrrev_b32 v113, 15, v25\n"
            "v_lshrrev_b32 v114, 15, v26\n"
            "v_and_b32 v111, " IDXMASK ", v111\n"
            "v_min_u32 v111, v111, v83\n"
            "buffer_load_dwordx4 v[24:27], v111, %[rs], 0 idxen " RAUX "\n"
            "v_fmamk_f32 v108, v108, 0x7e800000, v84\n"
            "v_fmamk_f32 v109, v109, 0x79800000, v84\n"
            "v_fmamk_f32 v110, v110, 0x7f000000, v84\n"
            "v_or_b32 v112, 3, v112\n"
            "v_or_b32 v113, 3, v113\n"
            "v_or_b32 v114, 3, v114\n"
            "s_sub_u32 s75, s75, 1\n"
            "s_waitcnt lgkmcnt(6)\n"
            "v_lshlrev_b32 v92, 16, v93\n"
            "v_lshlrev_b32 v94, 16, v95\n"
            "v_lshlrev_b32 v96, 16, v97\n"
            "v_pk_fma_f32 v[92:93], v[92:93], v[108:109], v[86:87] op_sel:[0,0,0] op_sel_hi:[1,0,1]\n"
            "v_pk_fma_f32 v[94:95], v[94:95], v[108:109], v[86:87] op_sel:[0,1,0] op_sel_hi:[1,1,1]\n"
            "v_pk_fma_f32 v[96:97], v[96:97], v[110:111], v[86:87] op_sel:[0,0,0] op_sel_hi:[1,0,1]\n"
            "v_lshl_add_u32 v92, v93, 16, v92\n"
            "v_lshl_add_u32 v94, v95, 16, v94\n"
            "v_lshl_add_u32 v96, v97, 16, v96\n"
            "v_sub_u32 v92, v92, v86\n"
            "v_mad_i32_i16 v88, v93, v112, v88\n"
            "ds_add_u32 v112, v92 offset:65533\n"
            "v_sub_u32 v94, v94, v86\n"
            "v_mad_i32_i16 v88, v95, v113, v88\n"
            "ds_add_u32 v113, v94 offset:65533\n"
            "v_sub_u32 v96, v96, v86\n"
            "v_mad_i32_i16 v88, v97, v114, v88\n"
            "ds_add_u32 v114, v96 offset:65533\n"
            "Ls5_%=:\n"
            "s_cmp_gt_i32 s75, s74\n"
            "s_cbranch_scc1 1f\n"
            "s_waitcnt vmcnt(6)\n"
            "1:\n"
            "s_waitcnt vmcnt(11)\n"
            "s_cmp_eq_u32 s75, s79\n"
            "s_cbranch_scc1 Le5_%=\n"
            "Lc5_%=:\n"
            "v_and_b32 v104, 0x1fffc, v32\n"
            "v_and_b32 v105, 0x1fffc, v33\n"
            "v_and_b32 v106, 0x1fffc, v34\n"
            "ds_read_b32 v93, v104\n"
            "ds_read_b32 v95, v105\n"
            "ds_read_b32 v97, v106\n"
            "v_add_u32_dpp v111, v81, v82 row_newbcast:5 row_mask:0xf bank_mask:0x3\n"
            "v_add_u32_dpp v111, v81, v82 row_newbcast:13 row_mask:0xf bank_mask:0xc\n"
            "v_and_b32 v108, 0x7fe, v31\n"
            "v_and_b32 v109, 0x1ff800, v31\n"
            "v_lshrrev_b32 v110, 22, v31\n"
            "v_lshrrev_b32 v112, 15, v28\n"
            "v_lshrrev_b32 v113, 15, v29\n"
            "v_lshrrev_b32 v114, 15, v30\n"
            "v_and_b32 v111, " IDXMASK ", v111\n"
            "v_min_u32 v111, v111, v83\n"
            "buffer_load_dwordx4 v[28:31], v111, %[rs], 0 idxen " RAUX "\n"
            "v_fmamk_f32 v108, v108, 0x7e800000, v84\n"
            "v_fmamk_f32 v109, v109, 0x79800000, v84\n"
            "v_fmamk_f32 v110, v110, 0x7f000000, v84\n"
            "v_or_b32 v112, 3, v112\n"
            "v_or_b32 v113, 3, v113\n"
            "v_or_b32 v114, 3, v114\n"
            "s_sub_u32 s75, s75, 1\n"
            "s_waitcnt lgkmcnt(6)\n"
            "v_lshlrev_b32 v98, 16, v99\n"
            "v_lshlrev_b32 v100, 16, v101\n"
            "v_lshlrev_b32 v102, 16, v103\n"
            "v_pk_fma_f32 v[98:99], v[98:99], v[108:109], v[86:87] op_sel:[0,0,0] op_sel_hi:[1,0,1]\n"
            "v_pk_fma_f32 v[100:101], v[100:101], v[108:109], v[86:87] op_sel:[0,1,0] op_sel_hi:[1,1,1]\n"
            "v_pk_fma_f32 v[102:103], v[102:103], v[110:111], v[86:87] op_sel:[0,0,0] op_sel_hi:[1,0,1]\n"
            "v_lshl_add_u32 v98, v99, 16, v98\n"
            "v_lshl_add_u32 v100, v101, 16, v100\n"
            "v_lshl_add_u32 v102, v103, 16, v102\n"
            "v_sub_u32 v98, v98, v86\n"
            "v_mad_i32_i16 v88, v99, v112, v88\n"
            "ds_add_u32 v112, v98 offset:65533\n"
            "v_sub_u32 v100, v100, v86\n"
            "v_mad_i32_i16 v88, v101, v113, v88\n"
            "ds_add_u32 v113, v100 offset:65533\n"
            "v_sub_u32 v102, v102, v86\n"
            "v_mad_i32_i16 v88, v103, v114, v88\n"
            "ds_add_u32 v114, v102 offset:65533\n"
            "Ls6_%=:\n"
            "s_cmp_gt_i32 s75, s74\n"
            "s_cbranch_scc1 1f\n"
            "s_waitcnt vmcnt(6)\n"
            "1:\n"
            "s_waitcnt vmcnt(11)\n"
            "s_cmp_eq_u32 s75, s79\n"
            "s_cbranch_scc1 Le6_%=\n"
            "Lc6_%=:\n"
            "v_and_b32 v104, 0x1fffc, v36\n"
            "v_and_b32 v105, 0x1fffc, v37\n"
            "v_and_b32 v106, 0x1fffc, v38\n"
            "ds_read_b32 v99, v104\n"
            "ds_read_b32 v101, v105\n"
            "ds_read_b32 v103, v106\n"
            "v_add_u32_dpp v111, v81, v82 row_newbcast:6 row_mask:0xf bank_mask:0x3\n"
            "v_add_u32_dpp v111, v81, v82 row_newbcast:14 row_mask:0xf bank_mask:0xc\n"
            "v_and_b32 v108, 0x7fe, v35\n"
            "v_and_b32 v109, 0x1ff800, v35\n"
            "v_lshrrev_b32 v110, 22, v35\n"
            "v_lshrrev_b32 v112, 15, v32\n"
            "v_lshrrev_b32 v113, 15, v33\n"
            "v_lshrrev_b32 v114, 15, v34\n"
            "v_and_b32 v111, " IDXMASK ", v111\n"
            "v_min_u32 v111, v111, v83\n"
            "buffer_load_dwordx4 v[32:35], v111, %[rs], 0 idxen " RAUX "\n"
            "v_fmamk_f32 v108, v108, 0x7e800000, v84\n"
            "v_fmamk_f32 v109, v109, 0x79800000, v84\n"
            "v_fmamk_f32 v110, v110, 0x7f000000, v84\n"
            "v_or_b32 v112, 3, v112\n"
            "v_or_b32 v113, 3, v113\n"
            "v_or_b32 v114, 3, v114\n"
            "s_sub_u32 s75, s75, 1\n"
            "s_waitcnt lgkmcnt(6)\n"
            "v_lshlrev_b32 v92, 16, v93\n"
            "v_lshlrev_b32 v94, 16, v95\n"
            "v_lshlrev_b32 v96, 16, v97\n"
            "v_pk_fma_f32 v[92:93], v[92:93], v[108:109], v[86:87] op_sel:[0,0,0] op_sel_hi:[1,0,1]\n"
            "v_pk_fma_f32 v[94:95], v[94:95], v[108:109], v[86:87] op_sel:[0,1,0] op_sel_hi:[1,1,1]\n"
            "v_pk_fma_f32 v[96:97], v[96:97], v[110:111], v[86:87] op_sel:[0,0,0] op_sel_hi:[1,0,1]\n"
            "v_lshl_add_u32 v92, v93, 16, v92\n"
            "v_lshl_add_u32 v94, v95, 16, v94\n"
            "v_lshl_add_u32 v96, v97, 16, v96\n"
            "v_sub_u32 v92, v92, v86\n"
            "v_mad_i32_i16 v88, v93, v112, v88\n"
            "ds_add_u32 v112, v92 offset:65533\n"
            "v_sub_u32 v94, v94, v86\n"
            "v_mad_i32_i16 v88, v95, v113, v88\n"
            "ds_add_u32 v113, v94 offset:65533\n"
            "v_sub_u32 v96, v96, v86\n"
            "v_mad_i32_i16 v88, v97, v114, v88\n"
            "ds_add_u32 v114, v96 offset:65533\n"
            "Ls7_%=:\n"
            "s_cmp_gt_i32 s75, s74\n"
            "s_cbranch_scc1 1f\n"
            "s_waitcnt vmcnt(6)\n"
            "1:\n"
            "s_waitcnt vmcnt(11)\n"
            "s_cmp_eq_u32 s75, s79\n"
            "s_cbranch_scc1 Le7_%=\n"
            "Lc7_%=:\n"
            "v_and_b32 v104, 0x1fffc, v8\n"
            "v_and_b32 v105, 0x1fffc, v9\n"
            "v_and_b32 v106, 0x1fffc, v10\n"
            "ds_read_b32 v93, v104\n"
            "ds_read_b32 v95, v105\n"
            "ds_read_b32 v97, v106\n"
            "v_add_u32_dpp v111, v81, v82 row_newbcast:7 row_mask:0xf bank_mask:0x3\n"
            "v_add_u32_dpp v111, v81, v82 row_newbcast:15 row_mask:0xf bank_mask:0xc\n"
            "v_and_b32 v108, 0x7fe, v39\n"
            "v_and_b32 v109, 0x1ff800, v39\n"
            "v_lshrrev_b32 v110, 22, v39\n"
            "v_lshrrev_b32 v112, 15, v36\n"
            "v_lshrrev_b32 v113, 15, v37\n"
            "v_lshrrev_b32 v114, 15, v38\n"
            "v_and_b32 v111, " IDXMASK ", v111\n"
            "v_min_u32 v111, v111, v83\n"
            "buffer_load_dwordx4 v[36:39], v111, %[rs], 0 idxen " RAUX "\n"
            "s_min_u32 s82, s82, 19\n"
            "s_add_u32 s82, s82, 1\n"
            "s_set_gpr_idx_on s82, gpr_idx(SRC0)\n"
            "s_nop 0\n"
            "v_mov_b32 v81, v60\n"
            "s_set_gpr_idx_off\n"
            "v_fmamk_f32 v108, v108, 0x7e800000, v84\n"
            "v_fmamk_f32 v109, v109, 0x79800000, v84\n"
            "v_fmamk_f32 v110, v110, 0x7f000000, v84\n"
            "v_or_b32 v112, 3, v112\n"
            "v_or_b32 v113, 3, v113\n"
            "v_or_b32 v114, 3, v114\n"
            "s_sub_u32 s75, s75, 1\n"
            "s_waitcnt lgkmcnt(6)\n"
            "v_lshlrev_b32 v98, 16, v99\n"
            "v_lshlrev_b32 v100, 16, v101\n"
            "v_lshlrev_b32 v102, 16, v103\n"
            "v_pk_fma_f32 v[98:99], v[98:99], v[108:109], v[86:87] op_sel:[0,0,0] op_sel_hi:[1,0,1]\n"
            "v_pk_fma_f32 v[100:101], v[100:101], v[108:109], v[86:87] op_sel:[0,1,0] op_sel_hi:[1,1,1]\n"
            "v_pk_fma_f32 v[102:103], v[102:103], v[110:111], v[86:87] op_sel:[0,0,0] op_sel_hi:[1,0,1]\n"
            "v_lshl_add_u32 v98, v99, 16, v98\n"
            "v_lshl_add_u32 v100, v101, 16, v100\n"
            "v_lshl_add_u32 v102, v103, 16, v102\n"
            "v_sub_u32 v98, v98, v86\n"
            "v_mad_i32_i16 v88, v99, v112, v88\n"
            "ds_add_u32 v112, v98 offset:65533\n"
            "v_sub_u32 v100, v100, v86\n"
            "v_mad_i32_i16 v88, v101, v113, v88\n"
            "ds_add_u32 v113, v100 offset:65533\n"
            "v_sub_u32 v102, v102, v86\n"
            "v_mad_i32_i16 v88, v103, v114, v88\n"
            "ds_add_u32 v114, v102 offset:65533\n"
            "s_branch Ls0_%=\n"
            "Le0_%=:\n"
            "s_cmp_lt_u32 s75, 8\n"
            "s_cbranch_scc1 1f\n"
            "s_swappc_b64 s[80:81], s[66:67]\n"
            "s_branch Lc0_%=\n"
            "1:\n"
            "s_cmp_lg_u32 s75, 7\n"
            "s_cbranch_scc1 1f\n"
            "s_setprio 2\n"
            "s_mov_b32 s79, 3\n"
            "s_branch Lc0_%=\n"
            "1:\n"
            "s_cmp_lg_u32 s75, 3\n"
            "s_cbranch_scc1 1f\n"
            "s_setprio 1\n"
            "s_mov_b32 s79, 1\n"
            "s_branch Lc0_%=\n"
            "1:\n"
            "s_setprio 0\n"
            "v_add_u32_dpp v111, v81, v82 row_newbcast:0 row_mask:0xf bank_mask:0x3\n"
            "v_add_u32_dpp v111, v81, v82 row_newbcast:8 row_mask:0xf bank_mask:0xc\n"
            "v_and_b32 v108, 0x7fe, v11\n"
            "v_and_b32 v109, 0x1ff800, v11\n"
            "v_lshrrev_b32 v110, 22, v11\n"
            "v_lshrrev_b32 v112, 15, v8\n"
            "v_lshrrev_b32 v113, 15, v9\n"
            "v_lshrrev_b32 v114, 15, v10\n"
            "v_and_b32 v111, " IDXMASK ", v111\n"
            "v_min_u32 v111, v111, v83\n"
            "buffer_load_dwordx4 v[8:11], v111, %[rs], 0 idxen " RAUX "\n"
            "v_fmamk_f32 v108, v108, 0x7e800000, v84\n"
            "v_fmamk_f32 v109, v109, 0x79800000, v84\n"
            "v_fmamk_f32 v110, v110, 0x7f000000, v84\n"
            "v_or_b32 v112, 3, v112\n"
            "v_or_b32 v113, 3, v113\n"
            "v_or_b32 v114, 3, v114\n"
            "s_waitcnt lgkmcnt(3)\n"
            "s_swappc_b64 s[80:81], s[70:71]\n"
            "v_lshlrev_b32 v92, 16, v93\n"
            "v_lshlrev_b32 v94, 16, v95\n"
            "v_lshlrev_b32 v96, 16, v97\n"
            "v_pk_fma_f32 v[92:93], v[92:93], v[108:109], v[86:87] op_sel:[0,0,0] op_sel_hi:[1,0,1]\n"
            "v_pk_fma_f32 v[94:95], v[94:95], v[108:109], v[86:87] op_sel:[0,1,0] op_sel_hi:[1,1,1]\n"
            "v_pk_fma_f32 v[96:97], v[96:97], v[110:111], v[86:87] op_sel:[0,0,0] op_sel_hi:[1,0,1]\n"
            "v_lshl_add_u32 v92, v93, 16, v92\n"
            "v_lshl_add_u32 v94, v95, 16, v94\n"
            "v_lshl_add_u32 v96, v97, 16, v96\n"
            "v_sub_u32 v92, v92, v86\n"
            "v_mad_i32_i16 v88, v93, v112, v88\n"
            "ds_add_u32 v112, v92 offset:65533\n"
            "v_sub_u32 v94, v94, v86\n"
            "v_mad_i32_i16 v88, v95, v113, v88\n"
            "ds_add_u32 v113, v94 offset:65533\n"
            "v_sub_u32 v96, v96, v86\n"
            "v_mad_i32_i16 v88, v97, v114, v88\n"
            "ds_add_u32 v114, v96 offset:65533\n"
            "v_and_b32 v104, 0x1fffc, v12\n"
            "v_and_b32 v105, 0x1fffc, v13\n"
            "v_and_b32 v106, 0x1fffc, v14\n"
            "s_swappc_b64 s[80:81], s[72:73]\n"
            "ds_read_b32 v99, v104\n"
            "ds_read_b32 v101, v105\n"
            "ds_read_b32 v103, v106\n"
            "s_waitcnt lgkmcnt(0)\n"
            "s_branch Ls1_%=\n"
            "Le1_%=:\n"
            "s_cmp_lt_u32 s75, 8\n"
            "s_cbranch_scc1 1f\n"
            "s_swappc_b64 s[80:81], s[66:67]\n"
            "s_branch Lc1_%=\n"
            "1:\n"
            "s_cmp_lg_u32 s75, 7\n"
            "s_cbranch_scc1 1f\n"
            "s_setprio 2\n"
            "s_mov_b32 s79, 3\n"
            "s_branch Lc1_%=\n"
            "1:\n"
            "s_cmp_lg_u32 s75, 3\n"
            "s_cbranch_scc1 1f\n"
            "s_setprio 1\n"
            "s_mov_b32 s79, 1\n"
            "s_branch Lc1_%=\n"
            "1:\n"
            "s_setprio 0\n"
            "v_add_u32_dpp v111, v81, v82 row_newbcast:1 row_mask:0xf bank_mask:0x3\n"
            "v_add_u32_dpp v111, v81, v82 row_newbcast:9 row_mask:0xf bank_mask:0xc\n"
            "v_and_b32 v108, 0x7fe, v15\n"
            "v_and_b32 v109, 0x1ff800, v15\n"
            "v_lshrrev_b32 v110, 22, v15\n"
            "v_lshrrev_b32 v112, 15, v12\n"
            "v_lshrrev_b32 v113, 15, v13\n"
            "v_lshrrev_b32 v114, 15, v14\n"
            "v_and_b32 v111, " IDXMASK ", v111\n"
            "v_min_u32 v111, v111, v83\n"
            "buffer_load_dwordx4 v[12:15], v111, %[rs], 0 idxen " RAUX "\n"
            "v_fmamk_f32 v108, v108, 0x7e800000, v84\n"
            "v_fmamk_f32 v109, v109, 0x79800000, v84\n"
            "v_fmamk_f32 v110, v110, 0x7f000000, v84\n"
            "v_or_b32 v112, 3, v112\n"
            "v_or_b32 v113, 3, v113\n"
            "v_or_b32 v114, 3, v114\n"
            "s_waitcnt lgkmcnt(3)\n"
            "s_swappc_b64 s[80:81], s[70:71]\n"
            "v_lshlrev_b32 v98, 16, v99\n"
            "v_lshlrev_b32 v100, 16, v101\n"
            "v_lshlrev_b32 v102, 16, v103\n"
            "v_pk_fma_f32 v[98:99], v[98:99], v[108:109], v[86:87] op_sel:[0,0,0] op_sel_hi:[1,0,1]\n"
            "v_pk_fma_f32 v[100:101], v[100:101], v[108:109], v[86:87] op_sel:[0,1,0] op_sel_hi:[1,1,1]\n"
            "v_pk_fma_f32 v[102:103], v[102:103], v[110:111], v[86:87] op_sel:[0,0,0] op_sel_hi:[1,0,1]\n"
            "v_lshl_add_u32 v98, v99, 16, v98\n"
            "v_lshl_add_u32 v100, v101, 16, v100\n"
            "v_lshl_add_u32 v102, v103, 16, v102\n"
            "v_sub_u32 v98, v98, v86\n"
            "v_mad_i32_i16 v88, v99, v112, v88\n"
            "ds_add_u32 v112, v98 offset:65533\n"
            "v_sub_u32 v100, v100, v86\n"
            "v_mad_i32_i16 v88, v101, v113, v88\n"
            "ds_add_u32 v113, v100 offset:65533\n"
            "v_sub_u32 v102, v102, v86\n"
            "v_mad_i32_i16 v88, v103, v114, v88\n"
            "ds_add_u32 v114, v102 offset:65533\n"
            "v_and_b32 v104, 0x1fffc, v16\n"
            "v_and_b32 v105, 0x1fffc, v17\n"
            "v_and_b32 v106, 0x1fffc, v18\n"
            "s_swappc_b64 s[80:81], s[72:73]\n"
            "ds_read_b32 v93, v104\n"
            "ds_read_b32 v95, v105\n"
            "ds_read_b32 v97, v106\n"
            "s_waitcnt lgkmcnt(0)\n"
            "s_branch Ls2_%=\n"
            "Le2_%=:\n"
            "s_cmp_lt_u32 s75, 8\n"
            "s_cbranch_scc1 1f\n"
            "s_swappc_b64 s[80:81], s[66:67]\n"
            "s_branch Lc2_%=\n"
            "1:\n"
            "s_cmp_lg_u32 s75, 7\n"
            "s_cbranch_scc1 1f\n"
            "s_setprio 2\n"
            "s_mov_b32 s79, 3\n"
            "s_branch Lc2_%=\n"
            "1:\n"
            "s_cmp_lg_u32 s75, 3\n"
            "s_cbranch_scc1 1f\n"
            "s_setprio 1\n"
            "s_mov_b32 s79, 1\n"
            "s_branch Lc2_%=\n"
            "1:\n"
            "s_setprio 0\n"
            "v_add_u32_dpp v111, v81, v82 row_newbcast:2 row_mask:0xf bank_mask:0x3\n"
            "v_add_u32_dpp v111, v81, v82 row_newbcast:10 row_mask:0xf bank_mask:0xc\n"
            "v_and_b32 v108, 0x7fe, v19\n"
            "v_and_b32 v109, 0x1ff800, v19\n"
            "v_lshrrev_b32 v110, 22, v19\n"
            "v_lshrrev_b32 v112, 15, v16\n"
            "v_lshrrev_b32 v113, 15, v17\n"
            "v_lshrrev_b32 v114, 15, v18\n"
            "v_and_b32 v111, " IDXMASK ", v111\n"
            "v_min_u32 v111, v111, v83\n"
            "buffer_load_dwordx4 v[16:19], v111, %[rs], 0 idxen " RAUX "\n"
            "v_fmamk_f32 v108, v108, 0x7e800000, v84\n"
            "v_fmamk_f32 v109, v109, 0x79800000, v84\n"
            "v_fmamk_f32 v110, v110, 0x7f000000, v84\n"
            "v_or_b32 v112, 3, v112\n"
            "v_or_b32 v113, 3, v113\n"
            "v_or_b32 v114, 3, v114\n"
            "s_waitcnt lgkmcnt(3)\n"
            "s_swappc_b64 s[80:81], s[70:71]\n"
            "v_lshlrev_b32 v92, 16, v93\n"
            "v_lshlrev_b32 v94, 16, v95\n"
            "v_lshlrev_b32 v96, 16, v97\n"
            "v_pk_fma_f32 v[92:93], v[92:93], v[108:109], v[86:87] op_sel:[0,0,0] op_sel_hi:[1,0,1]\n"
            "v_pk_fma_f32 v[94:95], v[94:95], v[108:109], v[86:87] op_sel:[0,1,0] op_sel_hi:[1,1,1]\n"
            "v_pk_fma_f32 v[96:97], v[96:97], v[110:111], v[86:87] op_sel:[0,0,0] op_sel_hi:[1,0,1]\n"
            "v_lshl_add_u32 v92, v93, 16, v92\n"
            "v_lshl_add_u32 v94, v95, 16, v94\n"
            "v_lshl_add_u32 v96, v97, 16, v96\n"
            "v_sub_u32 v92, v92, v86\n"
            "v_mad_i32_i16 v88, v93, v112, v88\n"
            "ds_add_u32 v112, v92 offset:65533\n"
            "v_sub_u32 v94, v94, v86\n"
            "v_mad_i32_i16 v88, v95, v113, v88\n"
            "ds_add_u32 v113, v94 offset:65533\n"
            "v_sub_u32 v96, v96, v86\n"
            "v_mad_i32_i16 v88, v97, v114, v88\n"
            "ds_add_u32 v114, v96 offset:65533\n"
            "v_and_b32 v104, 0x1fffc, v20\n"
            "v_and_b32 v105, 0x1fffc, v21\n"
            "v_and_b32 v106, 0x1fffc, v22\n"
            "s_swappc_b64 s[80:81], s[72:73]\n"
            "ds_read_b32 v99, v104\n"
            "ds_read_b32 v101, v105\n"
            "ds_read_b32 v103, v106\n"
            "s_waitcnt lgkmcnt(0)\n"
            "s_branch Ls3_%=\n"
            "Le3_%=:\n"
            "s_cmp_lt_u32 s75, 8\n"
            "s_cbranch_scc1 1f\n"
            "s_swappc_b64 s[80:81], s[66:67]\n"
            "s_branch Lc3_%=\n"
            "1:\n"
            "s_cmp_lg_u32 s75, 7\n"
            "s_cbranch_scc1 1f\n"
            "s_setprio 2\n"
            "s_mov_b32 s79, 3\n"
            "s_branch Lc3_%=\n"
            "1:\n"
            "s_cmp_lg_u32 s75, 3\n"
            "s_cbranch_scc1 1f\n"
            "s_setprio 1\n"
            "s_mov_b32 s79, 1\n"
            "s_branch Lc3_%=\n"
            "1:\n"
            "s_setprio 0\n"
            "v_add_u32_dpp v111, v81, v82 row_newbcast:3 row_mask:0xf bank_mask:0x3\n"
            "v_add_u32_dpp v111, v81, v82 row_newbcast:11 row_mask:0xf bank_mask:0xc\n"
            "v_and_b32 v108, 0x7fe, v23\n"
            "v_and_b32 v109, 0x1ff800, v23\n"
            "v_lshrrev_b32 v110, 22, v23\n"
            "v_lshrrev_b32 v112, 15, v20\n"
            "v_lshrrev_b32 v113, 15, v21\n"
            "v_lshrrev_b32 v114, 15, v22\n"
            "v_and_b32 v111, " IDXMASK ", v111\n"
            "v_min_u32 v111, v111, v83\n"
            "buffer_load_dwordx4 v[20:23], v111, %[rs], 0 idxen " RAUX "\n"
            "v_fmamk_f32 v108, v108, 0x7e800000, v84\n"
            "v_fmamk_f32 v109, v109, 0x79800000, v84\n"
            "v_fmamk_f32 v110, v110, 0x7f000000, v84\n"
            "v_or_b32 v112, 3, v112\n"
            "v_or_b32 v113, 3, v113\n"
            "v_or_b32 v114, 3, v114\n"
            "s_waitcnt lgkmcnt(3)\n"
            "s_swappc_b64 s[80:81], s[70:71]\n"
            "v_lshlrev_b32 v98, 16, v99\n"
            "v_lshlrev_b32 v100, 16, v101\n"
            "v_lshlrev_b32 v102, 16, v103\n"
            "v_pk_fma_f32 v[98:99], v[98:99], v[108:109], v[86:87] op_sel:[0,0,0] op_sel_hi:[1,0,1]\n"
            "v_pk_fma_f32 v[100:101], v[100:101], v[108:109], v[86:87] op_sel:[0,1,0] op_sel_hi:[1,1,1]\n"
            "v_pk_fma_f32 v[102:103], v[102:103], v[110:111], v[86:87] op_sel:[0,0,0] op_sel_hi:[1,0,1]\n"
            "v_lshl_add_u32 v98, v99, 16, v98\n"
            "v_lshl_add_u32 v100, v101, 16, v100\n"
            "v_lshl_add_u32 v102, v103, 16, v102\n"
            "v_sub_u32 v98, v98, v86\n"
            "v_mad_i32_i16 v88, v99, v112, v88\n"
            "ds_add_u32 v112, v98 offset:65533\n"
            "v_sub_u32 v100, v100, v86\n"
            "v_mad_i32_i16 v88, v101, v113, v88\n"
            "ds_add_u32 v113, v100 offset:65533\n"
            "v_sub_u32 v102, v102, v86\n"
            "v_mad_i32_i16 v88, v103, v114, v88\n"
            "ds_add_u32 v114, v102 offset:65533\n"
            "v_and_b32 v104, 0x1fffc, v24\n"
            "v_and_b32 v105, 0x1fffc, v25\n"
            "v_and_b32 v106, 0x1fffc, v26\n"
            "s_swappc_b64 s[80:81], s[72:73]\n"
            "ds_read_b32 v93, v104\n"
            "ds_read_b32 v95, v105\n"
            "ds_read_b32 v97, v106\n"
            "s_waitcnt lgkmcnt(0)\n"
            "s_branch Ls4_%=\n"
            "Le4_%=:\n"
            "s_cmp_lt_u32 s75, 8\n"
            "s_cbranch_scc1 1f\n"
            "s_swappc_b64 s[80:81], s[66:67]\n"
            "s_branch Lc4_%=\n"
            "1:\n"
            "s_cmp_lg_u32 s75, 7\n"
            "s_cbranch_scc1 1f\n"
            "s_setprio 2\n"
            "s_mov_b32 s79, 3\n"
            "s_branch Lc4_%=\n"
            "1:\n"
            "s_cmp_lg_u32 s75, 3\n"
            "s_cbranch_scc1 1f\n"
            "s_setprio 1\n"
            "s_mov_b32 s79, 1\n"
            "s_branch Lc4_%=\n"
            "1:\n"
            "s_setprio 0\n"
            "v_add_u32_dpp v111, v81, v82 row_newbcast:4 row_mask:0xf bank_mask:0x3\n"
            "v_add_u32_dpp v111, v81, v82 row_newbcast:12 row_mask:0xf bank_mask:0xc\n"
            "v_and_b32 v108, 0x7fe, v27\n"
            "v_and_b32 v109, 0x1ff800, v27\n"
            "v_lshrrev_b32 v110, 22, v27\n"
            "v_lshrrev_b32 v112, 15, v24\n"
            "v_lshrrev_b32 v113, 15, v25\n"
            "v_lshrrev_b32 v114, 15, v26\n"
            "v_and_b32 v111, " IDXMASK ", v111\n"
            "v_min_u32 v111, v111, v83\n"
            "buffer_load_dwordx4 v[24:27], v111, %[rs], 0 idxen " RAUX "\n"
            "v_fmamk_f32 v108, v108, 0x7e800000, v84\n"
            "v_fmamk_f32 v109, v109, 0x79800000, v84\n"
            "v_fmamk_f32 v110, v110, 0x7f000000, v84\n"
            "v_or_b32 v112, 3, v112\n"
            "v_or_b32 v113, 3, v113\n"
            "v_or_b32 v114, 3, v114\n"
            "s_waitcnt lgkmcnt(3)\n"
            "s_swappc_b64 s[80:81], s[70:71]\n"
            "v_lshlrev_b32 v92, 16, v93\n"
            "v_lshlrev_b32 v94, 16, v95\n"
            "v_lshlrev_b32 v96, 16, v97\n"
            "v_pk_fma_f32 v[92:93], v[92:93], v[108:109], v[86:87] op_sel:[0,0,0] op_sel_hi:[1,0,1]\n"
            "v_pk_fma_f32 v[94:95], v[94:95], v[108:109], v[86:87] op_sel:[0,1,0] op_sel_hi:[1,1,1]\n"
            "v_pk_fma_f32 v[96:97], v[96:97], v[110:111], v[86:87] op_sel:[0,0,0] op_sel_hi:[1,0,1]\n"
            "v_lshl_add_u32 v92, v93, 16, v92\n"
            "v_lshl_add_u32 v94, v95, 16, v94\n"
            "v_lshl_add_u32 v96, v97, 16, v96\n"
            "v_sub_u32 v92, v92, v86\n"
            "v_mad_i32_i16 v88, v93, v112, v88\n"
            "ds_add_u32 v112, v92 offset:65533\n"
            "v_sub_u32 v94, v94, v86\n"
            "v_mad_i32_i16 v88, v95, v113, v88\n"
            "ds_add_u32 v113, v94 offset:65533\n"
            "v_sub_u32 v96, v96, v86\n"
            "v_mad_i32_i16 v88, v97, v114, v88\n"
            "ds_add_u32 v114, v96 offset:65533\n"
            "v_and_b32 v104, 0x1fffc, v28\n"
            "v_and_b32 v105, 0x1fffc, v29\n"
            "v_and_b32 v106, 0x1fffc, v30\n"
            "s_swappc_b64 s[80:81], s[72:73]\n"
            "ds_read_b32 v99, v104\n"
            "ds_read_b32 v101, v105\n"
            "ds_read_b32 v103, v106\n"
            "s_waitcnt lgkmcnt(0)\n"
            "s_branch Ls5_%=\n"
            "Le5_%=:\n"
            "s_cmp_lt_u32 s75, 8\n"
            "s_cbranch_scc1 1f\n"
            "s_swappc_b64 s[80:81], s[66:67]\n"
            "s_branch Lc5_%=\n"
            "1:\n"
            "s_cmp_lg_u32 s75, 7\n"
            "s_cbranch_scc1 1f\n"
            "s_setprio 2\n"
            "s_mov_b32 s79, 3\n"
            "s_branch Lc5_%=\n"
            "1:\n"
            "s_cmp_lg_u32 s75, 3\n"
            "s_cbranch_scc1 1f\n"
            "s_setprio 1\n"
            "s_mov_b32 s79, 1\n"
            "s_branch Lc5_%=\n"
            "1:\n"
            "s_setprio 0\n"
            "v_add_u32_dpp v111, v81, v82 row_newbcast:5 row_mask:0xf bank_mask:0x3\n"
            "v_add_u32_dpp v111, v81, v82 row_newbcast:13 row_mask:0xf bank_mask:0xc\n"
            "v_and_b32 v108, 0x7fe, v31\n"
            "v_and_b32 v109, 0x1ff800, v31\n"
            "v_lshrrev_b32 v110, 22, v31\n"
            "v_lshrrev_b32 v112, 15, v28\n"
            "v_lshrrev_b32 v113, 15, v29\n"
            "v_lshrrev_b32 v114, 15, v30\n"
            "v_and_b32 v111, " IDXMASK ", v111\n"
            "v_min_u32 v111, v111, v83\n"
            "buffer_load_dwordx4 v[28:31], v111, %[rs], 0 idxen " RAUX "\n"
            "v_fmamk_f32 v108, v108, 0x7e800000, v84\n"
            "v_fmamk_f32 v109, v109, 0x79800000, v84\n"
            "v_fmamk_f32 v110, v110, 0x7f000000, v84\n"
            "v_or_b32 v112, 3, v112\n"
            "v_or_b32 v113, 3, v113\n"
            "v_or_b32 v114, 3, v114\n"
            "s_waitcnt lgkmcnt(3)\n"
            "s_swappc_b64 s[80:81], s[70:71]\n"
            "v_lshlrev_b32 v98, 16, v99\n"
            "v_lshlrev_b32 v100, 16, v101\n"
            "v_lshlrev_b32 v102, 16, v103\n"
            "v_pk_fma_f32 v[98:99], v[98:99], v[108:109], v[86:87] op_sel:[0,0,0] op_sel_hi:[1,0,1]\n"
            "v_pk_fma_f32 v[100:101], v[100:101], v[108:109], v[86:87] op_sel:[0,1,0] op_sel_hi:[1,1,1]\n"
            "v_pk_fma_f32 v[102:103], v[102:103], v[110:111], v[86:87] op_sel:[0,0,0] op_sel_hi:[1,0,1]\n"
            "v_lshl_add_u32 v98, v99, 16, v98\n"
            "v_lshl_add_u32 v100, v101, 16, v100\n"
            "v_lshl_add_u32 v102, v103, 16, v102\n"
            "v_sub_u32 v98, v98, v86\n"
            "v_mad_i32_i16 v88, v99, v112, v88\n"
            "ds_add_u32 v112, v98 offset:65533\n"
            "v_sub_u32 v100, v100, v86\n"
            "v_mad_i32_i16 v88, v101, v113, v88\n"
            "ds_add_u32 v113, v100 offset:65533\n"
            "v_sub_u32 v102, v102, v86\n"
            "v_mad_i32_i16 v88, v103, v114, v88\n"
            "ds_add_u32 v114, v102 offset:65533\n"
            "v_and_b32 v104, 0x1fffc, v32\n"
            "v_and_b32 v105, 0x1fffc, v33\n"
            "v_and_b32 v106, 0x1fffc, v34\n"
            "s_swappc_b64 s[80:81], s[72:73]\n"
            "ds_read_b32 v93, v104\n"
            "ds_read_b32 v95, v105\n"
            "ds_read_b32 v97, v106\n"
            "s_waitcnt lgkmcnt(0)\n"
            "s_branch Ls6_%=\n"
            "Le6_%=:\n"
            "s_cmp_lt_u32 s75, 8\n"
            "s_cbranch_scc1 1f\n"
            "s_swappc_b64 s[80:81], s[66:67]\n"
            "s_branch Lc6_%=\n"
            "1:\n"
            "s_cmp_lg_u32 s75, 7\n"
            "s_cbranch_scc1 1f\n"
            "s_setprio 2\n"
            "s_mov_b32 s79, 3\n"
            "s_branch Lc6_%=\n"
            "1:\n"
            "s_cmp_lg_u32 s75, 3\n"
            "s_cbranch_scc1 1f\n"
            "s_setprio 1\n"
            "s_mov_b32 s79, 1\n"
            "s_branch Lc6_%=\n"
            "1:\n"
            "s_setprio 0\n"
            "v_add_u32_dpp v111, v81, v82 row_newbcast:6 row_mask:0xf bank_mask:0x3\n"
            "v_add_u32_dpp v111, v81, v82 row_newbcast:14 row_mask:0xf bank_mask:0xc\n"
            "v_and_b32 v108, 0x7fe, v35\n"
            "v_and_b32 v109, 0x1ff800, v35\n"
            "v_lshrrev_b32 v110, 22, v35\n"
            "v_lshrrev_b32 v112, 15, v32\n"
            "v_lshrrev_b32 v113, 15, v33\n"
            "v_lshrrev_b32 v114, 15, v34\n"
            "v_and_b32 v111, " IDXMASK ", v111\n"
            "v_min_u32 v111, v111, v83\n"
            "buffer_load_dwordx4 v[32:35], v111, %[rs], 0 idxen " RAUX "\n"
            "v_fmamk_f32 v108, v108, 0x7e800000, v84\n"
            "v_fmamk_f32 v109, v109, 0x79800000, v84\n"
            "v_fmamk_f32 v110, v110, 0x7f000000, v84\n"
            "v_or_b32 v112, 3, v112\n"
            "v_or_b32 v113, 3, v113\n"
            "v_or_b32 v114, 3, v114\n"
            "s_waitcnt lgkmcnt(3)\n"
            "s_swappc_b64 s[80:81], s[70:71]\n"
            "v_lshlrev_b32 v92, 16, v93\n"
            "v_lshlrev_b32 v94, 16, v95\n"
            "v_lshlrev_b32 v96, 16, v97\n"
            "v_pk_fma_f32 v[92:93], v[92:93], v[108:109], v[86:87] op_sel:[0,0,0] op_sel_hi:[1,0,1]\n"
            "v_pk_fma_f32 v[94:95], v[94:95], v[108:109], v[86:87] op_sel:[0,1,0] op_sel_hi:[1,1,1]\n"
            "v_pk_fma_f32 v[96:97], v[96:97], v[110:111], v[86:87] op_sel:[0,0,0] op_sel_hi:[1,0,1]\n"
            "v_lshl_add_u32 v92, v93, 16, v92\n"
            "v_lshl_add_u32 v94, v95, 16, v94\n"
            "v_lshl_add_u32 v96, v97, 16, v96\n"
            "v_sub_u32 v92, v92, v86\n"
            "v_mad_i32_i16 v88, v93, v112, v88\n"
            "ds_add_u32 v112, v92 offset:65533\n"
            "v_sub_u32 v94, v94, v86\n"
            "v_mad_i32_i16 v88, v95, v113, v88\n"
            "ds_add_u32 v113, v94 offset:65533\n"
            "v_sub_u32 v96, v96, v86\n"
            "v_mad_i32_i16 v88, v97, v114, v88\n"
            "ds_add_u32 v114, v96 offset:65533\n"
            "v_and_b32 v104, 0x1fffc, v36\n"
            "v_and_b32 v105, 0x1fffc, v37\n"
            "v_and_b32 v106, 0x1fffc, v38\n"
            "s_swappc_b64 s[80:81], s[72:73]\n"
            "ds_read_b32 v99, v104\n"
            "ds_read_b32 v101, v105\n"
            "ds_read_b32 v103, v106\n"
            "s_waitcnt lgkmcnt(0)\n"
            "s_branch Ls7_%=\n"
            "Le7_%=:\n"
            "s_cmp_lt_u32 s75, 8\n"
            "s_cbranch_scc1 1f\n"
            "s_swappc_b64 s[80:81], s[66:67]\n"
            "s_branch Lc7_%=\n"
            "1:\n"
            "s_cmp_lg_u32 s75, 7\n"
            "s_cbranch_scc1 1f\n"
            "s_setprio 2\n"
            "s_mov_b32 s79, 3\n"
            "s_branch Lc7_%=\n"
            "1:\n"
            "s_cmp_lg_u32 s75, 3\n"
            "s_cbranch_scc1 1f\n"
            "s_setprio 1\n"
            "s_mov_b32 s79, 1\n"
            "s_branch Lc7_%=\n"
            "1:\n"
            "s_setprio 0\n"
            "v_add_u32_dpp v111, v81, v82 row_newbcast:7 row_mask:0xf bank_mask:0x3\n"
            "v_add_u32_dpp v111, v81, v82 row_newbcast:15 row_mask:0xf bank_mask:0xc\n"
            "v_and_b32 v108, 0x7fe, v39\n"
            "v_and_b32 v109, 0x1ff800, v39\n"
            "v_lshrrev_b32 v110, 22, v39\n"
            "v_lshrrev_b32 v112, 15, v36\n"
            "v_lshrrev_b32 v113, 15, v37\n"
            "v_lshrrev_b32 v114, 15, v38\n"
            "v_and_b32 v111, " IDXMASK ", v111\n"
            "v_min_u32 v111, v111, v83\n"
            "buffer_load_dwordx4 v[36:39], v111, %[rs], 0 idxen " RAUX "\n"
            "s_min_u32 s82, s82, 19\n"
            "s_add_u32 s82, s82, 1\n"
            "s_set_gpr_idx_on s82, gpr_idx(SRC0)\n"
            "s_nop 0\n"
            "v_mov_b32 v81, v60\n"
            "s_set_gpr_idx_off\n"
            "v_fmamk_f32 v108, v108, 0x7e800000, v84\n"
            "v_fmamk_f32 v109, v109, 0x79800000, v84\n"
            "v_fmamk_f32 v110, v110, 0x7f000000, v84\n"
            "v_or_b32 v112, 3, v112\n"
            "v_or_b32 v113, 3, v113\n"
            "v_or_b32 v114, 3, v114\n"
            "s_waitcnt lgkmcnt(3)\n"
            "s_swappc_b64 s[80:81], s[70:71]\n"
            "v_lshlrev_b32 v98, 16, v99\n"
            "v_lshlrev_b32 v100, 16, v101\n"
            "v_lshlrev_b32 v102, 16, v103\n"
            "v_pk_fma_f32 v[98:99], v[98:99], v[108:109], v[86:87] op_sel:[0,0,0] op_sel_hi:[1,0,1]\n"
            "v_pk_fma_f32 v[100:101], v[100:101], v[108:109], v[86:87] op_sel:[0,1,0] op_sel_hi:[1,1,1]\n"
            "v_pk_fma_f32 v[102:103], v[102:103], v[110:111], v[86:87] op_sel:[0,0,0] op_sel_hi:[1,0,1]\n"
            "v_lshl_add_u32 v98, v99, 16, v98\n"
            "v_lshl_add_u32 v100, v101, 16, v100\n"
            "v_lshl_add_u32 v102, v103, 16, v102\n"
            "v_sub_u32 v98, v98, v86\n"
            "v_mad_i32_i16 v88, v99, v112, v88\n"
            "ds_add_u32 v112, v98 offset:65533\n"
            "v_sub_u32 v100, v100, v86\n"
            "v_mad_i32_i16 v88, v101, v113, v88\n"
            "ds_add_u32 v113, v100 offset:65533\n"
            "v_sub_u32 v102, v102, v86\n"
            "v_mad_i32_i16 v88, v103, v114, v88\n"
            "ds_add_u32 v114, v102 offset:65533\n"
            "v_and_b32 v104, 0x1fffc, v8\n"
            "v_and_b32 v105, 0x1fffc, v9\n"
            "v_and_b32 v106, 0x1fffc, v10\n"
            "s_swappc_b64 s[80:81], s[72:73]\n"
            "ds_read_b32 v93, v104\n"
            "ds_read_b32 v95, v105\n"
            "ds_read_b32 v97, v106\n"
            "s_waitcnt lgkmcnt(0)\n"
            "s_branch Ls0_%=\n"
            "Lchg1%=:\n"
            "s_add_u32 s92, s92, 1\n"
            "s_cmp_eq_u32 s92, 8\n"
            "s_cbranch_scc1 1f\n"
            "s_barrier\n"
            "s_mov_b32 m0, %[wb]\n"
            "s_nop 0\n"
            "ds_write_addtid_b32 v40 offset:0\n"
            "ds_write_addtid_b32 v41 offset:256\n"
            "ds_write_addtid_b32 v42 offset:512\n"
            "ds_write_addtid_b32 v43 offset:768\n"
            "ds_write_addtid_b32 v44 offset:16384\n"
            "ds_write_addtid_b32 v45 offset:16640\n"
            "ds_write_addtid_b32 v46 offset:16896\n"
            "ds_write_addtid_b32 v47 offset:17152\n"
            "ds_write_addtid_b32 v48 offset:32768\n"
            "ds_write_addtid_b32 v49 offset:33024\n"
            "ds_write_addtid_b32 v50 offset:33280\n"
            "ds_write_addtid_b32 v51 offset:33536\n"
            "ds_write_addtid_b32 v52 offset:49152\n"
            "ds_write_addtid_b32 v53 offset:49408\n"
            "ds_write_addtid_b32 v54 offset:49664\n"
            "ds_write_addtid_b32 v55 offset:49920\n"
            "s_add_u32 m0, %[wb], 16384\n"
            "s_nop 0\n"
            "ds_write_addtid_b32 v56 offset:49152\n"
            "ds_write_addtid_b32 v57 offset:49408\n"
            "ds_write_addtid_b32 v58 offset:49664\n"
            "ds_write_addtid_b32 v59 offset:49920\n"
            "1:\n"
            "s_setpc_b64 s[80:81]\n"
            "Lchg2%=:\n"
            "s_cmp_eq_u32 s92, 8\n"
            "s_cbranch_scc1 Lfin%=\n"
            "s_lshr_b64 s[76:77], s[76:77], 8\n"
            "s_and_b32 s75, s76, 255\n"
            "s_movk_i32 s74, 1000\n"
            "s_sub_i32 s79, s75, %[j0]\n"
            "s_max_i32 s79, s79, 8\n"
            "s_cmp_eq_u32 s92, 7\n s_cselect_b32 s79, 8, s79\n"
            "s_setprio 3\n"
            "s_waitcnt lgkmcnt(3)\n"
            "s_barrier\n"
            "s_setpc_b64 s[80:81]\n"
            "Lchg3%=:\n"
            "s_sub_u32 s74, s75, 7\n"
            "s_mov_b32 s79, 7\n"
            "s_cmp_eq_u32 s92, 7\n"
            "s_cbranch_scc1 1f\n"
            "s_add_u32 s94, s94, %[i4]\n"
            "buffer_load_dwordx4 v[40:43], %[tid16], %[rm], s94 offen\n"
            "s_add_u32 s96, s94, 16384\n"
            "buffer_load_dwordx4 v[44:47], %[tid16], %[rm], s96 offen\n"
            "s_add_u32 s96, s94, 32768\n"
            "buffer_load_dwordx4 v[48:51], %[tid16], %[rm], s96 offen\n"
            "s_add_u32 s96, s94, 49152\n"
            "buffer_load_dwordx4 v[52:55], %[tid16], %[rm], s96 offen\n"
            "s_add_u32 s96, s94, 65536\n"
            "buffer_load_dwordx4 v[56:59], %[tid16], %[rm], s96 offen\n"
            "s_branch 2f\n"
            "1:\n"
            "s_mov_b32 s74, 0\n"
            "buffer_load_dword v50, v80, %[rd], 0 offen\n"
            "buffer_load_dword v48, %[ed], %[rd], 0 offen " NTH "\n"
            "buffer_load_dword v49, %[ed], %[rd], %[e1] offen " NTH "\n"
            "v_lshlrev_b32 v52, 2, %[ed]\n"
            "s_lshl_b32 s96, %[e1], 2\n"
            "buffer_load_dwordx4 v[40:43], v52, %[rmm], 0 offen " NTH "\n"
            "buffer_load_dwordx4 v[44:47], v52, %[rmm], s96 offen " NTH "\n"
            "2:\n"
            "s_setpc_b64 s[80:81]\n"
            "Lfin%=:\n"
            "s_waitcnt lgkmcnt(0)\n"
            "s_barrier\n"
            "ds_read_b128 v[60:63], v89 offset:0\n"
            "ds_read_b128 v[64:67], v89 offset:16384\n"
            "ds_read_b128 v[68:71], v89 offset:32768\n"
            "ds_read_b128 v[72:75], v89 offset:49152\n"
            "ds_read_b128 v[76:79], v90\n"
            "v_subrev_u32 v117, 65533, v89\n"
            "v_mov_b32 v118, 0\n"
            "v_bfe_u32 v116, %[tid16], 9, 3\n"
            "v_cmp_eq_u32 vcc, %[q], v116\n"
            "v_bfrev_b32 v115, 1\n"
            "s_nop 0\n"
            "v_cndmask_b32 v115, %[tid16], v115, vcc\n"
            "s_mov_b32 s96, 16384\n s_mov_b32 s97, 32768\n s_mov_b32 s92, 49152\n s_mov_b32 s93, 65536\n"
            "s_waitcnt lgkmcnt(4)\n"
            "buffer_store_dwordx4 v[60:63], v115, %[rp0], 0 offen sc1\n"
            "s_waitcnt lgkmcnt(3)\n"
            "buffer_store_dwordx4 v[64:67], v115, %[rp0], s96 offen sc1\n"
            "s_waitcnt lgkmcnt(2)\n"
            "buffer_store_dwordx4 v[68:71], v115, %[rp0], s97 offen sc1\n"
            "s_waitcnt lgkmcnt(1)\n"
            "buffer_store_dwordx4 v[72:75], v115, %[rp0], s92 offen sc1\n"
            "s_waitcnt lgkmcnt(0)\n"
            "buffer_store_dwordx4 v[76:79], v115, %[rp0], s93 offen sc1\n"
            "v_mad_i32_i16 v118, v60, v117, v118 op_sel:[1,0,0,0]\n"
            "v_add_u32 v116, 4, v117\n"
            "v_mad_i32_i16 v118, v61, v116, v118 op_sel:[1,0,0,0]\n"
            "v_add_u32 v116, 8, v117\n"
            "v_mad_i32_i16 v118, v62, v116, v118 op_sel:[1,0,0,0]\n"
            "v_add_u32 v116, 12, v117\n"
            "v_mad_i32_i16 v118, v63, v116, v118 op_sel:[1,0,0,0]\n"
            "v_add_u32 v117, 16384, v117\n"
            "v_mad_i32_i16 v118, v64, v117, v118 op_sel:[1,0,0,0]\n"
            "v_add_u32 v116, 4, v117\n"
            "v_mad_i32_i16 v118, v65, v116, v118 op_sel:[1,0,0,0]\n"
            "v_add_u32 v116, 8, v117\n"
            "v_mad_i32_i16 v118, v66, v116, v118 op_sel:[1,0,0,0]\n"
            "v_add_u32 v116, 12, v117\n"
            "v_mad_i32_i16 v118, v67, v116, v118 op_sel:[1,0,0,0]\n"
            "v_add_u32 v117, 16384, v117\n"
            "v_mad_i32_i16 v118, v68, v117, v118 op_sel:[1,0,0,0]\n"
            "v_add_u32 v116, 4, v117\n"
            "v_mad_i32_i16 v118, v69, v116, v118 op_sel:[1,0,0,0]\n"
            "v_add_u32 v116, 8, v117\n"
            "v_mad_i32_i16 v118, v70, v116, v118 op_sel:[1,0,0,0]\n"
            "v_add_u32 v116, 12, v117\n"
            "v_mad_i32_i16 v118, v71, v116, v118 op_sel:[1,0,0,0]\n"
            "v_add_u32 v117, 16384, v117\n"
            "v_mad_i32_i16 v118, v72, v117, v118 op_sel:[1,0,0,0]\n"
            "v_add_u32 v116, 4, v117\n"
            "v_mad_i32_i16 v118, v73, v116, v118 op_sel:[1,0,0,0]\n"
            "v_add_u32 v116, 8, v117\n"
            "v_mad_i32_i16 v118, v74, v116, v118 op_sel:[1,0,0,0]\n"
            "v_add_u32 v116, 12, v117\n"
            "v_mad_i32_i16 v118, v75, v116, v118 op_sel:[1,0,0,0]\n"
            "v_add_u32 v117, 16384, v117\n"
            "v_mad_i32_i16 v118, v76, v117, v118 op_sel:[1,0,0,0]\n"
            "v_add_u32 v116, 4, v117\n"
            "v_mad_i32_i16 v118, v77, v116, v118 op_sel:[1,0,0,0]\n"
            "v_add_u32 v116, 8, v117\n"
            "v_mad_i32_i16 v118, v78, v116, v118 op_sel:[1,0,0,0]\n"
            "v_add_u32 v116, 12, v117\n"
            "v_mad_i32_i16 v118, v79, v116, v118 op_sel:[1,0,0,0]\n"
            "v_add_u32 v117, 16384, v117\n"
            "v_sub_u32 v118, v88, v118\n"
            "v_mov_b32 v116, %[wb]\n"
            "v_mov_b32 v117, %[bad]\n"
            "v_add_u32_dpp v118, v118, v118 row_shr:1 row_mask:0xf bank_mask:0xf bound_ctrl:1\n"
            "s_nop 1\n"
            "v_add_u32_dpp v118, v118, v118 row_shr:2 row_mask:0xf bank_mask:0xf bound_ctrl:1\n"
            "s_nop 1\n"
            "v_add_u32_dpp v118, v118, v118 row_shr:4 row_mask:0xf bank_mask:0xf bound_ctrl:1\n"
            "s_nop 1\n"
            "v_add_u32_dpp v118, v118, v118 row_shr:8 row_mask:0xf bank_mask:0xf bound_ctrl:1\n"
            "s_nop 1\n"
            "v_add_u32_dpp v118, v118, v118 row_bcast:15 row_mask:0xa bank_mask:0xf\n"
            "s_nop 1\n"
            "v_add_u32_dpp v118, v118, v118 row_bcast:31 row_mask:0xc bank_mask:0xf\n"
            "s_nop 1\n"
            "v_lshrrev_b32 v116, 7, v116\n"
            "s_mov_b32 exec_lo, 0\n s_mov_b32 exec_hi, 0x80000000\n"
            "ds_write2_b32 v116, v118, v117 offset1:1\n"
            "s_mov_b64 exec, -1\n"
            "s_waitcnt vmcnt(5)\n"
            "s_branch Lend2%=\n"
            "Lend%=:\n"
            "s_waitcnt vmcnt(0)\n"
            "Lend2%=:\n"
            "s_setprio 0\n"
            "s_waitcnt lgkmcnt(0)\n"

            : [mode] "=&s"(mode), [bad] "=&s"(bad), [exs] "=&v"(exs), [tid16] "+v"(t16), "+v"(tc2), "=&{v[40:43]}"(mm[0]), "=&{v[44:47]}"(mm[1]), "=&{v[48:51]}"(dd)
            : [rs] "s"(rs), [rm] "s"(rm), [rp0] "s"(rp0), [rt] "s"(rt), [i4] "s"(LMAX * 4), [c4] "s"(a_.c4), [pb] "s"(81920 + wave * 5120), [wb] "s"(wave * 1024), [q] "s"(q), [j0] "s"(J0),
              [rmm] "s"(rmm), [rd] "s"(rd), [e1] "s"(128 * 32 * 128 * 2), [ed] "v"((((wave * 8 + q) * 32 + b) * 128 + lane * 2) * 2),
              [fof] "s"((NT * NWS * 3 + cp * (P1_GRID * 2)) * 4), [dumu] "s"(ly.dumu),
              [tof] "v"((w * NT + t0) * 12), [fb] "v"(frag_unit(w, ly)), [xb] "v"((blk_line(w) + grp_line(qt)) * 8), [hm] "v"(slot_line((lane >> 4) * 64 + b) * 8)
            : "memory", "vcc", "scc", "m0", "s66", "s67", "s70", "s71", "s72", "s73", "s74", "s75", "s76", "s77", "s79", "s80", "s81", "s82",
              "s84", "s85", "s86", "s87", "s88", "s89", "s90", "s91", "s92", "s93", "s94", "s96", "s97",
              "v8", "v9", "v10", "v11", "v12", "v13", "v14", "v15", "v16", "v17", "v18", "v19", "v20", "v21", "v22", "v23", "v24", "v25", "v26", "v27", "v28", "v29", "v30", "v31", "v32", "v33", "v34", "v35", "v36", "v37", "v38", "v39", "v52", "v53", "v54", "v55", "v56", "v57", "v58", "v59", "v60", "v61", "v62", "v63", "v64", "v65", "v66", "v67", "v68", "v69", "v70", "v71", "v72", "v73", "v74", "v75", "v76", "v77", "v78", "v79", "v80", "v81", "v82", "v83", "v84", "v85", "v86", "v87", "v88", "v89", "v90", "v91", "v92", "v93", "v94", "v95", "v96", "v97", "v98", "v99", "v100", "v101", "v102", "v103", "v104", "v105", "v106", "v107", "v108", "v109", "v110", "v111", "v112", "v113", "v114", "v115", "v116", "v117", "v118");
    }
    const int tid2 = (int)(t16 >> 4);
    kaptr ka = (kaptr)__builtin_amdgcn_kernarg_segment_ptr();
    asm volatile("" : "+s"(ka));
    unsigned long long* wm = (unsigned long long*)(ka->wgmode + b * 32);
    const int E = ka->E;
    const unsigned nblk = ka->O >> 7;
    const int ln = tid2 & 63, wv = __builtin_amdgcn_readfirstlane(tid2 >> 6);
    const bool two = (16 + wv) * 8 + q < nblk;
    typedef float v2f __attribute__((ext_vector_type(2)));
    typedef const __attribute__((address_space(4))) float* cfp;
    cfp wt = (cfp)(ka->flags + FL_SIZE + cp * 128);
    v2f g[2][2][2], h[2][2][2];
    auto gh = [&](int i0, int i1) {
#pragma unroll
        for (int p = 0; p < 2; p++) {
            if (p == 0 || two) {
#pragma unroll
                for (int i = i0; i < i1; i++) {
                    const float d = i ? hi_bf(dd[p]) : lo_bf(dd[p]);
#pragma unroll
                    for (int r = 0; r < 8; r++) {
                        const float df = d - wt[r], rb = __builtin_amdgcn_exp2f((df * -14.426950408889634f) * df);
                        const v2f r2 = {rb, rb};
#pragma unroll
                        for (int k = 0; k < 2; k++) {
                            const v2f u = {wt[8 + r * 8 + k * 2], wt[9 + r * 8 + k * 2]}, v = {wt[12 + r * 8 + k * 2], wt[13 + r * 8 + k * 2]};
                            g[p][i][k] = r ? __builtin_elementwise_fma(r2, u, g[p][i][k]) : r2 * u;
                            h[p][i][k] = r ? __builtin_elementwise_fma(r2, v, h[p][i][k]) : r2 * v;
                        }
                    }
                }
            }
        }
    };
    auto odd = [&]() {
        if (E & 1) {
            const int e = ((wv * 8 + q) * 32 + b) * 128 + ln * 2;
            unsigned dv = ka->dist[E - 1];
            asm volatile("" : "+v"(dv));
            dd.x = e == E - 1 ? dv : dd.x; dd.y = e + 128 * 32 * 128 == E - 1 ? dv : dd.y;
        }
    };
    v2u own[2] = {{0, 0}, {0, 0}};
    if (mode == 0) {
        asm volatile("" : "+s"(wm));
        odd();
        gh(0, 1);
        own[0] = *(const v2u*)(lds + OBASE + (wv * 8 + q) * 128 + ln * 2);
        if (two) own[1] = *(const v2u*)(lds + OBASE + ((16 + wv) * 8 + q) * 128 + ln * 2);
        asm volatile("s_waitcnt vmcnt(0)" : "+v"(g[0][0][0]), "+v"(h[0][0][1]) :: "memory");
        __syncthreads();
        const v2u cs = *(const v2u*)(lds + (ln & 15) * 2);
        if (__builtin_amdgcn_readlane(row_scan_add(cs.x), 15) != 0 || __ballot(cs.y != 0)) mode = 1;
    }
    if (mode != 0) {
        acc_fallback(lds, mode, *ka, tid2);
        asm volatile("s_waitcnt vmcnt(0)" ::: "memory");
        __syncthreads();
        if (tid2 == P2_THREADS - 64) { __builtin_amdgcn_fence(__ATOMIC_RELEASE, "agent"); asm volatile("s_waitcnt vmcnt(0)" ::: "memory"); }
        const __amdgpu_buffer_rsrc_t rmm = __builtin_amdgcn_make_buffer_rsrc((void*)ka->m, (short)0, E * 8, 0x00020000);
        const __amdgpu_buffer_rsrc_t rdd = __builtin_amdgcn_make_buffer_rsrc((void*)ka->dist, (short)0, E * 2, 0x00020000);
        const unsigned e = ((wv * 8 + q) * 32 + b) * 128 + ln * 2, e1 = e + 128 * 32 * 128;
        mm[0] = __builtin_amdgcn_raw_buffer_load_b128(rmm, e * 8, 0, 0); mm[1] = __builtin_amdgcn_raw_buffer_load_b128(rmm, e1 * 8, 0, 0);
        dd.x = __builtin_amdgcn_raw_buffer_load_b32(rdd, e * 2, 0, 0); dd.y = __builtin_amdgcn_raw_buffer_load_b32(rdd, e1 * 2, 0, 0);
        asm volatile("" : "+v"(mm[0]), "+v"(mm[1]), "+v"(dd));
        odd();
        gh(0, 1);
        own[0].x = own[0].y = own[1].x = own[1].y = 0x8000u;
        if (mode == 2) exs = 0;
    }
    v2u t0 = {0, 0}, t1 = t0, t2 = t0;
    const v2u add = {1u | (mode ? 256u << q : 0u), qt == 0 ? ((unsigned)__builtin_amdgcn_readfirstlane(exs) & 0xFFFF) << (16 * cp) : 0u};
    if (tid2 == P2_THREADS - 64) asm volatile("s_nop 4\n global_atomic_add_x2 %0, %1, %2, %3 sc0" : "+v"(t0) : "v"(0), "v"(add), "s"(wm) : "memory");
    if (wv == 15) __builtin_amdgcn_s_setprio(3);
    asm volatile("" : "+s"(wt));
    gh(1, 2);
    asm volatile("" : "+v"(g[0][1][0]), "+v"(h[0][1][1]), "+s"(wt) :: "memory");
    float wu[16];
#pragma unroll
    for (int i = 0; i < 16; i++) wu[i] = wt[72 + i];
    const unsigned* prb = ka->praw + (size_t)b * ka->O; unsigned short* outp = ka->out;
    int EP = ka->EP;
    asm volatile("" : "+s"(prb), "+s"(outp), "+s"(EP));
#pragma unroll
    for (int i = 0; i < 16; i++) asm volatile("" : "+s"(wu[i]));
    const __amdgpu_buffer_rsrc_t rpr = __builtin_amdgcn_make_buffer_rsrc((void*)prb, (short)0, (7 * EP + (int)(nblk << 7)) * 4, 0x00020000);
    const __amdgpu_buffer_rsrc_t rou = __builtin_amdgcn_make_buffer_rsrc((void*)outp, (short)0, E * 8, 0x00020000);
    unsigned po[8], vo = ((wv * 8 + q) * 128 + ln * 2) * 4;
#pragma unroll
    for (int j = 1; j < 8; j++) { po[j] = (j ^ q) * EP * 4; asm volatile("" : "+s"(po[j])); }
    asm volatile("" : "+v"(vo));
    if (wv == 15) {
        unsigned lo, hi, x, n;
#define LOOK(nw, od) "s_and_b32 %[x], %[lo], 255\n s_cmp_eq_u32 %[x], 8\n s_cbranch_scc1 2f\n global_load_dwordx2 %[" nw "], %[z], %[wm] sc1\n s_waitcnt vmcnt(2)\n" \
                     "v_mov_b64 v[126:127], %[" od "]\n s_nop 1\n v_readfirstlane_b32 %[lo], v126\n v_readfirstlane_b32 %[hi], v127\n"
        asm volatile("s_nop 4\n global_load_dwordx2 %[t1], %[z], %[wm] sc1\n s_sleep 4\n global_load_dwordx2 %[t2], %[z], %[wm] sc1\n s_waitcnt vmcnt(2)\n"
                     "v_mov_b64 v[126:127], %[t0]\n s_mov_b32 %[n], 0x200000\n s_nop 0\n v_readfirstlane_b32 %[lo], v126\n v_readfirstlane_b32 %[hi], v127\n"
                     "s_add_u32 %[lo], %[lo], %[a0]\n s_addc_u32 %[hi], %[hi], %[a1]\n"
                     "1:\n" LOOK("t0", "t1") LOOK("t1", "t2") LOOK("t2", "t0")
                     "s_sub_u32 %[n], %[n], 1\n s_cbranch_scc0 1b\n 2:\n"
                     : [lo] "=&s"(lo), [hi] "=&s"(hi), [x] "=&s"(x), [n] "=&s"(n), [t0] "+v"(t0), [t1] "+v"(t1), [t2] "+v"(t2)
                     : [z] "v"(0), [wm] "s"(wm), [a0] "s"(add.x), [a1] "s"(add.y) : "memory", "scc", "v126", "v127");
        if (lo & 0xFF00) { __builtin_amdgcn_fence(__ATOMIC_ACQUIRE, "agent"); asm volatile("s_waitcnt vmcnt(0)" ::: "memory"); }
        if (ln == 0) { lds[64] = (lo >> 8) & 255; lds[65] = hi; }
    }
    __syncthreads();
    v2u w[2][8];
#pragma unroll
    for (int p = 0; p < 2; p++) {
        w[p][0] = own[p];
        if (p == 0 || two) {
#pragma unroll
            for (int j = 1; j < 8; j++)
                w[p][j] = __builtin_amdgcn_raw_buffer_load_b64(rpr, vo + p * 65536, po[j], PR_AUX);
        }
    }
    const unsigned fbm = __builtin_amdgcn_readfirstlane(lds[64]);
    unsigned exw = __builtin_amdgcn_readfirstlane(lds[65]);
    if (cp) exw = (exw >> 16) | (exw << 16);
    v2f inv[2], c[2][2][2], mf[2][2][2];
    auto up = [&](const v2f* a, int d, v2f hh, v2f m0) {
        v2f s;
#pragma unroll
        for (int k = 0; k < 4; k++) {
            const v2f ak = {a[k >> 1][k & 1], a[k >> 1][k & 1]}, u = {wu[k * 4 + d * 2], wu[k * 4 + d * 2 + 1]};
            s = k ? __builtin_elementwise_fma(ak, u, s) : ak * u;
        }
        return __builtin_elementwise_fma(s, hh, m0);
    };
#pragma unroll
    for (int k = 0; k < 4; k++) inv[k >> 1][k & 1] = __uint_as_float(((exw >> (8 * k)) & 255) << 23) * (1.0f / 511.0f);
#pragma unroll
    for (int p = 0; p < 2; p++) {
        const unsigned blk = (p * 16 + wv) * 8 + q;
        if (p == 0 || two) {
#pragma unroll
            for (int i = 0; i < 4; i++) { mf[p][i >> 1][i & 1].x = lo_bf(mm[p][i]); mf[p][i >> 1][i & 1].y = hi_bf(mm[p][i]); }
            if (fbm && blk < nblk) {
                v2f x[2][2] = {{{0.f, 0.f}, {0.f, 0.f}}, {{0.f, 0.f}, {0.f, 0.f}}};
                const float* part = ka->part + (size_t)b * ka->O + blk * 128 + ln * 2;
#pragma unroll 1
                for (int j = 0; j < 8; j++)
                    if ((fbm >> j) & 1) {
#pragma unroll
                        for (int i = 0; i < 2; i++) {
                            const v2f y = {part[(size_t)(j * 2) * EP + i], part[(size_t)(j * 2 + 1) * EP + i]};
                            if ((j ^ cp) & 1) x[i][1] += y; else x[i][0] += y;
                        }
                    }
#pragma unroll
                for (int i = 0; i < 4; i++) x[i >> 1][i & 1] *= g[p][i >> 1][i & 1];
#pragma unroll
                for (int i = 0; i < 4; i++) mf[p][i >> 1][i & 1] = up(x[i >> 1], i & 1, h[p][i >> 1][i & 1], mf[p][i >> 1][i & 1]);
            }
#pragma unroll
            for (int i = 0; i < 2; i++)
#pragma unroll
                for (int k = 0; k < 2; k++) {
                    g[p][i][k] *= inv[k];
                    c[p][i][k].x = g[p][i][k].x * -131072.f; c[p][i][k].y = 0.f;
                    asm volatile("" : "+v"(g[p][i][k]), "+v"(c[p][i][k]), "+v"(mf[p][i][k]));
                }
        }
    }
#pragma unroll
    for (int p = 0; p < 2; p++) {
        const unsigned blk = (p * 16 + wv) * 8 + q;
        if (p == 0 || two) {
            v4u r;
#pragma unroll
            for (int i = 0; i < 2; i++) {
                v2f a[2];
#pragma unroll
                for (int k = 0; k < 2; k++) {
                    unsigned lo; int hi;
                    asm("v_add_u32_sdwa %0, %2, %3 dst_sel:DWORD dst_unused:UNUSED_PAD src0_sel:WORD_0 src1_sel:WORD_0\n"
                        "v_add_u32_sdwa %1, sext(%2), sext(%3) dst_sel:DWORD dst_unused:UNUSED_PAD src0_sel:WORD_1 src1_sel:WORD_1\n" : "=&v"(lo), "=&v"(hi) : "v"(w[p][k][i]), "v"(w[p][k + 2][i]));
#pragma unroll
                    for (int j = 4; j < 8; j += 2)
                        asm("v_add_u32_sdwa %0, %0, %2 dst_sel:DWORD dst_unused:UNUSED_PAD src0_sel:DWORD src1_sel:WORD_0\n"
                            "v_add_u32_sdwa %1, %1, sext(%2) dst_sel:DWORD dst_unused:UNUSED_PAD src0_sel:DWORD src1_sel:WORD_1\n" : "+v"(lo), "+v"(hi) : "v"(w[p][k + j][i]));
                    const v2f f = {(float)lo, (float)hi};
                    a[k] = __builtin_elementwise_fma(f, g[p][i][k], c[p][i][k]);
                }
#pragma unroll
                for (int d = 0; d < 2; d++) {
                    const v2f s = up(a, d, h[p][i][d], mf[p][i][d]);
                    asm("v_cvt_pk_bf16_f32 %0, %1, %2" : "=v"(r[i * 2 + d]) : "v"(s.x), "v"(s.y));
                }
            }
            __builtin_amdgcn_raw_buffer_store_b128(r, rou, ((blk * 32 + b) * 128 + ln * 2) * 8, 0, OUT_AUX);
        }
    }
    asm volatile("" :: "v"(t0), "v"(t1), "v"(t2));
}

extern "C" __global__ __launch_bounds__(256) void k_naive(const int* __restrict__ idx_in, const int* __restrict__ idx_out,
                                               const unsigned short* __restrict__ angle, const unsigned short* __restrict__ ang_freq,
                                               const unsigned short* __restrict__ m, const unsigned short* __restrict__ w_down,
                                               float* __restrict__ part, int T, int EP) {
    const float freq = bf2f(ang_freq[0]);
    for (long long t = (long long)blockIdx.x * 256 + threadIdx.x; t < T; t += (long long)gridDim.x * 256) {
        v2u mv = ((const v2u*)m)[idx_in[t]];
        float a[4] = {lo_bf(mv.x), hi_bf(mv.x), lo_bf(mv.y), hi_bf(mv.y)};
        float c = __cosf(freq * bf2f(angle[t]));
        int eo = idx_out[t];
#pragma unroll
        for (int d = 0; d < 4; d++) {
            float s = 0.f;
#pragma unroll
            for (int k = 0; k < 4; k++) s += a[k] * bf2f(w_down[k * 4 + d]);
            atomicAdd(&part[(size_t)d * EP + eo], s * c);
        }
    }
}

__global__ __launch_bounds__(256) void k_final(const unsigned short* __restrict__ m, const unsigned short* __restrict__ dist,
                                               const unsigned short* __restrict__ centers, const unsigned short* __restrict__ w_rbf,
                                               const unsigned short* __restrict__ w_sbf, const unsigned short* __restrict__ w_up,
                                               const float* __restrict__ part, unsigned short* __restrict__ out, int E, int EP) {
    const int e = blockIdx.x * 256 + threadIdx.x;
    if (e >= E) return;
    float a[4], g[4], h[4];
#pragma unroll
    for (int k = 0; k < 4; k++) a[k] = part[(size_t)k * EP + e];
    edge_gh(bf2f(dist[e]), centers, w_sbf, w_rbf, g, h);
    const v2u mv = ((const v2u*)m)[e];
    unsigned wu[8];
#pragma unroll
    for (int i = 0; i < 8; i++) wu[i] = ((cptr)w_up)[i];
    ((v2u*)out)[e] = edge_out(a, g, h, mv.x, mv.y, wu);
}

extern "C" void kernel_launch(void* const* d_in, const int* in_sizes, int n_in,
                              void* d_out, int out_size, void* d_ws, size_t ws_size,
                              hipStream_t stream) {
    const unsigned short* m = (const unsigned short*)d_in[0];
    const int* idx_in = (const int*)d_in[1];
    const int* idx_out = (const int*)d_in[2];
    const unsigned short* dist = (const unsigned short*)d_in[3];
    const unsigned short* angle = (const unsigned short*)d_in[4];
    const unsigned short* centers = (const unsigned short*)d_in[5];
    const unsigned short* ang_freq = (const unsigned short*)d_in[6];
    const unsigned short* w_rbf = (const unsigned short*)d_in[7];
    const unsigned short* w_sbf = (const unsigned short*)d_in[8];
    const unsigned short* w_down = (const unsigned short*)d_in[9];
    const unsigned short* w_up = (const unsigned short*)d_in[10];
    const int E = in_sizes[0] / 4;
    const int T = in_sizes[1];
    const int NR = T / (P1_GRID * CH);
    const int per = ((T - NR * (P1_GRID * CH) + P1_GRID - 1) / P1_GRID + 3) / 4 * 4;
    const unsigned O = (unsigned)(((E + 127) / 128 + 31) / 32) * 128;
    const unsigned I = (unsigned)(E + 31) / 32;
    const int EP = (int)O * 32;
    Lay ly;
    const unsigned meanr = (unsigned)(((size_t)NR * CH + per) / NT);
    ly.K0 = meanr >= 30 ? (meanr - 30) / 24 : 0;
    ly.KX = ly.K0 + ly.K0 * 2 / 3 + 4;
#define FCAP 4928
    ly.fcap = meanr >= 100 ? FCAP : 8192;
    const size_t lines = (size_t)PLANE * (ly.KX + 1);
    char* ws = (char*)d_ws;
    size_t o = 0;
    unsigned* praw = (unsigned*)(ws + o); o += (size_t)8 * EP * 4; o = (o + 255) / 256 * 256;
    unsigned* wgmode = (unsigned*)(ws + o); o += 16384;
    unsigned* mdp = (unsigned*)(ws + o); o += (size_t)2 * 32 * LMAX * 4;
    unsigned* tab = (unsigned*)(ws + o); o += (size_t)NT * NWS * 12;
    unsigned* flags = (unsigned*)(ws + o); o += FL_SIZE * 4 + 1024; o = (o + 255) / 256 * 256;
    o += (size_t)NWS * ly.fcap * 16;
    char* rec = ws + o;
    ly.dumu = (unsigned)(lines * 8);
    o += 0x200000 + ((size_t)ly.dumu + 64) * 16 - (size_t)PLANE * 128;
    float* part = (float*)(ws + o); o += (size_t)16 * EP * 4;
    const bool ok = E >= 16384 && O <= LMAX && I <= LMAX && T > 0 && T < (1 << 29) && ly.KX <= 30 && o <= ws_size;
    if (!ok) {
        part = (float*)ws;
        const int EQ = (E + 3) / 4 * 4;
        (void)hipMemsetAsync(part, 0, (size_t)4 * EQ * 4, stream);
        if (T > 0) k_naive<<<2048, 256, 0, stream>>>(idx_in, idx_out, angle, ang_freq, m, w_down, part, T, EQ);
        k_final<<<(E + 255) / 256, 256, 0, stream>>>(m, dist, centers, w_rbf, w_sbf, w_up, part, (unsigned short*)d_out, E, EQ);
        return;
    }
    k_bin<<<P1_GRID, P1_THREADS, 0, stream>>>(idx_in, idx_out, angle, ang_freq, m, w_down, mdp, E, T, rec, tab, flags, NR, per, ly, wgmode, centers, w_sbf, w_rbf, w_up);
    AccArgs aa;
    aa.rec = rec; aa.tab = tab; aa.flags = flags; aa.mdp = mdp; aa.part = part; aa.praw = praw; aa.wgmode = wgmode;
    aa.idx_in = idx_in; aa.idx_out = idx_out; aa.angle = angle; aa.ang_freq = ang_freq; aa.m = m; aa.w_down = w_down;
    aa.EP = EP; aa.O = O; aa.T = T; aa.c4 = (float)((double)E / 144.0);
    aa.dist = dist; aa.centers = centers; aa.w_rbf = w_rbf; aa.w_sbf = w_sbf; aa.w_up = w_up; aa.out = (unsigned short*)d_out; aa.E = E; aa.unused = 0;
    aa.ly = ly;
    k_acc<<<256, P2_THREADS, 0, stream>>>(aa);
}
```

```cpp
#include <hip/hip_runtime.h>
#include <hip/hip_bf16.h>
#include <stdint.h>

typedef int v4i __attribute__((ext_vector_type(4)));
typedef unsigned v4u __attribute__((ext_vector_type(4)));
typedef unsigned v2u __attribute__((ext_vector_type(2)));
typedef int v4i_rs __attribute__((ext_vector_type(4)));
typedef __attribute__((address_space(3))) unsigned lds_u32;
typedef __attribute__((address_space(3))) v4u lds_v4u;

static __device__ __forceinline__ float bf2f(unsigned short v) { return __uint_as_float(((unsigned)v) << 16); }
static __device__ __forceinline__ unsigned f2bf_bits(float f) {
    unsigned u = __float_as_uint(f);
    u += 0x7FFF + ((u >> 16) & 1);
    return u >> 16;
}
static __device__ __forceinline__ float lo_bf(unsigned u) { return __uint_as_float(u << 16); }
static __device__ __forceinline__ float hi_bf(unsigned u) { return __uint_as_float(u & 0xFFFF0000u); }
static __device__ __forceinline__ v4i_rs mk_rsrc(const void* p, unsigned bytes) {
    unsigned long long a = (unsigned long long)p;
    v4i_rs r = {(int)(unsigned)a, (int)(unsigned)((a >> 32) & 0xFFFF), (int)bytes, 0x00020000};
    return r;
}
static __device__ __forceinline__ lds_u32* ldsp(unsigned byte) { return (lds_u32*)(size_t)byte; }

#define P1_THREADS 1024
#define P1_GRID 256
#define NWS 256
#define CH (P1_THREADS * 4)
#define NT 1024
#define LMAX 20480
#define P2_THREADS 1024
#define OOB 0x80000000u
#define OBASE 20480
#define IDXB 4096
#define ST_AUX 16
#define LB 4096
#define LS 132
#define TINC 683
#define WL_OFF (LB + NT * LS)
#define MS_OFF (WL_OFF + 16 * 1024)
#define TB_OFF (MS_OFF + 128 + 320)
#define PC_OFF (TB_OFF + 32 * 33 * 4)
#define BIN_LDS (PC_OFF + 128)
#define SINC ((1u << 5) | (1u << 15) | (1u << 24))
#define SW0 (((tid & 3) << 6) | ((run & 16) << 1))
#define NSTAT 14
#define WM_CTR 1024
#define WM_BOX (WM_CTR + 128)
#define WM_LOC (WM_BOX + 256)
#define NTH "nt"
#define OUT_AUX 16
#define PR_AUX 18
#define TCH 7
#define CZERO ((512u << 1) | (512u << 11) | (512u << 22))
#define FL_SIZE (2 * P1_GRID * 2)

#define CB(c) ((((c) >> 3) << 15) | (((c) & 7) << 10))
#define TO(t) ((((t) & 15) * 4) | (((t) >> 6) << 6) | ((((t) >> 4) & 3) << 13))
#define HO 512
struct Grp { v4i io; v4i ii; v2u an; };
static __device__ __forceinline__ void load_grp(Grp& r, v4i_rs ro, v4i_rs ri, v4i_rs ra, unsigned c, unsigned vo16, unsigned vo8) {
    asm volatile("buffer_load_dwordx4 %0, %1, %2, %3 offen nt" : "=v"(r.io) : "v"(vo16), "s"(ro), "s"(c * 2));
    asm volatile("buffer_load_dwordx4 %0, %1, %2, %3 offen nt" : "=v"(r.ii) : "v"(vo16), "s"(ri), "s"(c * 2));
    asm volatile("buffer_load_dwordx2 %0, %1, %2, %3 offen nt" : "=v"(r.an) : "v"(vo8), "s"(ra), "s"(c));
}
#define TELLC ((rd & 6) == 0)

static __device__ __forceinline__ unsigned wave_scan_max(unsigned x) {
    x = max(x, (unsigned)__builtin_amdgcn_update_dpp(0, (int)x, 0x111, 0xF, 0xF, false));
    x = max(x, (unsigned)__builtin_amdgcn_update_dpp(0, (int)x, 0x112, 0xF, 0xF, false));
    x = max(x, (unsigned)__builtin_amdgcn_update_dpp(0, (int)x, 0x114, 0xF, 0xF, false));
    x = max(x, (unsigned)__builtin_amdgcn_update_dpp(0, (int)x, 0x118, 0xF, 0xF, false));
    x = max(x, (unsigned)__builtin_amdgcn_update_dpp(0, (int)x, 0x142, 0xA, 0xF, false));
    x = max(x, (unsigned)__builtin_amdgcn_update_dpp(0, (int)x, 0x143, 0xC, 0xF, false));
    return x;
}
static __device__ __forceinline__ unsigned row_scan_add(unsigned x) {
    x += (unsigned)__builtin_amdgcn_update_dpp(0, (int)x, 0x111, 0xF, 0xF, false);
    x += (unsigned)__builtin_amdgcn_update_dpp(0, (int)x, 0x112, 0xF, 0xF, false);
    x += (unsigned)__builtin_amdgcn_update_dpp(0, (int)x, 0x114, 0xF, 0xF, false);
    x += (unsigned)__builtin_amdgcn_update_dpp(0, (int)x, 0x118, 0xF, 0xF, false);
    return x;
}
static __device__ __forceinline__ unsigned wave_scan_add(unsigned x) {
    x = row_scan_add(x);
    x += (unsigned)__builtin_amdgcn_update_dpp(0, (int)x, 0x142, 0xA, 0xF, false);
    x += (unsigned)__builtin_amdgcn_update_dpp(0, (int)x, 0x143, 0xC, 0xF, false);
    return x;
}

static __device__ __forceinline__ float wave_scan_addf(float x) {
    x += __int_as_float(__builtin_amdgcn_update_dpp(0, __float_as_int(x), 0x111, 0xF, 0xF, false));
    x += __int_as_float(__builtin_amdgcn_update_dpp(0, __float_as_int(x), 0x112, 0xF, 0xF, false));
    x += __int_as_float(__builtin_amdgcn_update_dpp(0, __float_as_int(x), 0x114, 0xF, 0xF, false));
    x += __int_as_float(__builtin_amdgcn_update_dpp(0, __float_as_int(x), 0x118, 0xF, 0xF, false));
    x += __int_as_float(__builtin_amdgcn_update_dpp(0, __float_as_int(x), 0x142, 0xA, 0xF, false));
    x += __int_as_float(__builtin_amdgcn_update_dpp(0, __float_as_int(x), 0x143, 0xC, 0xF, false));
    return x;
}

struct Lay { unsigned K0, KX, dumu, fcap; };
#define PLANE (NWS * NT)
static __device__ __forceinline__ unsigned blk_line(unsigned w) { return (((w & 3) | ((w >> 3) << 2)) << 11) | ((w & 4) << 4); }
static __device__ __forceinline__ unsigned grp_line(unsigned q) { return ((q & 1) << 7) | ((q & 2) << 9); }
static __device__ __forceinline__ unsigned slot_line(unsigned s) { return (s >> 2) | ((s & 3) << 8); }
static __device__ __forceinline__ unsigned frag_unit(unsigned w, const Lay& ly) { return PLANE * 8 - (w + 1) * ly.fcap; }
static __device__ __forceinline__ char* rec_base(const char* p) { return (char*)p + ((int)(-(unsigned)(size_t)p & 0x1FFFFF) - PLANE * 128); }

static __device__ __forceinline__ void conv_tile(unsigned io, unsigned ii, unsigned& t4, unsigned& x) {
    unsigned y;
    asm("v_lshrrev_b32 %0, 5, %3\n v_and_b32 %1, 31, %4\n v_and_b32 %2, 0x7c, %0\n v_lshl_or_b32 %2, %1, 7, %2" : "=&v"(x), "=&v"(y), "=&v"(t4) : "v"(io), "v"(ii));
}
static __device__ __forceinline__ void conv_rec(unsigned io, unsigned ii, float ang, float frev, unsigned& A, unsigned& cq, unsigned& seen) {
    unsigned y;
    asm("v_lshrrev_b32 %1, 3, %3\n v_bfi_b32 %0, %4, %2, %0\n v_add_u32 %1, %5, %1\n v_lshl_add_u32 %0, %0, 17, %1"
        : "+v"(A), "=&v"(y) : "v"(io), "v"(ii), "s"(127u), "n"(IDXB << 17));
    float c = __builtin_amdgcn_cosf(__builtin_amdgcn_fractf(frev * ang));
    const unsigned cb = __float_as_uint(__builtin_fmaf(c, 511.0f, 12583424.0f));
    seen |= cb;
    cq = cb & 0x3FFu;
}
static __device__ __forceinline__ unsigned pad_rec(unsigned q) { return (0x7FFFu - (q & 511)) << 17; }

static __device__ __forceinline__ void prep_md(const v2u* mv, unsigned dst, const unsigned short* w_down, unsigned* mdp, unsigned plane, unsigned* mx, float* sq) {
    float wd[16];
#pragma unroll
    for (int i = 0; i < 8; i++) { unsigned u = ((const __attribute__((address_space(4))) unsigned*)w_down)[i]; wd[2 * i] = lo_bf(u); wd[2 * i + 1] = hi_bf(u); }
    unsigned o[2][4];
#pragma unroll
    for (int j = 0; j < 4; j++) {
        const float a[4] = {lo_bf(mv[j].x), hi_bf(mv[j].x), lo_bf(mv[j].y), hi_bf(mv[j].y)};
        float s[4];
#pragma unroll
        for (int d = 0; d < 4; d++) {
            s[d] = 0.f;
#pragma unroll
            for (int k = 0; k < 4; k++) s[d] += a[k] * wd[k * 4 + d];
            sq[d] += s[d] * s[d];
            mx[d] = max(mx[d], __float_as_uint(s[d]) & 0x7FFFFFFFu);
        }
#pragma unroll
        for (int p = 0; p < 2; p++) {
            const unsigned lo = f2bf_bits(s[2 * p]) & 0xFFFF;
            const unsigned sb = __float_as_uint(s[2 * p + 1]);
            const unsigned h = (unsigned)max((int)((sb & 0x7FFFFFFFu) - lo + 0x8000u), 0) & 0x7FFF0000u;
            o[p][j] = lo | h | (sb & 0x80000000u);
        }
    }
#pragma unroll
    for (int p = 0; p < 2; p++) {
        v4u v = {o[p][0], o[p][1], o[p][2], o[p][3]};
        *(v4u*)(mdp + (size_t)p * plane + dst) = v;
    }
}

static __device__ __forceinline__ unsigned touch_code(int tid) {
    unsigned alo, ahi, blo, r = 0;
    asm volatile("s_getpc_b64 s[96:97]\n s_add_u32 %0, s96, k_bin@rel32@lo+4\n s_addc_u32 %1, s97, k_bin@rel32@hi+12\n"
                 "s_getpc_b64 s[96:97]\n s_add_u32 %2, s96, k_naive@rel32@lo+4\n" : "=&s"(alo), "=&s"(ahi), "=&s"(blo) :: "scc", "s96", "s97");
    const unsigned size = blo - alo < 65536u ? blo - alo : 0u;
    const v4i_rs rc = {(int)alo, (int)(ahi & 0xFFFF), (int)size, 0x00020000};
    if (tid < 512) asm volatile("buffer_load_dword %0, %1, %2, 0 offen" : "=v"(r) : "v"(tid * 128), "s"(rc));
    return r;
}

struct Held { unsigned a[4], t[4], A[4], cd[4]; unsigned long long m[4]; };
struct Conv { unsigned A[4], t4[4], cq[4]; };
#define HB(k) "s_mov_b64 exec, %[m" #k "]\n ds_write_b32 %[a" #k "], %[A" #k "]\n ds_or_b32 %[t" #k "], %[c" #k "]\n"
static __device__ __forceinline__ void held_out(const Held& H) {
    asm volatile(HB(0) HB(1) HB(2) HB(3) "s_mov_b64 exec, -1\n"
                 :: [m0] "s"(H.m[0]), [m1] "s"(H.m[1]), [m2] "s"(H.m[2]), [m3] "s"(H.m[3]), [a0] "v"(H.a[0]), [a1] "v"(H.a[1]), [a2] "v"(H.a[2]), [a3] "v"(H.a[3]),
                   [t0] "v"(H.t[0]), [t1] "v"(H.t[1]), [t2] "v"(H.t[2]), [t3] "v"(H.t[3]),
                   [A0] "v"(H.A[0]), [A1] "v"(H.A[1]), [A2] "v"(H.A[2]), [A3] "v"(H.A[3]), [c0] "v"(H.cd[0]), [c1] "v"(H.cd[1]), [c2] "v"(H.cd[2]), [c3] "v"(H.cd[3])
                 : "memory");
}
static __device__ __forceinline__ void tickets(unsigned* v, const Conv& c, unsigned inc) {
    asm volatile("ds_add_rtn_u32 %0, %4, %8\n ds_add_rtn_u32 %1, %5, %8\n ds_add_rtn_u32 %2, %6, %8\n ds_add_rtn_u32 %3, %7, %8\n"
                 : "=&v"(v[0]), "=&v"(v[1]), "=&v"(v[2]), "=&v"(v[3]) : "v"(c.t4[0]), "v"(c.t4[1]), "v"(c.t4[2]), "v"(c.t4[3]), "v"(inc) : "memory");
}
static __device__ __forceinline__ void tickets_masked(unsigned* v, const Conv& c, unsigned inc, const unsigned long long* vm) {
#pragma unroll
    for (int k = 0; k < 4; k++) {
        v[k] = 0;
        asm volatile("s_mov_b64 exec, %3\n ds_add_rtn_u32 %0, %1, %2\n s_mov_b64 exec, -1" : "+v"(v[k]) : "v"(c.t4[k]), "v"(inc), "s"(vm[k]) : "memory");
    }
}
static __device__ __forceinline__ void place(Held& H, unsigned* v, Conv& c, const unsigned long long* vm, unsigned lb) {
    unsigned bt[4];
#pragma unroll
    for (int k = 0; k < 4; k++) asm("v_mad_u32_u24 %0, %1, %2, %3" : "=v"(bt[k]) : "v"(c.t4[k]), "n"(LS / 4), "v"(lb));
    asm volatile("s_waitcnt lgkmcnt(0)" : "+v"(v[0]), "+v"(v[1]), "+v"(v[2]), "+v"(v[3]));
#pragma unroll
    for (int k = 0; k < 4; k++) {
        H.m[k] = __ballot(v[k] >= 16384u);
        unsigned sh;
        asm("v_lshrrev_b32 %0, 7, %3\n v_lshrrev_b32 %2, 6, %3\n v_and_b32 %0, 0x7c, %0\n v_or_b32 %1, 12, %0\n v_add_u32 %0, %0, %4\n v_add_u32 %1, %1, %4"
            : "=&v"(H.a[k]), "=&v"(H.t[k]), "=&v"(sh) : "v"(v[k]), "v"(bt[k]));
        H.A[k] = c.A[k];
        asm("v_lshlrev_b32 %0, %1, %2" : "=v"(H.cd[k]) : "v"(sh), "v"(c.cq[k]));
        asm volatile("s_andn2_b64 exec, %[w], %[m]\n ds_write_b32 %[a], %[A]\n ds_or_b32 %[t], %[c]\n s_mov_b64 exec, -1\n"
                     :: [w] "s"(vm[k]), [m] "s"(H.m[k]), [a] "v"(H.a[k]), [A] "v"(H.A[k]), [t] "v"(H.t[k]), [c] "v"(H.cd[k]) : "memory", "scc");
    }
}
struct Own { v2u e; unsigned pc, fm, ovf, c0, c1; };
static __device__ __forceinline__ v4u read_unit(unsigned la) {
    const lds_u32* p = ldsp(la);
    v4u v = {p[0], p[1], p[2], p[3]};
    return v;
}
typedef __attribute__((address_space(3))) v2u lds_v2u;
static __device__ __forceinline__ unsigned long long low_lanes(unsigned groups) { return groups >= 8 ? ~0ull : (1ull << (groups * 8)) - 1; }
static __device__ __forceinline__ void flush_pass(v2u e, v4u v, unsigned long long act, unsigned k16, __amdgpu_buffer_rsrc_t rr) {
    unsigned vo;
    asm volatile("s_mov_b64 exec, %1\n ds_write_b32 %2, %3 offset:12\n s_mov_b64 exec, -1\n v_cndmask_b32 %0, %4, %5, %1"
                 : "=v"(vo) : "s"(act), "v"(e.x), "v"(0u), "v"(OOB), "v"(e.y + k16) : "memory");
    __builtin_amdgcn_raw_buffer_store_b128(v, rr, vo, 0, ST_AUX);
}
static __device__ __forceinline__ void flush(Own& o, unsigned& cnt, int tid, unsigned sinc, __amdgpu_buffer_rsrc_t rr) {
    const bool full = cnt >= 16384u;
    const unsigned long long bal = __ballot(full);
    const unsigned nfull = __popcll(bal);
    const unsigned wl = WL_OFF + (tid >> 6) * 1024;
    if (full) {
        unsigned sl;
        asm volatile("ds_add_rtn_u32 %0, %1, %2" : "=v"(sl) : "v"(o.pc), "v"(sinc) : "memory");
        const unsigned pos = __builtin_amdgcn_mbcnt_hi((unsigned)(bal >> 32), __builtin_amdgcn_mbcnt_lo((unsigned)bal, 0));
        cnt -= 24 * TINC;
        *ldsp(tid * 4) = cnt;
        o.ovf |= cnt;
        v2u e = o.e;
        o.fm >>= 1;
        o.e.y = min((o.e.y + PLANE * 128) ^ (o.fm & 8192u), OOB);
        const bool late = o.pc >= PC_OFF + 128;
        o.pc += 16;
        asm volatile("s_waitcnt lgkmcnt(0)" : "+v"(sl) :: "memory");
        sl ^= (sl << 3) & 0x18000300u;
        if (__builtin_expect(__ballot(late) != 0, 0)) {
            if (late) sl = (tid & 255) * SINC;
            else { o.c1 = __builtin_amdgcn_alignbit(o.c1, o.c0, 24); o.c0 = __builtin_amdgcn_alignbit(o.c0, sl, 24); }
        } else { o.c1 = __builtin_amdgcn_alignbit(o.c1, o.c0, 24); o.c0 = __builtin_amdgcn_alignbit(o.c0, sl, 24); }
        e.y |= sl & 0x19F80u;
        *(lds_v2u*)(size_t)(wl + pos * 8) = e;
    }
    const unsigned g = (tid >> 3) & 7, k16 = (tid & 7) * 16;
    v2u e0 = *(lds_v2u*)(size_t)(wl + g * 8), e1 = *(lds_v2u*)(size_t)(wl + g * 8 + 64);
    e0.x += k16; e1.x += k16;
    const v4u v0 = read_unit(e0.x), v1 = read_unit(e1.x);
    asm volatile("" ::: "memory");
    flush_pass(e0, v0, low_lanes(nfull), k16, rr);
    flush_pass(e1, v1, nfull > 8 ? low_lanes(nfull - 8) : 0ull, k16, rr);
    for (unsigned p = 16; p < nfull; p += 8) {
        v2u e = *(lds_v2u*)(size_t)(wl + (p + g) * 8);
        e.x += k16;
        const v4u v = read_unit(e.x);
        asm volatile("" ::: "memory");
        flush_pass(e, v, low_lanes(nfull - p), k16, rr);
    }
    asm volatile("s_waitcnt lgkmcnt(0)" ::: "memory");
}

extern "C" __global__ __launch_bounds__(P1_THREADS) void k_bin(const int* __restrict__ idx_in, const int* __restrict__ idx_out,
                                                    const unsigned short* __restrict__ angle, const unsigned short* __restrict__ ang_freq,
                                                    const unsigned short* __restrict__ m, const unsigned short* __restrict__ w_down,
                                                    unsigned* __restrict__ mdp, int E, int T,
                                                    char* __restrict__ rec_end, unsigned* __restrict__ tab, unsigned* __restrict__ flags,
                                                    int NR, int per, Lay ly, unsigned* __restrict__ wgmode,
                                                    const unsigned short* __restrict__ centers, const unsigned short* __restrict__ w_sbf,
                                                    const unsigned short* __restrict__ w_rbf, const unsigned short* __restrict__ w_up) {
    __shared__ __attribute__((aligned(16))) unsigned L[BIN_LDS / 4];
    const int tid = threadIdx.x;
    const unsigned run = blockIdx.x;
    char* const rec = rec_base(rec_end);
    float freq;
    const v4i_rs ro = mk_rsrc(idx_out, (unsigned)T * 4), ri = mk_rsrc(idx_in, (unsigned)T * 4), ra = mk_rsrc(angle, (unsigned)T * 2);
    const __amdgpu_buffer_rsrc_t rr = __builtin_amdgcn_make_buffer_rsrc((void*)rec, (short)0,
        (int)(ly.dumu * 16),
        0x00020000);
    const unsigned vo16 = TO(tid) * 4, vo8 = TO(tid) * 2;
    const unsigned shard = ((run >> 3) ^ (run >> 5) ^ run) & 3;
    const bool leader = run == shard * 8;
    const v4i_rs rw = mk_rsrc(wgmode + WM_CTR, (128 + 256 + 2048) * 4);
    const int wvi = __builtin_amdgcn_readfirstlane(tid >> 6), wave0 = wvi == 0;
    const unsigned ctr_o = shard * 128, box_o = (128 + shard * 64 + ((run & 7) | ((run >> 5) << 3))) * 4;
#define PLO(j) (((j) & 1) * 8192 + ((j) & 2) * 2048 + ((j) >> 2) * 16384 + ((j) >> 1) * 1280)
    const bool prober = ((run ^ (run >> 2)) & 8) == 0;
    const unsigned loc_o = (tid & 63) - 1u < 8u ? (128 + 256 + (run >> 4) * 128 + ((tid & 63) - 1) * 16 + (run & 15)) * 4 : OOB;
    const unsigned ppg = (((run & 7) + 1) * PLANE + (run >> 4) * 16384) * 128;
    if (wave0) {
        const v4u z = {0, 0, 0, 0};
        asm volatile("buffer_store_dwordx4 %0, %1, %2, 0 offen sc0 sc1" :: "v"(z), "v"(prober ? ppg + PLO(tid >> 3) + (tid & 7) * 16 : OOB), "s"(mk_rsrc(rec, ly.dumu * 16)) : "memory");
        asm volatile("buffer_store_dword %0, %1, %2, 0 offen sc1" :: "v"(0u), "v"(tid == 0 ? (leader ? ctr_o : box_o) : loc_o), "s"(rw) : "memory");
    }
    unsigned tc0 = touch_code(tid);
    unsigned fbits;
    asm volatile("buffer_load_ushort %0, off, %1, 0" : "=v"(fbits) : "s"(mk_rsrc(ang_freq, 2)));
    v2u mv[4];
    const unsigned gp = run * 80 + (tid >> 5) * 4;
    {
        const v4i_rs rm = mk_rsrc(m, (unsigned)E * 8);
        const unsigned o = tid < 640 ? (((gp & ~255u) | ((gp >> 2) & 63)) * 32 + (tid & 31)) * 8 : OOB;
#pragma unroll
        for (int j = 0; j < 4; j++) asm volatile("buffer_load_dwordx2 %0, %1, %2, %3 offen " NTH : "=v"(mv[j]) : "v"(o), "s"(rm), "s"(j * 64 * 32 * 8));
    }
    Grp tg; bool tv[4];
    const unsigned tbase = (unsigned)NR * (P1_GRID * CH) + run * per;
    {
        const bool in = tid * 4 < per;
#pragma unroll
        for (int k = 0; k < 4; k++) tv[k] = in && tbase + tid * 4 + k < (unsigned)T;
        const unsigned o16 = in ? tid * 16 : OOB, o8 = in ? tid * 8 : OOB;
        asm volatile("buffer_load_dwordx4 %0, %1, %2, %3 offen nt" : "=v"(tg.io) : "v"(o16), "s"(ro), "s"(tbase * 4));
        asm volatile("buffer_load_dwordx4 %0, %1, %2, %3 offen nt" : "=v"(tg.ii) : "v"(o16), "s"(ri), "s"(tbase * 4));
        asm volatile("buffer_load_dwordx2 %0, %1, %2, %3 offen nt" : "=v"(tg.an) : "v"(o8), "s"(ra), "s"(tbase * 2));
    }
    Grp cur[2];
    load_grp(cur[0], ro, ri, ra, NR > 0 ? CB(run) * 2 : 0x7F000000u, vo16, vo8);
    load_grp(cur[1], ro, ri, ra, NR > 1 ? CB(run + P1_GRID) * 2 : 0x7F000000u, vo16, vo8);
    Own own;
    own.e.x = LB + tid * LS; own.pc = PC_OFF + (tid >> 8) * 4; own.c0 = own.c1 = 0;
    own.ovf = (unsigned)(size_t)L << 14;
    own.fm = 0; own.e.y = OOB;
    *ldsp(tid * 4) = 64;
#pragma unroll
    for (int i = 0; i < 8; i++) *ldsp(LB + tid * LS + i * 16 + 12) = 0;
    { const v2u z = {0, 0}; *(lds_v2u*)(size_t)(WL_OFF + (tid >> 6) * 1024 + (tid & 63) * 8) = z; }
    if (tid < 32) *ldsp(MS_OFF + tid * 4) = 0;
    if (tid < 32) *ldsp(PC_OFF + tid * 4) = (SW0 & 0xFC) * SINC;
    if (run == 0 && tid < 64) wgmode[(tid >> 1) * 32 + (tid & 1)] = 0;
    if (run == 1 && tid < 64) {
        v4u v = {pad_rec(tid), pad_rec(tid + 64), pad_rec(tid + 128), CZERO};
        *(v4u*)(rec + ((size_t)ly.dumu + tid) * 16) = v;
    }
    if (run == 9 && tid >= 768 && (tid & 127) < 88) {
        const unsigned i = tid & 127, x = (tid >> 6) & 2, j = i - 8;
        const unsigned short* p = i < 8 ? centers + i : i >= 72 ? w_up + ((i - 72) ^ (x * 4)) : (j & 4) ? w_rbf + (j >> 3) * 4 + (j & 3) : w_sbf + (j >> 3) * 4 + ((j & 3) ^ x);
        __hip_atomic_store(flags + FL_SIZE + (tid - 768), (unsigned)*p << 16, __ATOMIC_RELAXED, __HIP_MEMORY_SCOPE_AGENT);
    }
    unsigned ans = 2;
    const unsigned tell_o = (tid & 63) < 16 ? (128 + 256 + (run >> 4) * 128 + (run & 7) * 16 + (tid & 63)) * 4 : OOB;
    if (wave0 && prober) {
        asm volatile("s_waitcnt vmcnt(16) lgkmcnt(0)" ::: "memory");
        if ((run & 7) < ly.KX) {
            unsigned long long t[7];
            unsigned x;
#define PRB1(i, j) "s_memtime %" #i "\n s_load_dword %7, %8, %" #j "\n s_waitcnt lgkmcnt(0)\n"
            asm volatile(PRB1(0, 9) PRB1(1, 10) PRB1(2, 11) PRB1(3, 12) PRB1(4, 13) PRB1(5, 14) "s_memtime %6\n s_waitcnt lgkmcnt(0)"
                         : "=&s"(t[0]), "=&s"(t[1]), "=&s"(t[2]), "=&s"(t[3]), "=&s"(t[4]), "=&s"(t[5]), "=&s"(t[6]), "=&s"(x)
                         : "s"(rec + ppg), "n"(PLO(0)), "n"(PLO(1)), "n"(PLO(2)), "n"(PLO(3)), "n"(PLO(4)), "n"(PLO(5)) : "memory");
            unsigned d[6];
#pragma unroll
            for (int i = 0; i < 6; i++) d[i] = (unsigned)t[i + 1] - (unsigned)t[i];
            ans = 2 | (((min(min(d[0], d[2]), d[4]) > min(min(d[1], d[3]), d[5]) ? 1 : 0) ^ (run >> 2)) & 1);
        }
        asm volatile("buffer_store_dword %0, %1, %2, 0 offen sc1" :: "v"(ans), "v"(tell_o), "s"(rw) : "memory");
    }
    unsigned seen = 0;
    auto convert1 = [&](Grp& gp, Conv& o) {
        conv_tile(gp.io.x, gp.ii.x, o.t4[0], o.A[0]); conv_tile(gp.io.y, gp.ii.y, o.t4[1], o.A[1]);
        conv_tile(gp.io.z, gp.ii.z, o.t4[2], o.A[2]); conv_tile(gp.io.w, gp.ii.w, o.t4[3], o.A[3]);
    };
    auto convert2 = [&](Grp& gp, Conv& o) {
        asm volatile("" : "+v"(gp.an), "+v"(o.A[0]), "+v"(o.A[1]), "+v"(o.A[2]), "+v"(o.A[3]));
        conv_rec(gp.io.x, gp.ii.x, lo_bf(gp.an.x), freq, o.A[0], o.cq[0], seen); conv_rec(gp.io.y, gp.ii.y, hi_bf(gp.an.x), freq, o.A[1], o.cq[1], seen);
        conv_rec(gp.io.z, gp.ii.z, lo_bf(gp.an.y), freq, o.A[2], o.cq[2], seen); conv_rec(gp.io.w, gp.ii.w, hi_bf(gp.an.y), freq, o.A[3], o.cq[3], seen);
#pragma unroll
        for (int k = 0; k < 4; k++) asm volatile("" : "+v"(o.A[k]), "+v"(o.cq[k]));
    };
    const unsigned tinc = TINC, lb7 = LB, sinc = SINC;
    Held H;
    {
        asm volatile("s_waitcnt vmcnt(9)" : "+v"(mv[0]), "+v"(mv[1]), "+v"(mv[2]), "+v"(mv[3]));
        if (tid < 640) {
            unsigned mx[4] = {0, 0, 0, 0};
            float sq[4] = {0.f, 0.f, 0.f, 0.f};
            prep_md(mv, (tid & 31) * LMAX + gp, w_down, mdp, 32 * LMAX, mx, sq);
#pragma unroll
            for (int d = 0; d < 4; d++) {
                const unsigned v = wave_scan_max(mx[d]);
                const float s = wave_scan_addf(sq[d]);
                if ((tid & 63) == 63) { *ldsp(MS_OFF + 128 + (tid >> 6) * 32 + d * 4) = v; *ldsp(MS_OFF + 128 + (tid >> 6) * 32 + 16 + d * 4) = __float_as_uint(s); }
            }
        }
        asm volatile("s_waitcnt vmcnt(3)" : "+v"(tg.io), "+v"(tg.ii), "+v"(tg.an), "+v"(fbits));
        asm volatile("" :: "v"(tc0));
        freq = lo_bf(fbits) * 0.15915494309189535f;
        if (T & 1) {
            const unsigned rel = (unsigned)T - 1 - (tbase + tid * 4);
            if (tv[0] && rel < 4) { const unsigned av = angle[T - 1]; if (rel == 0) tg.an.x = av; else tg.an.y = av; }
        }
        __syncthreads();
        if (tid >= P1_THREADS - 64) {
            const unsigned sa = MS_OFF + 128 + (tid & 15) * 32 + ((tid >> 4) & 3) * 4;
            unsigned mx = (tid & 15) < 10 ? *ldsp(sa) : 0u;
            float sq = (tid & 15) < 10 ? __uint_as_float(*ldsp(sa + 16)) : 0.f;
#pragma unroll
            for (int i = 0; i < 4; i++) {
                mx = max(mx, (unsigned)(i == 0 ? __builtin_amdgcn_update_dpp(0, (int)mx, 0x111, 0xF, 0xF, false) : i == 1 ? __builtin_amdgcn_update_dpp(0, (int)mx, 0x112, 0xF, 0xF, false)
                                        : i == 2 ? __builtin_amdgcn_update_dpp(0, (int)mx, 0x114, 0xF, 0xF, false) : __builtin_amdgcn_update_dpp(0, (int)mx, 0x118, 0xF, 0xF, false)));
                const int si = __float_as_int(sq);
                sq += __int_as_float(i == 0 ? __builtin_amdgcn_update_dpp(0, si, 0x111, 0xF, 0xF, false) : i == 1 ? __builtin_amdgcn_update_dpp(0, si, 0x112, 0xF, 0xF, false)
                                     : i == 2 ? __builtin_amdgcn_update_dpp(0, si, 0x114, 0xF, 0xF, false) : __builtin_amdgcn_update_dpp(0, si, 0x118, 0xF, 0xF, false));
            }
            mx = min(mx ? (mx >> 16) + 2 : 0u, 0x7F80u);
            const unsigned fx = (mx << 16) | (unsigned)__builtin_amdgcn_update_dpp(0, (int)mx, 0x142, 0xA, 0xF, false);
            const unsigned fy = (__float_as_uint(sq) & 0xFFFF0000u) | ((unsigned)__builtin_amdgcn_update_dpp(0, __float_as_int(sq), 0x142, 0xA, 0xF, false) >> 16);
            if ((tid & 31) == 31) __hip_atomic_store((unsigned long long*)(flags + (((tid >> 5) & 1) * P1_GRID + run) * 2), ((unsigned long long)fy << 32) | fx, __ATOMIC_RELAXED, __HIP_MEMORY_SCOPE_AGENT);
        }
        unsigned long long vm[4];
#pragma unroll
        for (int k = 0; k < 4; k++) vm[k] = __ballot(tv[k]);
        if (vm[0]) {
            unsigned v[4];
            Conv cv;
            convert1(tg, cv);
            tickets_masked(v, cv, tinc, vm);
            convert2(tg, cv);
            place(H, v, cv, vm, lb7);
            if ((H.m[0] & vm[0]) | (H.m[1] & vm[1]) | (H.m[2] & vm[2]) | (H.m[3] & vm[3])) own.ovf |= 16384u;
        }
#pragma unroll
        for (int k = 0; k < 4; k++) { H.m[k] = 0; H.a[k] = 0; H.t[k] = 0; H.A[k] = 0; H.cd[k] = 0; }
        if (NR < 2) asm volatile("s_waitcnt vmcnt(0)" ::: "memory");
    }
    const unsigned long long vm[4] = {~0ull, ~0ull, ~0ull, ~0ull};
    unsigned ca = NR > 1 ? CB(run + P1_GRID) * 2 : 16u;
    bool act = true;
#define HH 16
    const unsigned G = NR > NSTAT ? (NR - NSTAT) * (P1_GRID / 4) : 0, GH = G >= HH ? G - HH : G, GE = G >= HH ? G + HH : G;
    unsigned got = 0;
    unsigned dl = 0;
    int st = leader ? 1 : 0;
    auto answers = [&](bool need) {
        unsigned lc = __builtin_amdgcn_readfirstlane(*ldsp(MS_OFF + 108));
        if (!lc && need) {
            for (int i = 0; i < 4096 && !lc; i++) {
                unsigned x = 0;
                asm volatile("buffer_atomic_add %0, %1, %2, 0 offen sc0\n s_waitcnt vmcnt(0)" : "+v"(x) : "v"(loc_o), "s"(rw) : "memory");
                if ((__ballot(x != 0) & 510) == 510) lc = (unsigned)__ballot(x & 1) | 1;
            }
            if (!lc) { own.ovf |= 16384u; dl = 256; }
        }
        if (lc) {
            dl = (((lc >> 1) ^ lc ^ 1) & 255) | 256;
            own.fm = (dl & 255) << 13;
            own.e.y = ((PLANE + blk_line(run) + grp_line(tid >> 8)) * 128) ^ (own.fm & 8192u);
        }
    };
    unsigned cnt;
    if (NR == 0) { asm volatile("s_waitcnt lgkmcnt(0)" ::: "memory"); __syncthreads(); cnt = *ldsp(tid * 4); own.ovf |= cnt; }
    for (int rd = 0; NR > 0; rd += 2) {
        bool done = false;
#pragma unroll
        for (int r = 0; r < 2; r++) {
            unsigned v[4];
            Conv cv;
            const bool nact = wvi >= (ca & 31);
            const unsigned cl = nact ? ca & ~31u : 0x7F000000u;
            if (r || rd) load_grp(cur[r ^ 1], ro, ri, ra, cl, vo16, vo8);
            held_out(H);
            asm volatile("s_cmp_eq_u32 %3, 0\n s_cbranch_scc1 1f\n s_waitcnt vmcnt(5)\n 1:\n s_waitcnt vmcnt(8)" : "+v"(cur[r].io), "+v"(cur[r].ii), "+v"(cur[r].an) : "s"(wvi) : "scc");
            if (act) {
                convert1(cur[r], cv);
                tickets(v, cv, tinc);
                convert2(cur[r], cv);
                place(H, v, cv, vm, lb7);
            } else H.m[0] = H.m[1] = H.m[2] = H.m[3] = 0;
            act = nact;
            if (wave0) {
                asm volatile("s_waitcnt vmcnt(5)" : "+v"(got));
                const unsigned g = __builtin_amdgcn_readfirstlane(got);
                unsigned nx = rd + r + 2 < NR ? ca + P1_GRID * (CH * 2) : 16u;
                if (rd + r + 2 >= NSTAT) {
                    unsigned c = g, sub = 0;
                    if (g >= GH) { c = GH + ((g - GH) >> 1); sub = ((g - GH) & 1) * (HO * 2) - HO * 2 + 8; }
                    nx = st == 2 && g < GE ? CB(NSTAT * P1_GRID + c * 4 + shard) * 2 + sub : 16u;
                }
                if (st == 0 && g != 0) st = 1;
                if (st == 0 && rd + r + 3 == NSTAT) {
                    for (int i = 0; i < 128 && st == 0; i++) {
                        unsigned x = 0;
                        asm volatile("buffer_atomic_add %0, %1, %2, 0 offen sc0\n s_waitcnt vmcnt(0)" : "+v"(x) : "v"(tid == 0 ? box_o : OOB), "s"(rw) : "memory");
                        if (__builtin_amdgcn_readfirstlane(x) != 0) st = 1;
                    }
                }
                if (st == 1 && rd + r + 3 >= NSTAT) st = 2;
                if (ca == 16) nx = 16;
                if ((tid & 63) == 0) *ldsp(MS_OFF + 104) = nx;
                bool ask = !dl;
                if (ask && (__ballot(got != 0) & 510) == 510) { const unsigned lc = (unsigned)__ballot(got & 1) | 1; if (tid == 0) *ldsp(MS_OFF + 108) = lc; ask = false; }
                got = st == 2 && tid == 0 ? 1u : 0u;
                const bool tk = st != 1 && nx != 16;
                asm volatile("buffer_atomic_add %0, %1, %2, 0 offen sc0" : "+v"(got) : "v"(tid == 0 ? (tk ? (st == 2 ? ctr_o : box_o) : OOB) : ask ? loc_o : OOB), "s"(rw) : "memory");
                asm volatile("buffer_store_dword %0, %1, %2, 0 offen sc1" :: "v"(ans), "v"(prober && r == 0 && TELLC ? tell_o : OOB), "s"(rw) : "memory");
                asm volatile("buffer_store_dword %0, %1, %2, 0 offen sc1" :: "v"(1u), "v"(leader ? (128 + shard * 64 + tid) * 4 : OOB), "s"(rw) : "memory");
            }
            asm volatile("s_waitcnt lgkmcnt(0)" ::: "memory");
            __syncthreads();
            cnt = *ldsp(tid * 4);
            const unsigned nx = *ldsp(MS_OFF + 104);
            if (!dl) answers(__ballot(cnt >= 16384u) != 0);
            flush(own, cnt, tid, sinc, rr);
            if (ca == 16) { done = true; break; }
            ca = __builtin_amdgcn_readfirstlane(nx);
            __syncthreads();
        }
        if (done) break;
    }
    unsigned tc1 = touch_code(tid);
    {
        cnt &= 16383u;
        const unsigned nu = (cnt + 2 * TINC) >> 11;
        const unsigned inc = wave_scan_add(nu), off = inc - nu;
        const unsigned tot = ((unsigned)__builtin_amdgcn_readlane((int)inc, 63) + 7) & ~7u;
        unsigned fb = 0;
        if ((tid & 63) == 0) fb = atomicAdd(L + MS_OFF / 4, tot);
        const unsigned wl = WL_OFF + (tid >> 6) * 1024;
        if (nu) {
            const unsigned la = wl + off * 2, src = (tid & 63) * LS;
#pragma unroll
            for (int k = 7; k >= 0; k--) asm volatile("ds_write_b16 %0, %1 offset:%2" :: "v"(la), "v"(src + k * 16), "n"(k * 2) : "memory");
        }
        if ((cnt >> 11) < nu) {
            const unsigned c2 = cnt + TINC, ua = own.e.x + ((cnt >> 7) & 0x70);
            const bool two = (c2 >> 11) < nu;
            *ldsp(ua + 8) = pad_rec(run);
            if (two) *ldsp(ua + 4) = pad_rec(run + 256);
            asm volatile("ds_or_b32 %0, %1 offset:12" :: "v"(ua), "v"(two ? (512u << 11) | (512u << 22) : 512u << 22) : "memory");
        }
        fb = __builtin_amdgcn_readfirstlane(fb);
        const unsigned nl = (own.pc - PC_OFF) >> 4, tp = ((tid & 31) << 5) | ((tid ^ (tid >> 5)) & 31), tq = tid ^ (tid >> 5);
        const unsigned long long cc = (((unsigned long long)own.c1 << 32) | own.c0) << ((64 - 8 * min(nl, 8u)) & 63);
        *ldsp(TB_OFF + ((tid & 31) * 33 + (tid >> 5)) * 4) = min(nl, 31u) | (nu << 5) | ((dl & 255) << 9) | ((fb + off) << 18);
        *ldsp(LB + 128 + tp * LS) = (unsigned)(cc >> 32);
        if (own.ovf >= 16384u || nl > ly.KX || (seen & 0x30000000u) || fb + tot > ly.fcap) {
            __hip_atomic_store(flags + run * 2, 0x7F807F80u, __ATOMIC_RELAXED, __HIP_MEMORY_SCOPE_AGENT);
            __hip_atomic_store(flags + (P1_GRID + run) * 2, 0x7F807F80u, __ATOMIC_RELAXED, __HIP_MEMORY_SCOPE_AGENT);
        }
        typedef __attribute__((address_space(3))) unsigned short lds_u16;
        const unsigned lb = LB + (tid >> 6) * (64 * LS), ln = tid & 63;
        unsigned sa[5];
#pragma unroll
        for (int i = 0; i < 5; i++) sa[i] = lb + (*(lds_u16*)(size_t)(wl + i * 128 + ln * 2) & 0x3FFCu);
        asm volatile("s_waitcnt lgkmcnt(0)" ::: "memory");
        __syncthreads();
        held_out(H);
        *ldsp(tp * 4) = (unsigned)cc;
        typedef unsigned v3u __attribute__((ext_vector_type(3)));
        v3u tw;
        tw.x = *ldsp(TB_OFF + (tid + (tid >> 5)) * 4); tw.y = *ldsp(LB + 128 + tq * LS);
        asm volatile("s_waitcnt lgkmcnt(0)" ::: "memory");
        __syncthreads();
        const __amdgpu_buffer_rsrc_t rf = __builtin_amdgcn_make_buffer_rsrc((void*)(rec + (size_t)(frag_unit(run, ly) + fb) * 16), (short)0, fb + tot > ly.fcap ? 0 : tot * 16, 0x00020000);
        v4u fv[5];
#pragma unroll
        for (int i = 0; i < 5; i++) fv[i] = read_unit(sa[i]);
        tw.z = *ldsp(tq * 4);
        asm volatile("buffer_store_dwordx3 %0, %1, %2, 0 offen sc1" :: "v"(tw), "v"(tid * 12), "s"(mk_rsrc(tab + run * (NT * 3), NT * 12)) : "memory");
#pragma unroll
        for (int i = 0; i < 5; i++) __builtin_amdgcn_raw_buffer_store_b128(fv[i], rf, ln * 16 + i * 1024, 0, 16);
        for (unsigned p = 320; p < tot; p += 64) {
            const v4u v = read_unit(lb + (*(lds_u16*)(size_t)(wl + (p + ln) * 2) & 0x3FFCu));
            __builtin_amdgcn_raw_buffer_store_b128(v, rf, (p + ln) * 16, 0, 16);
        }
        asm volatile("s_waitcnt vmcnt(0)" :: "v"(tc1));
    }
    asm volatile("s_waitcnt vmcnt(0)" :: "v"(cur[0].io), "v"(cur[0].ii), "v"(cur[0].an), "v"(cur[1].io), "v"(cur[1].ii), "v"(cur[1].an), "v"(got));
}

struct AccArgs {
    const char* rec; const unsigned* tab; const unsigned* flags; const unsigned* mdp; float* part; unsigned* praw; unsigned* wgmode;
    const int* idx_in; const int* idx_out; const unsigned short* angle; const unsigned short* ang_freq; const unsigned short* m; const unsigned short* w_down;
    const unsigned short* dist; const unsigned short* centers; const unsigned short* w_rbf; const unsigned short* w_sbf; const unsigned short* w_up;
    unsigned short* out;
    int EP; unsigned O; int T; float c4; int E; int unused;
    Lay ly;
};
typedef char AccArgs_has_no_padding[sizeof(AccArgs) == 19 * 8 + 10 * 4 ? 1 : -1];

typedef const __attribute__((address_space(4))) unsigned* cptr;
static __device__ __forceinline__ void edge_gh(float d, const unsigned short* centers, const unsigned short* w_sbf, const unsigned short* w_rbf, float* g, float* h) {
#pragma unroll
    for (int k = 0; k < 4; k++) g[k] = h[k] = 0.f;
#pragma unroll
    for (int r = 0; r < 8; r++) {
        const unsigned cu = ((cptr)centers)[r >> 1];
        const float df = d - ((r & 1) ? hi_bf(cu) : lo_bf(cu));
        const float rb = __expf(-10.f * df * df);
#pragma unroll
        for (int k = 0; k < 2; k++) {
            const unsigned u = ((cptr)w_sbf)[r * 2 + k], v = ((cptr)w_rbf)[r * 2 + k];
            g[2 * k] += rb * lo_bf(u); g[2 * k + 1] += rb * hi_bf(u); h[2 * k] += rb * lo_bf(v); h[2 * k + 1] += rb * hi_bf(v);
        }
    }
}
static __device__ __forceinline__ v2u edge_out(const float* agg, const float* g, const float* h, unsigned m0, unsigned m1, const unsigned* wu) {
    float a[4];
#pragma unroll
    for (int k = 0; k < 4; k++) a[k] = agg[k] * g[k];
    const float mm[4] = {lo_bf(m0), hi_bf(m0), lo_bf(m1), hi_bf(m1)};
    float o[4];
#pragma unroll
    for (int dd = 0; dd < 4; dd++) {
        float s = 0.f;
#pragma unroll
        for (int k = 0; k < 4; k++) { const unsigned u = wu[k * 2 + (dd >> 1)]; s += a[k] * ((dd & 1) ? hi_bf(u) : lo_bf(u)); }
        o[dd] = mm[dd] + s * h[dd];
    }
    v2u r;
    asm("v_cvt_pk_bf16_f32 %0, %1, %2" : "=v"(r.x) : "v"(o[0]), "v"(o[1]));
    asm("v_cvt_pk_bf16_f32 %0, %1, %2" : "=v"(r.y) : "v"(o[2]), "v"(o[3]));
    return r;
}

typedef const __attribute__((address_space(4))) AccArgs* kaptr;
static __device__ __forceinline__ void acc_fallback(unsigned* lds, unsigned mode, const __attribute__((address_space(4))) AccArgs& a_, int tid) {
    const char* rec = rec_base(a_.rec); const unsigned* flags = a_.flags; const unsigned* mdp = a_.mdp;
    const int EP = a_.EP, T = a_.T; const Lay ly = {a_.ly.K0, a_.ly.KX, a_.ly.dumu, a_.ly.fcap};
    const int lane = tid & 63;
    const int xcd = blockIdx.x & 7, jj = blockIdx.x >> 3;
    const int qt = xcd >> 1, cp = jj & 1, b = (xcd & 1) * 16 + (jj >> 1);
    const unsigned O = a_.O;
    float* pout0 = a_.part + (size_t)(qt * 4 + cp * 2) * EP + (size_t)b * O;
    const unsigned G = tid >> 3, k = tid & 7;
    {
        unsigned* praw = a_.praw + (size_t)(b * 8 + qt * 2 + cp) * O;
        for (unsigned i = tid; i < O; i += P2_THREADS) praw[i] = 0x8000u;
    }
    if (mode == 1) {
        unsigned* acc = lds + OBASE;
        const float Xm = 2139095040.0f / (float)(NWS * (ly.KX * 24 + 24)) - 2.0f;
#pragma unroll 1
        for (int c = 0; c < 2; c++) {
            __syncthreads();
            if (tid == 0) lds[0] = 0;
            __syncthreads();
            if (tid < NWS) { unsigned v = wave_scan_max((flags[(cp * P1_GRID + tid) * 2] >> (16 * c)) & 0xFFFF); if (lane == 63) atomicMax(&lds[0], v); }
            __syncthreads();
            unsigned mbits = __builtin_amdgcn_readfirstlane(lds[0]);
            if (mbits == 0) mbits = 0x3f80;
            const float M = __uint_as_float(mbits << 16);
            const float S = Xm / (511.0f * M), inv = M / Xm;
            float racc[20];
#pragma unroll
            for (int r = 0; r < 20; r++) racc[r] = 0.f;
#pragma unroll 1
            for (int s = 0; s < 8; s++) {
                __syncthreads();
                const unsigned* src = mdp + (size_t)cp * (32 * LMAX) + (size_t)(qt * 8 + s) * LMAX;
                for (unsigned i = tid; i < LMAX; i += P2_THREADS) {
                    unsigned w = src[i];
                    lds[(i & ~255u) | ((i & 3) << 6) | ((i >> 2) & 63)] = __float_as_uint((c ? __uint_as_float(w) : lo_bf(w)) * S);
                }
                for (int i = tid; i < OBASE; i += P2_THREADS) acc[i] = 0;
                __syncthreads();
                const unsigned t = b * 32 + qt * 8 + s;
                for (unsigned r = G; r < NWS; r += 128) {
                    const unsigned* te = a_.tab + (r * NT + t) * 3;
                    const unsigned tw = te[0], nl = tw & 31, nu = (tw >> 5) & 15;
                    for (unsigned l = 0; l <= nl; l++) {
                        unsigned u;
                        if (l < nl) u = (((l + 1) * PLANE + blk_line(r) + grp_line(qt) + slot_line(l < 8 ? (te[1 + (l >> 2)] >> (24 - 8 * (l & 3))) & 255 : s * 32 + b)) ^ ((__popc((tw >> 9) & 511 & ((2u << l) - 1)) & 1) << 6)) * 8 + k;
                        else { if (k >= nu) break; u = frag_unit(r, ly) + (tw >> 18) + k; }
                        v4u q = *(const v4u*)(rec + (size_t)u * 16);
                        unsigned a[3] = {q.x, q.y, q.z};
#pragma unroll
                        for (int j = 0; j < 3; j++) {
                            const int cq = (int)((q.w >> (j == 0 ? 1 : j == 1 ? 11 : 22)) & 1023) - 512;
                            const int x = __float2int_rn(__uint_as_float(lds[(a[j] >> 2) & 0x7FFF]) * (float)cq);
                            if ((a[j] >> 17) - IDXB < OBASE) atomicAdd(&acc[(a[j] >> 17) - IDXB], (unsigned)x);
                        }
                    }
                }
                __syncthreads();
#pragma unroll
                for (int r = 0; r < 20; r++) racc[r] += (float)(int)acc[r * P2_THREADS + tid] * inv;
            }
#pragma unroll
            for (int r = 0; r < 20; r++) if ((unsigned)(r * P2_THREADS + tid) < O) pout0[(size_t)c * EP + r * P2_THREADS + tid] = racc[r];
        }
    }
    if (mode == 2) {
        float* facc = (float*)lds;
        const float freq = bf2f(a_.ang_freq[0]);
        float wd[2][4];
#pragma unroll
        for (int kk = 0; kk < 4; kk++) { wd[0][kk] = bf2f(a_.w_down[kk * 4 + cp * 2]); wd[1][kk] = bf2f(a_.w_down[kk * 4 + cp * 2 + 1]); }
        __syncthreads();
        for (int i = tid; i < 2 * OBASE; i += P2_THREADS) facc[i] = 0.f;
        __syncthreads();
        for (int t = tid; t < T; t += P2_THREADS) {
            const unsigned io = a_.idx_out[t], ii = a_.idx_in[t];
            if (((io >> 7) & 31) == (unsigned)b && ((ii >> 3) & 3) == (unsigned)qt) {
                const v2u mv = ((const v2u*)a_.m)[ii];
                const float a[4] = {lo_bf(mv.x), hi_bf(mv.x), lo_bf(mv.y), hi_bf(mv.y)};
                const float cs = __cosf(freq * bf2f(a_.angle[t]));
                const unsigned ol = (io & 127u) | ((io >> 5) & ~127u);
#pragma unroll
                for (int c = 0; c < 2; c++)
                    atomicAdd(&facc[c * OBASE + ol], (a[0] * wd[c][0] + a[1] * wd[c][1] + a[2] * wd[c][2] + a[3] * wd[c][3]) * cs);
            }
        }
        __syncthreads();
        for (unsigned idx = tid; idx < O; idx += P2_THREADS) { pout0[idx] = facc[idx]; pout0[(size_t)EP + idx] = facc[OBASE + idx]; }
    }
}

#define SWAPX 2
#define WXOR ((wave & 1) << 2)
#define RAUX ""
#define J0 (wave >> 1)
#define IDXMASK "0x8fffffff"
#define STR2(x) #x
#define STR(x) STR2(x)
#define WT_OFF 3149824
#define PLAN_IT 160

extern "C" __global__ __launch_bounds__(P2_THREADS) void k_acc(AccArgs a_) {
    const Lay ly = a_.ly;
    __shared__ __attribute__((aligned(16))) unsigned lds[2 * OBASE];
    const int tid = threadIdx.x;
    const int wave = __builtin_amdgcn_readfirstlane(tid >> 6), lane = tid & 63;
    const int xcd = blockIdx.x & 7, jj = blockIdx.x >> 3;
    const int qt = xcd >> 1, cp = jj & 1, b = (xcd & 1) * 16 + (jj >> 1);
    const unsigned O = a_.O;
    unsigned stv = 0;
    unsigned tc2 = touch_code(tid);
    unsigned* praw = a_.praw + (size_t)(b * 8 + qt * 2 + cp) * O;
    const unsigned* mds = a_.mdp + (size_t)cp * (32 * LMAX) + (size_t)(qt * 8) * LMAX;
    unsigned mode = (unsigned)(size_t)lds ? 2u : 0u;
    unsigned exs = 0, bad = 0;
    v4u mm[2], dd;
    const int q = qt * 2 + cp;
    unsigned t16 = tid * 16;
    if (mode == 0) {
        const unsigned w = wave * 16 + ((lane & 15) ^ (cp ? SWAPX : 0) ^ (WXOR)), t0 = b * 32 + qt * 8 + (lane >> 4) * 2;
        v4i_rs rs = mk_rsrc(rec_base(a_.rec), ly.dumu + 64);
        rs.y |= 16 << 16;
        const v4i_rs rm = mk_rsrc(mds, 8 * LMAX * 4);
        const v4i_rs rp0 = mk_rsrc(praw, O * 4);
        const v4i_rs rt = mk_rsrc(a_.tab, WT_OFF + 1024);
        const v4i_rs rmm = mk_rsrc(a_.m, a_.E * 8), rd = mk_rsrc(a_.dist, a_.E * 2);
        asm volatile(
            "s_setprio 3\n"
            "buffer_load_dwordx3 v[32:34], %[tof], %[rt], 0 offen\n"
            "buffer_load_dwordx3 v[36:38], %[tof], %[rt], 0 offen offset:12\n"
            "v_and_b32 v10, 1023, %[tid16]\n"
            "s_cmp_lg_u32 %[wb], 0x3c00\n s_cbranch_scc1 1f\n"
            "s_and_b32 s96, %[q], 1\n s_lshl_b32 s96, s96, 9\n s_add_u32 s96, s96, " STR(WT_OFF) "\n"
            "s_buffer_load_dword s97, %[rt], s96\n s_add_u32 s96, s96, 64\n"
            "s_buffer_load_dword s97, %[rt], s96\n s_add_u32 s96, s96, 64\n"
            "s_buffer_load_dword s97, %[rt], s96\n s_add_u32 s96, s96, 64\n"
            "s_buffer_load_dword s97, %[rt], s96\n s_add_u32 s96, s96, 64\n"
            "s_buffer_load_dword s97, %[rt], s96\n s_add_u32 s96, s96, 64\n"
            "s_buffer_load_dword s97, %[rt], s96\n s_add_u32 s96, s96, 64\n"
            "1:\n"
            "v_bfrev_b32 v12, 1\n"
            "v_add_u32 v11, %[pb], v10\n"
            "v_bfrev_b32 v13, 1\n"
            "v_bfrev_b32 v14, 1\n"
            "v_bfrev_b32 v15, 1\n"
            "ds_write_b128 v11, v[12:15] offset:0\n"
            "ds_write_b128 v11, v[12:15] offset:1024\n"
            "ds_write_b128 v11, v[12:15] offset:2048\n"
            "ds_write_b128 v11, v[12:15] offset:3072\n"
            "ds_write_b128 v11, v[12:15] offset:4096\n"
            "v_bfrev_b32 v80, 1\n"
            "v_mov_b32 v84, 0xb9000000\n"
            "v_mov_b32 v88, 0\n"
            "v_add_u32 v89, 81920, %[tid16]\n"
            "v_add_u32 v90, 65536, v89\n"
            "v_lshrrev_b32 v16, 4, v10\n"
            "v_add_u32 v83, %[dumu], v16\n"
            "v_and_b32 v17, 7, v16\n"
            "v_lshl_or_b32 v82, v17, 28, v17\n"
            "v_lshrrev_b32 v18, 3, v16\n"
            "v_lshl_add_u32 v17, v17, 3, v18\n"
            "v_lshlrev_b32 v17, 2, v17\n"
            "v_add_u32 v17, %[pb], v17\n"
            "s_getpc_b64 s[70:71]\n"
            "Lpc%=:\n"
            "s_add_u32 s72, s70, Lchg2%=-Lpc%=\n s_addc_u32 s73, s71, 0\n"
            "s_add_u32 s66, s70, Lchg3%=-Lpc%=\n s_addc_u32 s67, s71, 0\n"
            "s_add_u32 s70, s70, Lchg1%=-Lpc%=\n s_addc_u32 s71, s71, 0\n"
            "s_waitcnt vmcnt(0)\n"
            "buffer_load_dwordx4 v[92:95], v10, %[rt], %[fof] offen\n"
            "buffer_load_dwordx4 v[96:99], v10, %[rt], %[fof] offen offset:1024\n"
            "s_mov_b32 s94, 0\n"
            "buffer_load_dwordx4 v[40:43], %[tid16], %[rm], s94 offen\n"
            "s_add_u32 s96, s94, 16384\n"
            "buffer_load_dwordx4 v[44:47], %[tid16], %[rm], s96 offen\n"
            "s_add_u32 s96, s94, 32768\n"
            "buffer_load_dwordx4 v[48:51], %[tid16], %[rm], s96 offen\n"
            "s_add_u32 s96, s94, 49152\n"
            "buffer_load_dwordx4 v[52:55], %[tid16], %[rm], s96 offen\n"
            "s_add_u32 s96, s94, 65536\n"
            "buffer_load_dwordx4 v[56:59], %[tid16], %[rm], s96 offen\n"
            "v_and_b32 v20, 31, v32\n v_and_b32 v21, 31, v36\n v_bfe_u32 v22, v32, 5, 4\n v_bfe_u32 v23, v36, 5, 4\n"
            "v_min_u32 v24, 1, v22\n v_min_u32 v25, 1, v23\n v_add_u32 v24, v24, v20\n v_add_u32 v25, v25, v21\n"
            "v_lshl_or_b32 v24, v25, 16, v24\n"
            "v_and_b32 v19, 0x3fe00, v32\n v_lshrrev_b32 v8, 18, v32\n v_lshrrev_b32 v9, 18, v36\n"
            "v_add_u32_dpp v25, v24, v24 row_shr:1 row_mask:0xf bank_mask:0xf bound_ctrl:1\n"
            "v_add_u32 v8, %[fb], v8\n v_add_u32 v9, %[fb], v9\n"
            "v_add_u32_dpp v25, v25, v25 row_shr:2 row_mask:0xf bank_mask:0xf bound_ctrl:1\n"
            "v_sub_u32 v26, 8, v22\n v_sub_u32 v27, 8, v23\n"
            "v_add_u32_dpp v25, v25, v25 row_shr:4 row_mask:0xf bank_mask:0xf bound_ctrl:1\n"
            "v_lshl_or_b32 v8, v26, 28, v8\n v_lshl_or_b32 v9, v27, 28, v9\n"
            "v_add_u32_dpp v25, v25, v25 row_shr:8 row_mask:0xf bank_mask:0xf bound_ctrl:1\n"
            "s_nop 1\n"
            "v_readlane_b32 s84, v25, 15\n"
            "v_readlane_b32 s85, v25, 31\n"
            "v_readlane_b32 s86, v25, 47\n"
            "v_readlane_b32 s87, v25, 63\n"
            "v_sub_u32 v25, v25, v24\n"
            "s_mov_b32 s96, 0\n s_mov_b64 s[76:77], 0\n"
            "s_add_u32 s84, s84, 0x70007\n"
            "s_bfe_u32 s88, s84, 0xd0003\n s_lshr_b32 s89, s84, 19\n"
            "s_max_u32 s88, s88, 8\n s_max_u32 s89, s89, 8\n"
            "s_add_u32 s90, s96, s88\n"
            "s_pack_ll_b32_b16 s91, s96, s90\n"
            "s_add_u32 s96, s90, s89\n"
            "s_lshl_b32 s89, s89, 8\n s_or_b32 s88, s88, s89\n s_and_b32 s88, s88, 0xffff\n"
            "s_or_b32 s76, s76, s88\n"
            "s_lshl_b32 s91, s91, 3\n"
            "v_mov_b32 v26, s91\n"
            "s_add_u32 s85, s85, 0x70007\n"
            "s_bfe_u32 s88, s85, 0xd0003\n s_lshr_b32 s89, s85, 19\n"
            "s_max_u32 s88, s88, 8\n s_max_u32 s89, s89, 8\n"
            "s_add_u32 s90, s96, s88\n"
            "s_pack_ll_b32_b16 s91, s96, s90\n"
            "s_add_u32 s96, s90, s89\n"
            "s_lshl_b32 s89, s89, 8\n s_or_b32 s88, s88, s89\n s_and_b32 s88, s88, 0xffff\n"
            "s_lshl_b32 s88, s88, 16\n"
            "s_or_b32 s76, s76, s88\n"
            "s_lshl_b32 s91, s91, 3\n"
            "s_mov_b32 exec_lo, 0xffff0000\n s_mov_b32 exec_hi, 0xffffffff\n"
            "v_mov_b32 v26, s91\n"
            "s_add_u32 s86, s86, 0x70007\n"
            "s_bfe_u32 s88, s86, 0xd0003\n s_lshr_b32 s89, s86, 19\n"
            "s_max_u32 s88, s88, 8\n s_max_u32 s89, s89, 8\n"
            "s_add_u32 s90, s96, s88\n"
            "s_pack_ll_b32_b16 s91, s96, s90\n"
            "s_add_u32 s96, s90, s89\n"
            "s_lshl_b32 s89, s89, 8\n s_or_b32 s88, s88, s89\n s_and_b32 s88, s88, 0xffff\n"
            "s_or_b32 s77, s77, s88\n"
            "s_lshl_b32 s91, s91, 3\n"
            "s_mov_b32 exec_lo, 0x0\n s_mov_b32 exec_hi, 0xffffffff\n"
            "v_mov_b32 v26, s91\n"
            "s_add_u32 s87, s87, 0x70007\n"
            "s_bfe_u32 s88, s87, 0xd0003\n s_lshr_b32 s89, s87, 19\n"
            "s_max_u32 s88, s88, 8\n s_max_u32 s89, s89, 8\n"
            "s_add_u32 s90, s96, s88\n"
            "s_pack_ll_b32_b16 s91, s96, s90\n"
            "s_add_u32 s96, s90, s89\n"
            "s_lshl_b32 s89, s89, 8\n s_or_b32 s88, s88, s89\n s_and_b32 s88, s88, 0xffff\n"
            "s_lshl_b32 s88, s88, 16\n"
            "s_or_b32 s77, s77, s88\n"
            "s_lshl_b32 s91, s91, 3\n"
            "s_mov_b32 exec_lo, 0x0\n s_mov_b32 exec_hi, 0xffff0000\n"
            "v_mov_b32 v26, s91\n"
            "s_mov_b64 exec, -1\n"
            "s_mov_b32 %[bad], 0\n"
            "s_cmp_gt_u32 s96, " STR(PLAN_IT) "\n"
            "s_cbranch_scc0 1f\n"
            "s_mov_b32 %[bad], 1\n s_mov_b32 s76, 0x08080808\n s_mov_b32 s77, 0x08080808\n"
            "s_branch Lnoplan%=\n"
            "1:\n"
            "v_add_u32 v25, v25, v26\n"
            "v_and_b32 v26, 0xffff, v25\n v_lshrrev_b32 v27, 16, v25\n"
            "v_lshlrev_b32 v26, 2, v26\n v_lshlrev_b32 v27, 2, v27\n"
            "v_add_u32 v26, %[pb], v26\n v_add_u32 v27, %[pb], v27\n"
            "v_lshl_add_u32 v28, v20, 2, v26\n v_lshl_add_u32 v29, v21, 2, v27\n"
            "v_cmp_ne_u32 vcc, 0, v22\n v_cmp_ne_u32 s[88:89], 0, v23\n"
            "v_mov_b32 v18, %[xb]\n"
            "s_mov_b64 exec, vcc\n ds_write_b32 v28, v8\n s_mov_b64 exec, s[88:89]\n ds_write_b32 v29, v9\n s_mov_b64 exec, -1\n"
            "v_min_u32 v24, v20, v21\n"
            "v_mov_b32 v39, 0x802\n"
            "s_movk_i32 s90, 0x19f8\n"
            "v_cmp_gt_u32 vcc, 4, v24\n"
            "s_cmp_lg_u64 vcc, 0\n s_cbranch_scc0 4f\n"
            "v_cmp_lt_u32 vcc, 0, v20\n v_cmp_lt_u32 s[88:89], 0, v21\n"
            "v_and_b32 v35, 512, v19\n v_add_u32 v18, 2097152, v18\n v_lshrrev_b32 v19, 1, v19\n v_xor_b32 v18, v18, v35\n"
            "v_mul_u32_u24_sdwa v30, v33, v39 dst_sel:DWORD dst_unused:UNUSED_PAD src0_sel:BYTE_3 src1_sel:DWORD\n"
            "v_mul_u32_u24_sdwa v31, v37, v39 dst_sel:DWORD dst_unused:UNUSED_PAD src0_sel:BYTE_3 src1_sel:DWORD\n"
            "v_and_or_b32 v30, v30, s90, v18\n"
            "v_and_or_b32 v31, v31, s90, v18\n"
            "s_or_b64 s[84:85], vcc, s[88:89]\n"
            "s_cbranch_scc0 Lnoplan%=\n"
            "s_mov_b64 exec, vcc\n ds_write_b32 v26, v30 offset:0\n s_mov_b64 exec, s[88:89]\n ds_write_b32 v27, v31 offset:0\n s_mov_b64 exec, -1\n"
            "v_cmp_lt_u32 vcc, 1, v20\n v_cmp_lt_u32 s[88:89], 1, v21\n"
            "v_and_b32 v35, 512, v19\n v_add_u32 v18, 2097152, v18\n v_lshrrev_b32 v19, 1, v19\n v_xor_b32 v18, v18, v35\n"
            "v_mul_u32_u24_sdwa v30, v33, v39 dst_sel:DWORD dst_unused:UNUSED_PAD src0_sel:BYTE_2 src1_sel:DWORD\n"
            "v_mul_u32_u24_sdwa v31, v37, v39 dst_sel:DWORD dst_unused:UNUSED_PAD src0_sel:BYTE_2 src1_sel:DWORD\n"
            "v_and_or_b32 v30, v30, s90, v18\n"
            "v_and_or_b32 v31, v31, s90, v18\n"
            "s_or_b64 s[84:85], vcc, s[88:89]\n"
            "s_cbranch_scc0 Lnoplan%=\n"
            "s_mov_b64 exec, vcc\n ds_write_b32 v26, v30 offset:4\n s_mov_b64 exec, s[88:89]\n ds_write_b32 v27, v31 offset:4\n s_mov_b64 exec, -1\n"
            "v_cmp_lt_u32 vcc, 2, v20\n v_cmp_lt_u32 s[88:89], 2, v21\n"
            "v_and_b32 v35, 512, v19\n v_add_u32 v18, 2097152, v18\n v_lshrrev_b32 v19, 1, v19\n v_xor_b32 v18, v18, v35\n"
            "v_mul_u32_u24_sdwa v30, v33, v39 dst_sel:DWORD dst_unused:UNUSED_PAD src0_sel:BYTE_1 src1_sel:DWORD\n"
            "v_mul_u32_u24_sdwa v31, v37, v39 dst_sel:DWORD dst_unused:UNUSED_PAD src0_sel:BYTE_1 src1_sel:DWORD\n"
            "v_and_or_b32 v30, v30, s90, v18\n"
            "v_and_or_b32 v31, v31, s90, v18\n"
            "s_or_b64 s[84:85], vcc, s[88:89]\n"
            "s_cbranch_scc0 Lnoplan%=\n"
            "s_mov_b64 exec, vcc\n ds_write_b32 v26, v30 offset:8\n s_mov_b64 exec, s[88:89]\n ds_write_b32 v27, v31 offset:8\n s_mov_b64 exec, -1\n"
            "v_cmp_lt_u32 vcc, 3, v20\n v_cmp_lt_u32 s[88:89], 3, v21\n"
            "v_and_b32 v35, 512, v19\n v_add_u32 v18, 2097152, v18\n v_lshrrev_b32 v19, 1, v19\n v_xor_b32 v18, v18, v35\n"
            "v_mul_u32_u24_sdwa v30, v33, v39 dst_sel:DWORD dst_unused:UNUSED_PAD src0_sel:BYTE_0 src1_sel:DWORD\n"
            "v_mul_u32_u24_sdwa v31, v37, v39 dst_sel:DWORD dst_unused:UNUSED_PAD src0_sel:BYTE_0 src1_sel:DWORD\n"
            "v_and_or_b32 v30, v30, s90, v18\n"
            "v_and_or_b32 v31, v31, s90, v18\n"
            "s_or_b64 s[84:85], vcc, s[88:89]\n"
            "s_cbranch_scc0 Lnoplan%=\n"
            "s_mov_b64 exec, vcc\n ds_write_b32 v26, v30 offset:12\n s_mov_b64 exec, s[88:89]\n ds_write_b32 v27, v31 offset:12\n s_mov_b64 exec, -1\n"
            "s_branch 2f\n"
            "4:\n"
            "v_and_b32 v35, 512, v19\n v_add_u32 v18, 2097152, v18\n v_lshrrev_b32 v19, 1, v19\n v_xor_b32 v18, v18, v35\n"
            "v_mul_u32_u24_sdwa v30, v33, v39 dst_sel:DWORD dst_unused:UNUSED_PAD src0_sel:BYTE_3 src1_sel:DWORD\n"
            "v_mul_u32_u24_sdwa v31, v37, v39 dst_sel:DWORD dst_unused:UNUSED_PAD src0_sel:BYTE_3 src1_sel:DWORD\n"
            "v_and_or_b32 v30, v30, s90, v18\n"
            "v_and_or_b32 v31, v31, s90, v18\n"
            "v_and_b32 v35, 512, v19\n v_add_u32 v18, 2097152, v18\n v_lshrrev_b32 v19, 1, v19\n v_xor_b32 v18, v18, v35\n"
            "v_mul_u32_u24_sdwa v28, v33, v39 dst_sel:DWORD dst_unused:UNUSED_PAD src0_sel:BYTE_2 src1_sel:DWORD\n"
            "v_mul_u32_u24_sdwa v29, v37, v39 dst_sel:DWORD dst_unused:UNUSED_PAD src0_sel:BYTE_2 src1_sel:DWORD\n"
            "v_and_or_b32 v28, v28, s90, v18\n"
            "v_and_or_b32 v29, v29, s90, v18\n"
            "ds_write2_b32 v26, v30, v28 offset0:0 offset1:1\n ds_write2_b32 v27, v31, v29 offset0:0 offset1:1\n"
            "v_and_b32 v35, 512, v19\n v_add_u32 v18, 2097152, v18\n v_lshrrev_b32 v19, 1, v19\n v_xor_b32 v18, v18, v35\n"
            "v_mul_u32_u24_sdwa v30, v33, v39 dst_sel:DWORD dst_unused:UNUSED_PAD src0_sel:BYTE_1 src1_sel:DWORD\n"
            "v_mul_u32_u24_sdwa v31, v37, v39 dst_sel:DWORD dst_unused:UNUSED_PAD src0_sel:BYTE_1 src1_sel:DWORD\n"
            "v_and_or_b32 v30, v30, s90, v18\n"
            "v_and_or_b32 v31, v31, s90, v18\n"
            "v_and_b32 v35, 512, v19\n v_add_u32 v18, 2097152, v18\n v_lshrrev_b32 v19, 1, v19\n v_xor_b32 v18, v18, v35\n"
            "v_mul_u32_u24_sdwa v28, v33, v39 dst_sel:DWORD dst_unused:UNUSED_PAD src0_sel:BYTE_0 src1_sel:DWORD\n"
            "v_mul_u32_u24_sdwa v29, v37, v39 dst_sel:DWORD dst_unused:UNUSED_PAD src0_sel:BYTE_0 src1_sel:DWORD\n"
            "v_and_or_b32 v28, v28, s90, v18\n"
            "v_and_or_b32 v29, v29, s90, v18\n"
            "ds_write2_b32 v26, v30, v28 offset0:2 offset1:3\n ds_write2_b32 v27, v31, v29 offset0:2 offset1:3\n"
            "2:\n"
            "v_cmp_lt_u32 vcc, 4, v20\n v_cmp_lt_u32 s[88:89], 4, v21\n"
            "v_and_b32 v35, 512, v19\n v_add_u32 v18, 2097152, v18\n v_lshrrev_b32 v19, 1, v19\n v_xor_b32 v18, v18, v35\n"
            "v_mul_u32_u24_sdwa v30, v34, v39 dst_sel:DWORD dst_unused:UNUSED_PAD src0_sel:BYTE_3 src1_sel:DWORD\n"
            "v_mul_u32_u24_sdwa v31, v38, v39 dst_sel:DWORD dst_unused:UNUSED_PAD src0_sel:BYTE_3 src1_sel:DWORD\n"
            "v_and_or_b32 v30, v30, s90, v18\n"
            "v_and_or_b32 v31, v31, s90, v18\n"
            "s_or_b64 s[84:85], vcc, s[88:89]\n"
            "s_cbranch_scc0 Lnoplan%=\n"
            "s_mov_b64 exec, vcc\n ds_write_b32 v26, v30 offset:16\n s_mov_b64 exec, s[88:89]\n ds_write_b32 v27, v31 offset:16\n s_mov_b64 exec, -1\n"
            "v_cmp_lt_u32 vcc, 5, v20\n v_cmp_lt_u32 s[88:89], 5, v21\n"
            "v_and_b32 v35, 512, v19\n v_add_u32 v18, 2097152, v18\n v_lshrrev_b32 v19, 1, v19\n v_xor_b32 v18, v18, v35\n"
            "v_mul_u32_u24_sdwa v30, v34, v39 dst_sel:DWORD dst_unused:UNUSED_PAD src0_sel:BYTE_2 src1_sel:DWORD\n"
            "v_mul_u32_u24_sdwa v31, v38, v39 dst_sel:DWORD dst_unused:UNUSED_PAD src0_sel:BYTE_2 src1_sel:DWORD\n"
            "v_and_or_b32 v30, v30, s90, v18\n"
            "v_and_or_b32 v31, v31, s90, v18\n"
            "s_or_b64 s[84:85], vcc, s[88:89]\n"
            "s_cbranch_scc0 Lnoplan%=\n"
            "s_mov_b64 exec, vcc\n ds_write_b32 v26, v30 offset:20\n s_mov_b64 exec, s[88:89]\n ds_write_b32 v27, v31 offset:20\n s_mov_b64 exec, -1\n"
            "v_cmp_lt_u32 vcc, 6, v20\n v_cmp_lt_u32 s[88:89], 6, v21\n"
            "v_and_b32 v35, 512, v19\n v_add_u32 v18, 2097152, v18\n v_lshrrev_b32 v19, 1, v19\n v_xor_b32 v18, v18, v35\n"
            "v_mul_u32_u24_sdwa v30, v34, v39 dst_sel:DWORD dst_unused:UNUSED_PAD src0_sel:BYTE_1 src1_sel:DWORD\n"
            "v_mul_u32_u24_sdwa v31, v38, v39 dst_sel:DWORD dst_unused:UNUSED_PAD src0_sel:BYTE_1 src1_sel:DWORD\n"
            "v_and_or_b32 v30, v30, s90, v18\n"
            "v_and_or_b32 v31, v31, s90, v18\n"
            "s_or_b64 s[84:85], vcc, s[88:89]\n"
            "s_cbranch_scc0 Lnoplan%=\n"
            "s_mov_b64 exec, vcc\n ds_write_b32 v26, v30 offset:24\n s_mov_b64 exec, s[88:89]\n ds_write_b32 v27, v31 offset:24\n s_mov_b64 exec, -1\n"
            "v_cmp_lt_u32 vcc, 7, v20\n v_cmp_lt_u32 s[88:89], 7, v21\n"
            "v_and_b32 v35, 512, v19\n v_add_u32 v18, 2097152, v18\n v_lshrrev_b32 v19, 1, v19\n v_xor_b32 v18, v18, v35\n"
            "v_mul_u32_u24_sdwa v30, v34, v39 dst_sel:DWORD dst_unused:UNUSED_PAD src0_sel:BYTE_0 src1_sel:DWORD\n"
            "v_mul_u32_u24_sdwa v31, v38, v39 dst_sel:DWORD dst_unused:UNUSED_PAD src0_sel:BYTE_0 src1_sel:DWORD\n"
            "v_and_or_b32 v30, v30, s90, v18\n"
            "v_and_or_b32 v31, v31, s90, v18\n"
            "s_or_b64 s[84:85], vcc, s[88:89]\n"
            "s_cbranch_scc0 Lnoplan%=\n"
            "s_mov_b64 exec, vcc\n ds_write_b32 v26, v30 offset:28\n s_mov_b64 exec, s[88:89]\n ds_write_b32 v27, v31 offset:28\n s_mov_b64 exec, -1\n"
            "s_mov_b32 s90, 8\n"
            "v_or_b32 v18, v18, %[hm]\n"
            "3:\n"
            "v_cmp_lt_u32 vcc, s90, v20\n v_cmp_lt_u32 s[88:89], s90, v21\n"
            "v_add_u32 v18, 2097152, v18\n"
            "s_add_u32 s90, s90, 1\n"
            "v_xor_b32 v31, 64, v18\n"
            "s_or_b64 s[84:85], vcc, s[88:89]\n"
            "s_cbranch_scc0 Lnoplan%=\n"
            "s_mov_b64 exec, vcc\n ds_write_b32 v26, v18 offset:32\n s_mov_b64 exec, s[88:89]\n ds_write_b32 v27, v31 offset:32\n s_mov_b64 exec, -1\n"
            "v_add_u32 v26, 4, v26\n v_add_u32 v27, 4, v27\n"
            "s_branch 3b\n"
            "Lnoplan%=:\n"
            "ds_read_b32 v60, v17 offset:0\n"
            "ds_read_b32 v61, v17 offset:256\n"
            "ds_read_b32 v62, v17 offset:512\n"
            "ds_read_b32 v63, v17 offset:768\n"
            "ds_read_b32 v64, v17 offset:1024\n"
            "ds_read_b32 v65, v17 offset:1280\n"
            "ds_read_b32 v66, v17 offset:1536\n"
            "ds_read_b32 v67, v17 offset:1792\n"
            "ds_read_b32 v68, v17 offset:2048\n"
            "ds_read_b32 v69, v17 offset:2304\n"
            "ds_read_b32 v70, v17 offset:2560\n"
            "ds_read_b32 v71, v17 offset:2816\n"
            "ds_read_b32 v72, v17 offset:3072\n"
            "ds_read_b32 v73, v17 offset:3328\n"
            "ds_read_b32 v74, v17 offset:3584\n"
            "ds_read_b32 v75, v17 offset:3840\n"
            "ds_read_b32 v76, v17 offset:4096\n"
            "ds_read_b32 v77, v17 offset:4352\n"
            "ds_read_b32 v78, v17 offset:4608\n"
            "ds_read_b32 v79, v17 offset:4864\n"
            "v_mov_b32 v100, 0x8000\n"
            "v_mov_b32 v101, 0x8000\n"
            "v_mov_b32 v102, 0x8000\n"
            "v_mov_b32 v103, 0x8000\n"
            "v_mov_b32 v107, v11\n"
            "s_mov_b32 s82, 0\n"
            "s_waitcnt lgkmcnt(15)\n"
            "v_mov_b32 v81, v60\n"
            "s_nop 1\n"
            "v_add_u32_dpp v111, v81, v82 row_newbcast:0 row_mask:0xf bank_mask:0x3\n"
            "v_add_u32_dpp v111, v81, v82 row_newbcast:8 row_mask:0xf bank_mask:0xc\n"
            "v_and_b32 v111, " IDXMASK ", v111\n"
            "v_min_u32 v111, v111, v83\n"
            "buffer_load_dwordx4 v[8:11], v111, %[rs], 0 idxen " RAUX "\n"
            "v_add_u32_dpp v111, v81, v82 row_newbcast:1 row_mask:0xf bank_mask:0x3\n"
            "v_add_u32_dpp v111, v81, v82 row_newbcast:9 row_mask:0xf bank_mask:0xc\n"
            "v_and_b32 v111, " IDXMASK ", v111\n"
            "v_min_u32 v111, v111, v83\n"
            "buffer_load_dwordx4 v[12:15], v111, %[rs], 0 idxen " RAUX "\n"
            "v_add_u32_dpp v111, v81, v82 row_newbcast:2 row_mask:0xf bank_mask:0x3\n"
            "v_add_u32_dpp v111, v81, v82 row_newbcast:10 row_mask:0xf bank_mask:0xc\n"
            "v_and_b32 v111, " IDXMASK ", v111\n"
            "v_min_u32 v111, v111, v83\n"
            "buffer_load_dwordx4 v[16:19], v111, %[rs], 0 idxen " RAUX "\n"
            "v_add_u32_dpp v111, v81, v82 row_newbcast:3 row_mask:0xf bank_mask:0x3\n"
            "v_add_u32_dpp v111, v81, v82 row_newbcast:11 row_mask:0xf bank_mask:0xc\n"
            "v_and_b32 v111, " IDXMASK ", v111\n"
            "v_min_u32 v111, v111, v83\n"
            "buffer_load_dwordx4 v[20:23], v111, %[rs], 0 idxen " RAUX "\n"
            "v_add_u32_dpp v111, v81, v82 row_newbcast:4 row_mask:0xf bank_mask:0x3\n"
            "v_add_u32_dpp v111, v81, v82 row_newbcast:12 row_mask:0xf bank_mask:0xc\n"
            "v_and_b32 v111, " IDXMASK ", v111\n"
            "v_min_u32 v111, v111, v83\n"
            "buffer_load_dwordx4 v[24:27], v111, %[rs], 0 idxen " RAUX "\n"
            "v_add_u32_dpp v111, v81, v82 row_newbcast:5 row_mask:0xf bank_mask:0x3\n"
            "v_add_u32_dpp v111, v81, v82 row_newbcast:13 row_mask:0xf bank_mask:0xc\n"
            "v_and_b32 v111, " IDXMASK ", v111\n"
            "v_min_u32 v111, v111, v83\n"
            "buffer_load_dwordx4 v[28:31], v111, %[rs], 0 idxen " RAUX "\n"
            "v_add_u32_dpp v111, v81, v82 row_newbcast:6 row_mask:0xf bank_mask:0x3\n"
            "v_add_u32_dpp v111, v81, v82 row_newbcast:14 row_mask:0xf bank_mask:0xc\n"
            "v_and_b32 v111, " IDXMASK ", v111\n"
            "v_min_u32 v111, v111, v83\n"
            "buffer_load_dwordx4 v[32:35], v111, %[rs], 0 idxen " RAUX "\n"
            "v_add_u32_dpp v111, v81, v82 row_newbcast:7 row_mask:0xf bank_mask:0x3\n"
            "v_add_u32_dpp v111, v81, v82 row_newbcast:15 row_mask:0xf bank_mask:0xc\n"
            "v_and_b32 v111, " IDXMASK ", v111\n"
            "v_min_u32 v111, v111, v83\n"
            "buffer_load_dwordx4 v[36:39], v111, %[rs], 0 idxen " RAUX "\n"
            "s_min_u32 s82, s82, 19\n"
            "s_add_u32 s82, s82, 1\n"
            "s_set_gpr_idx_on s82, gpr_idx(SRC0)\n"
            "s_nop 0\n"
            "v_mov_b32 v81, v60\n"
            "s_set_gpr_idx_off\n"
            "s_setprio 0\n"
            "ds_write_b128 v107, v[100:103] offset:0\n"
            "ds_write_b128 v107, v[100:103] offset:1024\n"
            "ds_write_b128 v107, v[100:103] offset:2048\n"
            "ds_write_b128 v107, v[100:103] offset:3072\n"
            "ds_write_b128 v107, v[100:103] offset:4096\n"
            "s_waitcnt vmcnt(13)\n"
            "v_pk_max_u16 v92, v92, v94\n v_pk_max_u16 v96, v96, v98\n"
            "v_lshlrev_b32 v104, 16, v93\n v_lshlrev_b32 v105, 16, v95\n v_lshlrev_b32 v106, 16, v97\n v_lshlrev_b32 v108, 16, v99\n"
            "v_pk_max_u16 v92, v92, v96\n"
            "v_add_f32 v104, v104, v105\n v_add_f32 v106, v106, v108\n v_add_f32 v93, v93, v95\n v_add_f32 v97, v97, v99\n"
            "v_and_b32 v94, 0xffff, v92\n v_lshrrev_b32 v92, 16, v92\n"
            "v_add_f32 v104, v104, v106\n v_add_f32 v93, v93, v97\n"
            "s_nop 0\n"
            "v_max_u32_dpp v94, v94, v94 row_shr:1 row_mask:0xf bank_mask:0xf bound_ctrl:1\n"
            "v_max_u32_dpp v92, v92, v92 row_shr:1 row_mask:0xf bank_mask:0xf bound_ctrl:1\n"
            "v_add_f32_dpp v104, v104, v104 row_shr:1 row_mask:0xf bank_mask:0xf bound_ctrl:1\n"
            "v_add_f32_dpp v93, v93, v93 row_shr:1 row_mask:0xf bank_mask:0xf bound_ctrl:1\n"
            "v_max_u32_dpp v94, v94, v94 row_shr:2 row_mask:0xf bank_mask:0xf bound_ctrl:1\n"
            "v_max_u32_dpp v92, v92, v92 row_shr:2 row_mask:0xf bank_mask:0xf bound_ctrl:1\n"
            "v_add_f32_dpp v104, v104, v104 row_shr:2 row_mask:0xf bank_mask:0xf bound_ctrl:1\n"
            "v_add_f32_dpp v93, v93, v93 row_shr:2 row_mask:0xf bank_mask:0xf bound_ctrl:1\n"
            "v_max_u32_dpp v94, v94, v94 row_shr:4 row_mask:0xf bank_mask:0xf bound_ctrl:1\n"
            "v_max_u32_dpp v92, v92, v92 row_shr:4 row_mask:0xf bank_mask:0xf bound_ctrl:1\n"
            "v_add_f32_dpp v104, v104, v104 row_shr:4 row_mask:0xf bank_mask:0xf bound_ctrl:1\n"
            "v_add_f32_dpp v93, v93, v93 row_shr:4 row_mask:0xf bank_mask:0xf bound_ctrl:1\n"
            "v_max_u32_dpp v94, v94, v94 row_shr:8 row_mask:0xf bank_mask:0xf bound_ctrl:1\n"
            "v_max_u32_dpp v92, v92, v92 row_shr:8 row_mask:0xf bank_mask:0xf bound_ctrl:1\n"
            "v_add_f32_dpp v104, v104, v104 row_shr:8 row_mask:0xf bank_mask:0xf bound_ctrl:1\n"
            "v_add_f32_dpp v93, v93, v93 row_shr:8 row_mask:0xf bank_mask:0xf bound_ctrl:1\n"
            "v_max_u32_dpp v94, v94, v94 row_bcast:15 row_mask:0xa bank_mask:0xf\n"
            "v_max_u32_dpp v92, v92, v92 row_bcast:15 row_mask:0xa bank_mask:0xf\n"
            "v_add_f32_dpp v104, v104, v104 row_bcast:15 row_mask:0xa bank_mask:0xf\n"
            "v_add_f32_dpp v93, v93, v93 row_bcast:15 row_mask:0xa bank_mask:0xf\n"
            "v_max_u32_dpp v94, v94, v94 row_bcast:31 row_mask:0xc bank_mask:0xf\n"
            "v_max_u32_dpp v92, v92, v92 row_bcast:31 row_mask:0xc bank_mask:0xf\n"
            "v_add_f32_dpp v104, v104, v104 row_bcast:31 row_mask:0xc bank_mask:0xf\n"
            "v_add_f32_dpp v93, v93, v93 row_bcast:31 row_mask:0xc bank_mask:0xf\n"
            "s_nop 1\n"
            "v_readlane_b32 s84, v94, 63\n v_readlane_b32 s85, v92, 63\n v_readlane_b32 s86, v104, 63\n v_readlane_b32 s87, v93, 63\n"
            "s_mov_b32 %[mode], 0\n"
            "s_max_u32 s96, s84, s85\n s_cmp_ge_u32 s96, 0x7f00\n s_cselect_b32 %[mode], 2, 0\n"
            "s_cmp_eq_u32 s84, 0\n s_cselect_b32 s84, 0x3f80, s84\n s_cselect_b32 s86, 0x7f000000, s86\n"
            "s_cmp_eq_u32 s85, 0\n s_cselect_b32 s85, 0x3f80, s85\n s_cselect_b32 s87, 0x7f000000, s87\n"
            "s_min_u32 s96, s84, s85\n s_cmp_lt_u32 s96, 0x800\n s_cselect_b32 %[mode], 2, %[mode]\n"
            "s_cmp_lg_u32 %[mode], 0\n"
            "s_cbranch_scc1 Lend%=\n"
            "s_lshl_b32 s84, s84, 16\n s_lshl_b32 s85, s85, 16\n"
            "v_mov_b32 v104, s84\n v_mov_b32 v105, s85\n"
            "v_mul_f32 v106, 0x3e82d0e5, v104\n v_mul_f32 v108, 0x3e82d0e5, v105\n"
            "v_mul_f32 v104, v104, v104\n v_mul_f32 v105, v105, v105\n"
            "v_add_u32 v106, 0x7fffff, v106\n v_add_u32 v108, 0x7fffff, v108\n"
            "v_mul_f32 v104, %[c4], v104\n v_mul_f32 v105, %[c4], v105\n"
            "v_and_b32 v106, 0x7f800000, v106\n v_and_b32 v108, 0x7f800000, v108\n"
            "v_cmp_lt_f32 vcc, s86, v104\n v_cmp_lt_f32 s[88:89], s87, v105\n"
            "v_add_u32 v86, 0xc00000, v106\n v_add_u32 v87, 0xc00000, v108\n"
            "v_lshrrev_b32 v106, 23, v106\n v_lshrrev_b32 v108, 15, v108\n"
            "v_or_b32 %[exs], v106, v108\n"
            "s_or_b64 vcc, vcc, s[88:89]\n"
            "s_cselect_b32 %[mode], 1, 0\n"
            "s_cbranch_scc1 Lend%=\n"
            "s_waitcnt vmcnt(8)\n"
            "s_mov_b32 m0, %[wb]\n"
            "s_nop 0\n"
            "ds_write_addtid_b32 v40 offset:0\n"
            "ds_write_addtid_b32 v41 offset:256\n"
            "ds_write_addtid_b32 v42 offset:512\n"
            "ds_write_addtid_b32 v43 offset:768\n"
            "ds_write_addtid_b32 v44 offset:16384\n"
            "ds_write_addtid_b32 v45 offset:16640\n"
            "ds_write_addtid_b32 v46 offset:16896\n"
            "ds_write_addtid_b32 v47 offset:17152\n"
            "ds_write_addtid_b32 v48 offset:32768\n"
            "ds_write_addtid_b32 v49 offset:33024\n"
            "ds_write_addtid_b32 v50 offset:33280\n"
            "ds_write_addtid_b32 v51 offset:33536\n"
            "ds_write_addtid_b32 v52 offset:49152\n"
            "ds_write_addtid_b32 v53 offset:49408\n"
            "ds_write_addtid_b32 v54 offset:49664\n"
            "ds_write_addtid_b32 v55 offset:49920\n"
            "s_add_u32 m0, %[wb], 16384\n"
            "s_nop 0\n"
            "ds_write_addtid_b32 v56 offset:49152\n"
            "ds_write_addtid_b32 v57 offset:49408\n"
            "ds_write_addtid_b32 v58 offset:49664\n"
            "ds_write_addtid_b32 v59 offset:49920\n"
            "s_mov_b32 s92, 0\n"
            "s_and_b32 s75, s76, 255\n"
            "s_movk_i32 s74, 1000\n"
            "s_sub_i32 s79, s75, %[j0]\n"
            "s_max_i32 s79, s79, 8\n"
            "s_cmp_eq_u32 s92, 7\n s_cselect_b32 s79, 8, s79\n"
            "s_setprio 3\n"
            "s_waitcnt lgkmcnt(0)\n"
            "s_barrier\n"
            "s_waitcnt vmcnt(7)\n"
            "v_and_b32 v104, 0x1fffc, v8\n"
            "v_and_b32 v105, 0x1fffc, v9\n"
            "v_and_b32 v106, 0x1fffc, v10\n"
            "ds_read_b32 v93, v104\n"
            "ds_read_b32 v95, v105\n"
            "ds_read_b32 v97, v106\n"
            "s_waitcnt lgkmcnt(0)\n"
            "Ls0_%=:\n"
            "s_cmp_gt_i32 s75, s74\n"
            "s_cbranch_scc1 1f\n"
            "s_waitcnt vmcnt(6)\n"
            "1:\n"
            "s_waitcnt vmcnt(11)\n"
            "s_cmp_eq_u32 s75, s79\n"
            "s_cbranch_scc1 Le0_%=\n"
            "Lc0_%=:\n"
            "v_and_b32 v104, 0x1fffc, v12\n"
            "v_and_b32 v105, 0x1fffc, v13\n"
            "v_and_b32 v106, 0x1fffc, v14\n"
            "ds_read_b32 v99, v104\n"
            "ds_read_b32 v101, v105\n"
            "ds_read_b32 v103, v106\n"
            "v_add_u32_dpp v111, v81, v82 row_newbcast:0 row_mask:0xf bank_mask:0x3\n"
            "v_add_u32_dpp v111, v81, v82 row_newbcast:8 row_mask:0xf bank_mask:0xc\n"
            "v_and_b32 v108, 0x7fe, v11\n"
            "v_and_b32 v109, 0x1ff800, v11\n"
            "v_lshrrev_b32 v110, 22, v11\n"
            "v_lshrrev_b32 v112, 15, v8\n"
            "v_lshrrev_b32 v113, 15, v9\n"
            "v_lshrrev_b32 v114, 15, v10\n"
            "v_and_b32 v111, " IDXMASK ", v111\n"
            "v_min_u32 v111, v111, v83\n"
            "buffer_load_dwordx4 v[8:11], v111, %[rs], 0 idxen " RAUX "\n"
            "v_fmamk_f32 v108, v108, 0x7e800000, v84\n"
            "v_fmamk_f32 v109, v109, 0x79800000, v84\n"
            "v_fmamk_f32 v110, v110, 0x7f000000, v84\n"
            "v_or_b32 v112, 3, v112\n"
            "v_or_b32 v113, 3, v113\n"
            "v_or_b32 v114, 3, v114\n"
            "s_sub_u32 s75, s75, 1\n"
            "s_waitcnt lgkmcnt(6)\n"
            "v_lshlrev_b32 v92, 16, v93\n"
            "v_lshlrev_b32 v94, 16, v95\n"
            "v_lshlrev_b32 v96, 16, v97\n"
            "v_pk_fma_f32 v[92:93], v[92:93], v[108:109], v[86:87] op_sel:[0,0,0] op_sel_hi:[1,0,1]\n"
            "v_pk_fma_f32 v[94:95], v[94:95], v[108:109], v[86:87] op_sel:[0,1,0] op_sel_hi:[1,1,1]\n"
            "v_pk_fma_f32 v[96:97], v[96:97], v[110:111], v[86:87] op_sel:[0,0,0] op_sel_hi:[1,0,1]\n"
            "v_lshl_add_u32 v92, v93, 16, v92\n"
            "v_lshl_add_u32 v94, v95, 16, v94\n"
            "v_lshl_add_u32 v96, v97, 16, v96\n"
            "v_sub_u32 v92, v92, v86\n"
            "v_mad_i32_i16 v88, v93, v112, v88\n"
            "ds_add_u32 v112, v92 offset:65533\n"
            "v_sub_u32 v94, v94, v86\n"
            "v_mad_i32_i16 v88, v95, v113, v88\n"
            "ds_add_u32 v113, v94 offset:65533\n"
            "v_sub_u32 v96, v96, v86\n"
            "v_mad_i32_i16 v88, v97, v114, v88\n"
            "ds_add_u32 v114, v96 offset:65533\n"
            "Ls1_%=:\n"
            "s_cmp_gt_i32 s75, s74\n"
            "s_cbranch_scc1 1f\n"
            "s_waitcnt vmcnt(6)\n"
            "1:\n"
            "s_waitcnt vmcnt(11)\n"
            "s_cmp_eq_u32 s75, s79\n"
            "s_cbranch_scc1 Le1_%=\n"
            "Lc1_%=:\n"
            "v_and_b32 v104, 0x1fffc, v16\n"
            "v_and_b32 v105, 0x1fffc, v17\n"
            "v_and_b32 v106, 0x1fffc, v18\n"
            "ds_read_b32 v93, v104\n"
            "ds_read_b32 v95, v105\n"
            "ds_read_b32 v97, v106\n"
            "v_add_u32_dpp v111, v81, v82 row_newbcast:1 row_mask:0xf bank_mask:0x3\n"
            "v_add_u32_dpp v111, v81, v82 row_newbcast:9 row_mask:0xf bank_mask:0xc\n"
            "v_and_b32 v108, 0x7fe, v15\n"
            "v_and_b32 v109, 0x1ff800, v15\n"
            "v_lshrrev_b32 v110, 22, v15\n"
            "v_lshrrev_b32 v112, 15, v12\n"
            "v_lshrrev_b32 v113, 15, v13\n"
            "v_lshrrev_b32 v114, 15, v14\n"
            "v_and_b32 v111, " IDXMASK ", v111\n"
            "v_min_u32 v111, v111, v83\n"
            "buffer_load_dwordx4 v[12:15], v111, %[rs], 0 idxen " RAUX "\n"
            "v_fmamk_f32 v108, v108, 0x7e800000, v84\n"
            "v_fmamk_f32 v109, v109, 0x79800000, v84\n"
            "v_fmamk_f32 v110, v110, 0x7f000000, v84\n"
            "v_or_b32 v112, 3, v112\n"
            "v_or_b32 v113, 3, v113\n"
            "v_or_b32 v114, 3, v114\n"
            "s_sub_u32 s75, s75, 1\n"
            "s_waitcnt lgkmcnt(6)\n"
            "v_lshlrev_b32 v98, 16, v99\n"
            "v_lshlrev_b32 v100, 16, v101\n"
            "v_lshlrev_b32 v102, 16, v103\n"
            "v_pk_fma_f32 v[98:99], v[98:99], v[108:109], v[86:87] op_sel:[0,0,0] op_sel_hi:[1,0,1]\n"
            "v_pk_fma_f32 v[100:101], v[100:101], v[108:109], v[86:87] op_sel:[0,1,0] op_sel_hi:[1,1,1]\n"
            "v_pk_fma_f32 v[102:103], v[102:103], v[110:111], v[86:87] op_sel:[0,0,0] op_sel_hi:[1,0,1]\n"
            "v_lshl_add_u32 v98, v99, 16, v98\n"
            "v_lshl_add_u32 v100, v101, 16, v100\n"
            "v_lshl_add_u32 v102, v103, 16, v102\n"
            "v_sub_u32 v98, v98, v86\n"
            "v_mad_i32_i16 v88, v99, v112, v88\n"
            "ds_add_u32 v112, v98 offset:65533\n"
            "v_sub_u32 v100, v100, v86\n"
            "v_mad_i32_i16 v88, v101, v113, v88\n"
            "ds_add_u32 v113, v100 offset:65533\n"
            "v_sub_u32 v102, v102, v86\n"
            "v_mad_i32_i16 v88, v103, v114, v88\n"
            "ds_add_u32 v114, v102 offset:65533\n"
            "Ls2_%=:\n"
            "s_cmp_gt_i32 s75, s74\n"
            "s_cbranch_scc1 1f\n"
            "s_waitcnt vmcnt(6)\n"
            "1:\n"
            "s_waitcnt vmcnt(11)\n"
            "s_cmp_eq_u32 s75, s79\n"
            "s_cbranch_scc1 Le2_%=\n"
            "Lc2_%=:\n"
            "v_and_b32 v104, 0x1fffc, v20\n"
            "v_and_b32 v105, 0x1fffc, v21\n"
            "v_and_b32 v106, 0x1fffc, v22\n"
            "ds_read_b32 v99, v104\n"
            "ds_read_b32 v101, v105\n"
            "ds_read_b32 v103, v106\n"
            "v_add_u32_dpp v111, v81, v82 row_newbcast:2 row_mask:0xf bank_mask:0x3\n"
            "v_add_u32_dpp v111, v81, v82 row_newbcast:10 row_mask:0xf bank_mask:0xc\n"
            "v_and_b32 v108, 0x7fe, v19\n"
            "v_and_b32 v109, 0x1ff800, v19\n"
            "v_lshrrev_b32 v110, 22, v19\n"
            "v_lshrrev_b32 v112, 15, v16\n"
            "v_lshrrev_b32 v113, 15, v17\n"
            "v_lshrrev_b32 v114, 15, v18\n"
            "v_and_b32 v111, " IDXMASK ", v111\n"
            "v_min_u32 v111, v111, v83\n"
            "buffer_load_dwordx4 v[16:19], v111, %[rs], 0 idxen " RAUX "\n"
            "v_fmamk_f32 v108, v108, 0x7e800000, v84\n"
            "v_fmamk_f32 v109, v109, 0x79800000, v84\n"
            "v_fmamk_f32 v110, v110, 0x7f000000, v84\n"
            "v_or_b32 v112, 3, v112\n"
            "v_or_b32 v113, 3, v113\n"
            "v_or_b32 v114, 3, v114\n"
            "s_sub_u32 s75, s75, 1\n"
            "s_waitcnt lgkmcnt(6)\n"
            "v_lshlrev_b32 v92, 16, v93\n"
            "v_lshlrev_b32 v94, 16, v95\n"
            "v_lshlrev_b32 v96, 16, v97\n"
            "v_pk_fma_f32 v[92:93], v[92:93], v[108:109], v[86:87] op_sel:[0,0,0] op_sel_hi:[1,0,1]\n"
            "v_pk_fma_f32 v[94:95], v[94:95], v[108:109], v[86:87] op_sel:[0,1,0] op_sel_hi:[1,1,1]\n"
            "v_pk_fma_f32 v[96:97], v[96:97], v[110:111], v[86:87] op_sel:[0,0,0] op_sel_hi:[1,0,1]\n"
            "v_lshl_add_u32 v92, v93, 16, v92\n"
            "v_lshl_add_u32 v94, v95, 16, v94\n"
            "v_lshl_add_u32 v96, v97, 16, v96\n"
            "v_sub_u32 v92, v92, v86\n"
            "v_mad_i32_i16 v88, v93, v112, v88\n"
            "ds_add_u32 v112, v92 offset:65533\n"
            "v_sub_u32 v94, v94, v86\n"
            "v_mad_i32_i16 v88, v95, v113, v88\n"
            "ds_add_u32 v113, v94 offset:65533\n"
            "v_sub_u32 v96, v96, v86\n"
            "v_mad_i32_i16 v88, v97, v114, v88\n"
            "ds_add_u32 v114, v96 offset:65533\n"
            "Ls3_%=:\n"
            "s_cmp_gt_i32 s75, s74\n"
            "s_cbranch_scc1 1f\n"
            "s_waitcnt vmcnt(6)\n"
            "1:\n"
            "s_waitcnt vmcnt(11)\n"
            "s_cmp_eq_u32 s75, s79\n"
            "s_cbranch_scc1 Le3_%=\n"
            "Lc3_%=:\n"
            "v_and_b32 v104, 0x1fffc, v24\n"
            "v_and_b32 v105, 0x1fffc, v25\n"
            "v_and_b32 v106, 0x1fffc, v26\n"
            "ds_read_b32 v93, v104\n"
            "ds_read_b32 v95, v105\n"
            "ds_read_b32 v97, v106\n"
            "v_add_u32_dpp v111, v81, v82 row_newbcast:3 row_mask:0xf bank_mask:0x3\n"
            "v_add_u32_dpp v111, v81, v82 row_newbcast:11 row_mask:0xf bank_mask:0xc\n"
            "v_and_b32 v108, 0x7fe, v23\n"
            "v_and_b32 v109, 0x1ff800, v23\n"
            "v_lshrrev_b32 v110, 22, v23\n"
            "v_lshrrev_b32 v112, 15, v20\n"
            "v_lshrrev_b32 v113, 15, v21\n"
            "v_lshrrev_b32 v114, 15, v22\n"
            "v_and_b32 v111, " IDXMASK ", v111\n"
            "v_min_u32 v111, v111, v83\n"
            "buffer_load_dwordx4 v[20:23], v111, %[rs], 0 idxen " RAUX "\n"
            "v_fmamk_f32 v108, v108, 0x7e800000, v84\n"
            "v_fmamk_f32 v109, v109, 0x79800000, v84\n"
            "v_fmamk_f32 v110, v110, 0x7f000000, v84\n"
            "v_or_b32 v112, 3, v112\n"
            "v_or_b32 v113, 3, v113\n"
            "v_or_b32 v114, 3, v114\n"
            "s_sub_u32 s75, s75, 1\n"
            "s_waitcnt lgkmcnt(6)\n"
            "v_lshlrev_b32 v98, 16, v99\n"
            "v_lshlrev_b32 v100, 16, v101\n"
            "v_lshlrev_b32 v102, 16, v103\n"
            "v_pk_fma_f32 v[98:99], v[98:99], v[108:109], v[86:87] op_sel:[0,0,0] op_sel_hi:[1,0,1]\n"
            "v_pk_fma_f32 v[100:101], v[100:101], v[108:109], v[86:87] op_sel:[0,1,0] op_sel_hi:[1,1,1]\n"
            "v_pk_fma_f32 v[102:103], v[102:103], v[110:111], v[86:87] op_sel:[0,0,0] op_sel_hi:[1,0,1]\n"
            "v_lshl_add_u32 v98, v99, 16, v98\n"
            "v_lshl_add_u32 v100, v101, 16, v100\n"
            "v_lshl_add_u32 v102, v103, 16, v102\n"
            "v_sub_u32 v98, v98, v86\n"
            "v_mad_i32_i16 v88, v99, v112, v88\n"
            "ds_add_u32 v112, v98 offset:65533\n"
            "v_sub_u32 v100, v100, v86\n"
            "v_mad_i32_i16 v88, v101, v113, v88\n"
            "ds_add_u32 v113, v100 offset:65533\n"
            "v_sub_u32 v102, v102, v86\n"
            "v_mad_i32_i16 v88, v103, v114, v88\n"
            "ds_add_u32 v114, v102 offset:65533\n"
            "Ls4_%=:\n"
            "s_cmp_gt_i32 s75, s74\n"
            "s_cbranch_scc1 1f\n"
            "s_waitcnt vmcnt(6)\n"
            "1:\n"
            "s_waitcnt vmcnt(11)\n"
            "s_cmp_eq_u32 s75, s79\n"
            "s_cbranch_scc1 Le4_%=\n"
            "Lc4_%=:\n"
            "v_and_b32 v104, 0x1fffc, v28\n"
            "v_and_b32 v105, 0x1fffc, v29\n"
            "v_and_b32 v106, 0x1fffc, v30\n"
            "ds_read_b32 v99, v104\n"
            "ds_read_b32 v101, v105\n"
            "ds_read_b32 v103, v106\n"
            "v_add_u32_dpp v111, v81, v82 row_newbcast:4 row_mask:0xf bank_mask:0x3\n"
            "v_add_u32_dpp v111, v81, v82 row_newbcast:12 row_mask:0xf bank_mask:0xc\n"
            "v_and_b32 v108, 0x7fe, v27\n"
            "v_and_b32 v109, 0x1ff800, v27\n"
            "v_lshrrev_b32 v110, 22, v27\n"
            "v_lshrrev_b32 v112, 15, v24\n"
            "v_lshrrev_b32 v113, 15, v25\n"
            "v_lshrrev_b32 v114, 15, v26\n"
            "v_and_b32 v111, " IDXMASK ", v111\n"
            "v_min_u32 v111, v111, v83\n"
            "buffer_load_dwordx4 v[24:27], v111, %[rs], 0 idxen " RAUX "\n"
            "v_fmamk_f32 v108, v108, 0x7e800000, v84\n"
            "v_fmamk_f32 v109, v109, 0x79800000, v84\n"
            "v_fmamk_f32 v110, v110, 0x7f000000, v84\n"
            "v_or_b32 v112, 3, v112\n"
            "v_or_b32 v113, 3, v113\n"
            "v_or_b32 v114, 3, v114\n"
            "s_sub_u32 s75, s75, 1\n"
            "s_waitcnt lgkmcnt(6)\n"
            "v_lshlrev_b32 v92, 16, v93\n"
            "v_lshlrev_b32 v94, 16, v95\n"
            "v_lshlrev_b32 v96, 16, v97\n"
            "v_pk_fma_f32 v[92:93], v[92:93], v[108:109], v[86:87] op_sel:[0,0,0] op_sel_hi:[1,0,1]\n"
            "v_pk_fma_f32 v[94:95], v[94:95], v[108:109], v[86:87] op_sel:[0,1,0] op_sel_hi:[1,1,1]\n"
            "v_pk_fma_f32 v[96:97], v[96:97], v[110:111], v[86:87] op_sel:[0,0,0] op_sel_hi:[1,0,1]\n"
            "v_lshl_add_u32 v92, v93, 16, v92\n"
            "v_lshl_add_u32 v94, v95, 16, v94\n"
            "v_lshl_add_u32 v96, v97, 16, v96\n"
            "v_sub_u32 v92, v92, v86\n"
            "v_mad_i32_i16 v88, v93, v112, v88\n"
            "ds_add_u32 v112, v92 offset:65533\n"
            "v_sub_u32 v94, v94, v86\n"
            "v_mad_i32_i16 v88, v95, v113, v88\n"
            "ds_add_u32 v113, v94 offset:65533\n"
            "v_sub_u32 v96, v96, v86\n"
            "v_mad_i32_i16 v88, v97, v114, v88\n"
            "ds_add_u32 v114, v96 offset:65533\n"
            "Ls5_%=:\n"
            "s_cmp_gt_i32 s75, s74\n"
            "s_cbranch_scc1 1f\n"
            "s_waitcnt vmcnt(6)\n"
            "1:\n"
            "s_waitcnt vmcnt(11)\n"
            "s_cmp_eq_u32 s75, s79\n"
            "s_cbranch_scc1 Le5_%=\n"
            "Lc5_%=:\n"
            "v_and_b32 v104, 0x1fffc, v32\n"
            "v_and_b32 v105, 0x1fffc, v33\n"
            "v_and_b32 v106, 0x1fffc, v34\n"
            "ds_read_b32 v93, v104\n"
            "ds_read_b32 v95, v105\n"
            "ds_read_b32 v97, v106\n"
            "v_add_u32_dpp v111, v81, v82 row_newbcast:5 row_mask:0xf bank_mask:0x3\n"
            "v_add_u32_dpp v111, v81, v82 row_newbcast:13 row_mask:0xf bank_mask:0xc\n"
            "v_and_b32 v108, 0x7fe, v31\n"
            "v_and_b32 v109, 0x1ff800, v31\n"
            "v_lshrrev_b32 v110, 22, v31\n"
            "v_lshrrev_b32 v112, 15, v28\n"
            "v_lshrrev_b32 v113, 15, v29\n"
            "v_lshrrev_b32 v114, 15, v30\n"
            "v_and_b32 v111, " IDXMASK ", v111\n"
            "v_min_u32 v111, v111, v83\n"
            "buffer_load_dwordx4 v[28:31], v111, %[rs], 0 idxen " RAUX "\n"
            "v_fmamk_f32 v108, v108, 0x7e800000, v84\n"
            "v_fmamk_f32 v109, v109, 0x79800000, v84\n"
            "v_fmamk_f32 v110, v110, 0x7f000000, v84\n"
            "v_or_b32 v112, 3, v112\n"
            "v_or_b32 v113, 3, v113\n"
            "v_or_b32 v114, 3, v114\n"
            "s_sub_u32 s75, s75, 1\n"
            "s_waitcnt lgkmcnt(6)\n"
            "v_lshlrev_b32 v98, 16, v99\n"
            "v_lshlrev_b32 v100, 16, v101\n"
            "v_lshlrev_b32 v102, 16, v103\n"
            "v_pk_fma_f32 v[98:99], v[98:99], v[108:109], v[86:87] op_sel:[0,0,0] op_sel_hi:[1,0,1]\n"
            "v_pk_fma_f32 v[100:101], v[100:101], v[108:109], v[86:87] op_sel:[0,1,0] op_sel_hi:[1,1,1]\n"
            "v_pk_fma_f32 v[102:103], v[102:103], v[110:111], v[86:87] op_sel:[0,0,0] op_sel_hi:[1,0,1]\n"
            "v_lshl_add_u32 v98, v99, 16, v98\n"
            "v_lshl_add_u32 v100, v101, 16, v100\n"
            "v_lshl_add_u32 v102, v103, 16, v102\n"
            "v_sub_u32 v98, v98, v86\n"
            "v_mad_i32_i16 v88, v99, v112, v88\n"
            "ds_add_u32 v112, v98 offset:65533\n"
            "v_sub_u32 v100, v100, v86\n"
            "v_mad_i32_i16 v88, v101, v113, v88\n"
            "ds_add_u32 v113, v100 offset:65533\n"
            "v_sub_u32 v102, v102, v86\n"
            "v_mad_i32_i16 v88, v103, v114, v88\n"
            "ds_add_u32 v114, v102 offset:65533\n"
            "Ls6_%=:\n"
            "s_cmp_gt_i32 s75, s74\n"
            "s_cbranch_scc1 1f\n"
            "s_waitcnt vmcnt(6)\n"
            "1:\n"
            "s_waitcnt vmcnt(11)\n"
            "s_cmp_eq_u32 s75, s79\n"
            "s_cbranch_scc1 Le6_%=\n"
            "Lc6_%=:\n"
            "v_and_b32 v104, 0x1fffc, v36\n"
            "v_and_b32 v105, 0x1fffc, v37\n"
            "v_and_b32 v106, 0x1fffc, v38\n"
            "ds_read_b32 v99, v104\n"
            "ds_read_b32 v101, v105\n"
            "ds_read_b32 v103, v106\n"
            "v_add_u32_dpp v111, v81, v82 row_newbcast:6 row_mask:0xf bank_mask:0x3\n"
            "v_add_u32_dpp v111, v81, v82 row_newbcast:14 row_mask:0xf bank_mask:0xc\n"
            "v_and_b32 v108, 0x7fe, v35\n"
            "v_and_b32 v109, 0x1ff800, v35\n"
            "v_lshrrev_b32 v110, 22, v35\n"
            "v_lshrrev_b32 v112, 15, v32\n"
            "v_lshrrev_b32 v113, 15, v33\n"
            "v_lshrrev_b32 v114, 15, v34\n"
            "v_and_b32 v111, " IDXMASK ", v111\n"
            "v_min_u32 v111, v111, v83\n"
            "buffer_load_dwordx4 v[32:35], v111, %[rs], 0 idxen " RAUX "\n"
            "v_fmamk_f32 v108, v108, 0x7e800000, v84\n"
            "v_fmamk_f32 v109, v109, 0x79800000, v84\n"
            "v_fmamk_f32 v110, v110, 0x7f000000, v84\n"
            "v_or_b32 v112, 3, v112\n"
            "v_or_b32 v113, 3, v113\n"
            "v_or_b32 v114, 3, v114\n"
            "s_sub_u32 s75, s75, 1\n"
            "s_waitcnt lgkmcnt(6)\n"
            "v_lshlrev_b32 v92, 16, v93\n"
            "v_lshlrev_b32 v94, 16, v95\n"
            "v_lshlrev_b32 v96, 16, v97\n"
            "v_pk_fma_f32 v[92:93], v[92:93], v[108:109], v[86:87] op_sel:[0,0,0] op_sel_hi:[1,0,1]\n"
            "v_pk_fma_f32 v[94:95], v[94:95], v[108:109], v[86:87] op_sel:[0,1,0] op_sel_hi:[1,1,1]\n"
            "v_pk_fma_f32 v[96:97], v[96:97], v[110:111], v[86:87] op_sel:[0,0,0] op_sel_hi:[1,0,1]\n"
            "v_lshl_add_u32 v92, v93, 16, v92\n"
            "v_lshl_add_u32 v94, v95, 16, v94\n"
            "v_lshl_add_u32 v96, v97, 16, v96\n"
            "v_sub_u32 v92, v92, v86\n"
            "v_mad_i32_i16 v88, v93, v112, v88\n"
            "ds_add_u32 v112, v92 offset:65533\n"
            "v_sub_u32 v94, v94, v86\n"
            "v_mad_i32_i16 v88, v95, v113, v88\n"
            "ds_add_u32 v113, v94 offset:65533\n"
            "v_sub_u32 v96, v96, v86\n"
            "v_mad_i32_i16 v88, v97, v114, v88\n"
            "ds_add_u32 v114, v96 offset:65533\n"
            "Ls7_%=:\n"
            "s_cmp_gt_i32 s75, s74\n"
            "s_cbranch_scc1 1f\n"
            "s_waitcnt vmcnt(6)\n"
            "1:\n"
            "s_waitcnt vmcnt(11)\n"
            "s_cmp_eq_u32 s75, s79\n"
            "s_cbranch_scc1 Le7_%=\n"
            "Lc7_%=:\n"
            "v_and_b32 v104, 0x1fffc, v8\n"
            "v_and_b32 v105, 0x1fffc, v9\n"
            "v_and_b32 v106, 0x1fffc, v10\n"
            "ds_read_b32 v93, v104\n"
            "ds_read_b32 v95, v105\n"
            "ds_read_b32 v97, v106\n"
            "v_add_u32_dpp v111, v81, v82 row_newbcast:7 row_mask:0xf bank_mask:0x3\n"
            "v_add_u32_dpp v111, v81, v82 row_newbcast:15 row_mask:0xf bank_mask:0xc\n"
            "v_and_b32 v108, 0x7fe, v39\n"
            "v_and_b32 v109, 0x1ff800, v39\n"
            "v_lshrrev_b32 v110, 22, v39\n"
            "v_lshrrev_b32 v112, 15, v36\n"
            "v_lshrrev_b32 v113, 15, v37\n"
            "v_lshrrev_b32 v114, 15, v38\n"
            "v_and_b32 v111, " IDXMASK ", v111\n"
            "v_min_u32 v111, v111, v83\n"
            "buffer_load_dwordx4 v[36:39], v111, %[rs], 0 idxen " RAUX "\n"
            "s_min_u32 s82, s82, 19\n"
            "s_add_u32 s82, s82, 1\n"
            "s_set_gpr_idx_on s82, gpr_idx(SRC0)\n"
            "s_nop 0\n"
            "v_mov_b32 v81, v60\n"
            "s_set_gpr_idx_off\n"
            "v_fmamk_f32 v108, v108, 0x7e800000, v84\n"
            "v_fmamk_f32 v109, v109, 0x79800000, v84\n"
            "v_fmamk_f32 v110, v110, 0x7f000000, v84\n"
            "v_or_b32 v112, 3, v112\n"
            "v_or_b32 v113, 3, v113\n"
            "v_or_b32 v114, 3, v114\n"
            "s_sub_u32 s75, s75, 1\n"
            "s_waitcnt lgkmcnt(6)\n"
            "v_lshlrev_b32 v98, 16, v99\n"
            "v_lshlrev_b32 v100, 16, v101\n"
            "v_lshlrev_b32 v102, 16, v103\n"
            "v_pk_fma_f32 v[98:99], v[98:99], v[108:109], v[86:87] op_sel:[0,0,0] op_sel_hi:[1,0,1]\n"
            "v_pk_fma_f32 v[100:101], v[100:101], v[108:109], v[86:87] op_sel:[0,1,0] op_sel_hi:[1,1,1]\n"
            "v_pk_fma_f32 v[102:103], v[102:103], v[110:111], v[86:87] op_sel:[0,0,0] op_sel_hi:[1,0,1]\n"
            "v_lshl_add_u32 v98, v99, 16, v98\n"
            "v_lshl_add_u32 v100, v101, 16, v100\n"
            "v_lshl_add_u32 v102, v103, 16, v102\n"
            "v_sub_u32 v98, v98, v86\n"
            "v_mad_i32_i16 v88, v99, v112, v88\n"
            "ds_add_u32 v112, v98 offset:65533\n"
            "v_sub_u32 v100, v100, v86\n"
            "v_mad_i32_i16 v88, v101, v113, v88\n"
            "ds_add_u32 v113, v100 offset:65533\n"
            "v_sub_u32 v102, v102, v86\n"
            "v_mad_i32_i16 v88, v103, v114, v88\n"
            "ds_add_u32 v114, v102 offset:65533\n"
            "s_branch Ls0_%=\n"
            "Le0_%=:\n"
            "s_cmp_lt_u32 s75, 8\n"
            "s_cbranch_scc1 1f\n"
            "s_swappc_b64 s[80:81], s[66:67]\n"
            "s_branch Lc0_%=\n"
            "1:\n"
            "s_cmp_lg_u32 s75, 7\n"
            "s_cbranch_scc1 1f\n"
            "s_setprio 2\n"
            "s_mov_b32 s79, 3\n"
            "s_branch Lc0_%=\n"
            "1:\n"
            "s_cmp_lg_u32 s75, 3\n"
            "s_cbranch_scc1 1f\n"
            "s_setprio 1\n"
            "s_mov_b32 s79, 1\n"
            "s_branch Lc0_%=\n"
            "1:\n"
            "s_setprio 0\n"
            "v_add_u32_dpp v111, v81, v82 row_newbcast:0 row_mask:0xf bank_mask:0x3\n"
            "v_add_u32_dpp v111, v81, v82 row_newbcast:8 row_mask:0xf bank_mask:0xc\n"
            "v_and_b32 v108, 0x7fe, v11\n"
            "v_and_b32 v109, 0x1ff800, v11\n"
            "v_lshrrev_b32 v110, 22, v11\n"
            "v_lshrrev_b32 v112, 15, v8\n"
            "v_lshrrev_b32 v113, 15, v9\n"
            "v_lshrrev_b32 v114, 15, v10\n"
            "v_and_b32 v111, " IDXMASK ", v111\n"
            "v_min_u32 v111, v111, v83\n"
            "buffer_load_dwordx4 v[8:11], v111, %[rs], 0 idxen " RAUX "\n"
            "v_fmamk_f32 v108, v108, 0x7e800000, v84\n"
            "v_fmamk_f32 v109, v109, 0x79800000, v84\n"
            "v_fmamk_f32 v110, v110, 0x7f000000, v84\n"
            "v_or_b32 v112, 3, v112\n"
            "v_or_b32 v113, 3, v113\n"
            "v_or_b32 v114, 3, v114\n"
            "s_waitcnt lgkmcnt(3)\n"
            "s_swappc_b64 s[80:81], s[70:71]\n"
            "v_lshlrev_b32 v92, 16, v93\n"
            "v_lshlrev_b32 v94, 16, v95\n"
            "v_lshlrev_b32 v96, 16, v97\n"
            "v_pk_fma_f32 v[92:93], v[92:93], v[108:109], v[86:87] op_sel:[0,0,0] op_sel_hi:[1,0,1]\n"
            "v_pk_fma_f32 v[94:95], v[94:95], v[108:109], v[86:87] op_sel:[0,1,0] op_sel_hi:[1,1,1]\n"
            "v_pk_fma_f32 v[96:97], v[96:97], v[110:111], v[86:87] op_sel:[0,0,0] op_sel_hi:[1,0,1]\n"
            "v_lshl_add_u32 v92, v93, 16, v92\n"
            "v_lshl_add_u32 v94, v95, 16, v94\n"
            "v_lshl_add_u32 v96, v97, 16, v96\n"
            "v_sub_u32 v92, v92, v86\n"
            "v_mad_i32_i16 v88, v93, v112, v88\n"
            "ds_add_u32 v112, v92 offset:65533\n"
            "v_sub_u32 v94, v94, v86\n"
            "v_mad_i32_i16 v88, v95, v113, v88\n"
            "ds_add_u32 v113, v94 offset:65533\n"
            "v_sub_u32 v96, v96, v86\n"
            "v_mad_i32_i16 v88, v97, v114, v88\n"
            "ds_add_u32 v114, v96 offset:65533\n"
            "v_and_b32 v104, 0x1fffc, v12\n"
            "v_and_b32 v105, 0x1fffc, v13\n"
            "v_and_b32 v106, 0x1fffc, v14\n"
            "s_swappc_b64 s[80:81], s[72:73]\n"
            "ds_read_b32 v99, v104\n"
            "ds_read_b32 v101, v105\n"
            "ds_read_b32 v103, v106\n"
            "s_waitcnt lgkmcnt(0)\n"
            "s_branch Ls1_%=\n"
            "Le1_%=:\n"
            "s_cmp_lt_u32 s75, 8\n"
            "s_cbranch_scc1 1f\n"
            "s_swappc_b64 s[80:81], s[66:67]\n"
            "s_branch Lc1_%=\n"
            "1:\n"
            "s_cmp_lg_u32 s75, 7\n"
            "s_cbranch_scc1 1f\n"
            "s_setprio 2\n"
            "s_mov_b32 s79, 3\n"
            "s_branch Lc1_%=\n"
            "1:\n"
            "s_cmp_lg_u32 s75, 3\n"
            "s_cbranch_scc1 1f\n"
            "s_setprio 1\n"
            "s_mov_b32 s79, 1\n"
            "s_branch Lc1_%=\n"
            "1:\n"
            "s_setprio 0\n"
            "v_add_u32_dpp v111, v81, v82 row_newbcast:1 row_mask:0xf bank_mask:0x3\n"
            "v_add_u32_dpp v111, v81, v82 row_newbcast:9 row_mask:0xf bank_mask:0xc\n"
            "v_and_b32 v108, 0x7fe, v15\n"
            "v_and_b32 v109, 0x1ff800, v15\n"
            "v_lshrrev_b32 v110, 22, v15\n"
            "v_lshrrev_b32 v112, 15, v12\n"
            "v_lshrrev_b32 v113, 15, v13\n"
            "v_lshrrev_b32 v114, 15, v14\n"
            "v_and_b32 v111, " IDXMASK ", v111\n"
            "v_min_u32 v111, v111, v83\n"
            "buffer_load_dwordx4 v[12:15], v111, %[rs], 0 idxen " RAUX "\n"
            "v_fmamk_f32 v108, v108, 0x7e800000, v84\n"
            "v_fmamk_f32 v109, v109, 0x79800000, v84\n"
            "v_fmamk_f32 v110, v110, 0x7f000000, v84\n"
            "v_or_b32 v112, 3, v112\n"
            "v_or_b32 v113, 3, v113\n"
            "v_or_b32 v114, 3, v114\n"
            "s_waitcnt lgkmcnt(3)\n"
            "s_swappc_b64 s[80:81], s[70:71]\n"
            "v_lshlrev_b32 v98, 16, v99\n"
            "v_lshlrev_b32 v100, 16, v101\n"
            "v_lshlrev_b32 v102, 16, v103\n"
            "v_pk_fma_f32 v[98:99], v[98:99], v[108:109], v[86:87] op_sel:[0,0,0] op_sel_hi:[1,0,1]\n"
            "v_pk_fma_f32 v[100:101], v[100:101], v[108:109], v[86:87] op_sel:[0,1,0] op_sel_hi:[1,1,1]\n"
            "v_pk_fma_f32 v[102:103], v[102:103], v[110:111], v[86:87] op_sel:[0,0,0] op_sel_hi:[1,0,1]\n"
            "v_lshl_add_u32 v98, v99, 16, v98\n"
            "v_lshl_add_u32 v100, v101, 16, v100\n"
            "v_lshl_add_u32 v102, v103, 16, v102\n"
            "v_sub_u32 v98, v98, v86\n"
            "v_mad_i32_i16 v88, v99, v112, v88\n"
            "ds_add_u32 v112, v98 offset:65533\n"
            "v_sub_u32 v100, v100, v86\n"
            "v_mad_i32_i16 v88, v101, v113, v88\n"
            "ds_add_u32 v113, v100 offset:65533\n"
            "v_sub_u32 v102, v102, v86\n"
            "v_mad_i32_i16 v88, v103, v114, v88\n"
            "ds_add_u32 v114, v102 offset:65533\n"
            "v_and_b32 v104, 0x1fffc, v16\n"
            "v_and_b32 v105, 0x1fffc, v17\n"
            "v_and_b32 v106, 0x1fffc, v18\n"
            "s_swappc_b64 s[80:81], s[72:73]\n"
            "ds_read_b32 v93, v104\n"
            "ds_read_b32 v95, v105\n"
            "ds_read_b32 v97, v106\n"
            "s_waitcnt lgkmcnt(0)\n"
            "s_branch Ls2_%=\n"
            "Le2_%=:\n"
            "s_cmp_lt_u32 s75, 8\n"
            "s_cbranch_scc1 1f\n"
            "s_swappc_b64 s[80:81], s[66:67]\n"
            "s_branch Lc2_%=\n"
            "1:\n"
            "s_cmp_lg_u32 s75, 7\n"
            "s_cbranch_scc1 1f\n"
            "s_setprio 2\n"
            "s_mov_b32 s79, 3\n"
            "s_branch Lc2_%=\n"
            "1:\n"
            "s_cmp_lg_u32 s75, 3\n"
            "s_cbranch_scc1 1f\n"
            "s_setprio 1\n"
            "s_mov_b32 s79, 1\n"
            "s_branch Lc2_%=\n"
            "1:\n"
            "s_setprio 0\n"
            "v_add_u32_dpp v111, v81, v82 row_newbcast:2 row_mask:0xf bank_mask:0x3\n"
            "v_add_u32_dpp v111, v81, v82 row_newbcast:10 row_mask:0xf bank_mask:0xc\n"
            "v_and_b32 v108, 0x7fe, v19\n"
            "v_and_b32 v109, 0x1ff800, v19\n"
            "v_lshrrev_b32 v110, 22, v19\n"
            "v_lshrrev_b32 v112, 15, v16\n"
            "v_lshrrev_b32 v113, 15, v17\n"
            "v_lshrrev_b32 v114, 15, v18\n"
            "v_and_b32 v111, " IDXMASK ", v111\n"
            "v_min_u32 v111, v111, v83\n"
            "buffer_load_dwordx4 v[16:19], v111, %[rs], 0 idxen " RAUX "\n"
            "v_fmamk_f32 v108, v108, 0x7e800000, v84\n"
            "v_fmamk_f32 v109, v109, 0x79800000, v84\n"
            "v_fmamk_f32 v110, v110, 0x7f000000, v84\n"
            "v_or_b32 v112, 3, v112\n"
            "v_or_b32 v113, 3, v113\n"
            "v_or_b32 v114, 3, v114\n"
            "s_waitcnt lgkmcnt(3)\n"
            "s_swappc_b64 s[80:81], s[70:71]\n"
            "v_lshlrev_b32 v92, 16, v93\n"
            "v_lshlrev_b32 v94, 16, v95\n"
            "v_lshlrev_b32 v96, 16, v97\n"
            "v_pk_fma_f32 v[92:93], v[92:93], v[108:109], v[86:87] op_sel:[0,0,0] op_sel_hi:[1,0,1]\n"
            "v_pk_fma_f32 v[94:95], v[94:95], v[108:109], v[86:87] op_sel:[0,1,0] op_sel_hi:[1,1,1]\n"
            "v_pk_fma_f32 v[96:97], v[96:97], v[110:111], v[86:87] op_sel:[0,0,0] op_sel_hi:[1,0,1]\n"
            "v_lshl_add_u32 v92, v93, 16, v92\n"
            "v_lshl_add_u32 v94, v95, 16, v94\n"
            "v_lshl_add_u32 v96, v97, 16, v96\n"
            "v_sub_u32 v92, v92, v86\n"
            "v_mad_i32_i16 v88, v93, v112, v88\n"
            "ds_add_u32 v112, v92 offset:65533\n"
            "v_sub_u32 v94, v94, v86\n"
            "v_mad_i32_i16 v88, v95, v113, v88\n"
            "ds_add_u32 v113, v94 offset:65533\n"
            "v_sub_u32 v96, v96, v86\n"
            "v_mad_i32_i16 v88, v97, v114, v88\n"
            "ds_add_u32 v114, v96 offset:65533\n"
            "v_and_b32 v104, 0x1fffc, v20\n"
            "v_and_b32 v105, 0x1fffc, v21\n"
            "v_and_b32 v106, 0x1fffc, v22\n"
            "s_swappc_b64 s[80:81], s[72:73]\n"
            "ds_read_b32 v99, v104\n"
            "ds_read_b32 v101, v105\n"
            "ds_read_b32 v103, v106\n"
            "s_waitcnt lgkmcnt(0)\n"
            "s_branch Ls3_%=\n"
            "Le3_%=:\n"
            "s_cmp_lt_u32 s75, 8\n"
            "s_cbranch_scc1 1f\n"
            "s_swappc_b64 s[80:81], s[66:67]\n"
            "s_branch Lc3_%=\n"
            "1:\n"
            "s_cmp_lg_u32 s75, 7\n"
            "s_cbranch_scc1 1f\n"
            "s_setprio 2\n"
            "s_mov_b32 s79, 3\n"
            "s_branch Lc3_%=\n"
            "1:\n"
            "s_cmp_lg_u32 s75, 3\n"
            "s_cbranch_scc1 1f\n"
            "s_setprio 1\n"
            "s_mov_b32 s79, 1\n"
            "s_branch Lc3_%=\n"
            "1:\n"
            "s_setprio 0\n"
            "v_add_u32_dpp v111, v81, v82 row_newbcast:3 row_mask:0xf bank_mask:0x3\n"
            "v_add_u32_dpp v111, v81, v82 row_newbcast:11 row_mask:0xf bank_mask:0xc\n"
            "v_and_b32 v108, 0x7fe, v23\n"
            "v_and_b32 v109, 0x1ff800, v23\n"
            "v_lshrrev_b32 v110, 22, v23\n"
            "v_lshrrev_b32 v112, 15, v20\n"
            "v_lshrrev_b32 v113, 15, v21\n"
            "v_lshrrev_b32 v114, 15, v22\n"
            "v_and_b32 v111, " IDXMASK ", v111\n"
            "v_min_u32 v111, v111, v83\n"
            "buffer_load_dwordx4 v[20:23], v111, %[rs], 0 idxen " RAUX "\n"
            "v_fmamk_f32 v108, v108, 0x7e800000, v84\n"
            "v_fmamk_f32 v109, v109, 0x79800000, v84\n"
            "v_fmamk_f32 v110, v110, 0x7f000000, v84\n"
            "v_or_b32 v112, 3, v112\n"
            "v_or_b32 v113, 3, v113\n"
            "v_or_b32 v114, 3, v114\n"
            "s_waitcnt lgkmcnt(3)\n"
            "s_swappc_b64 s[80:81], s[70:71]\n"
            "v_lshlrev_b32 v98, 16, v99\n"
            "v_lshlrev_b32 v100, 16, v101\n"
            "v_lshlrev_b32 v102, 16, v103\n"
            "v_pk_fma_f32 v[98:99], v[98:99], v[108:109], v[86:87] op_sel:[0,0,0] op_sel_hi:[1,0,1]\n"
            "v_pk_fma_f32 v[100:101], v[100:101], v[108:109], v[86:87] op_sel:[0,1,0] op_sel_hi:[1,1,1]\n"
            "v_pk_fma_f32 v[102:103], v[102:103], v[110:111], v[86:87] op_sel:[0,0,0] op_sel_hi:[1,0,1]\n"
            "v_lshl_add_u32 v98, v99, 16, v98\n"
            "v_lshl_add_u32 v100, v101, 16, v100\n"
            "v_lshl_add_u32 v102, v103, 16, v102\n"
            "v_sub_u32 v98, v98, v86\n"
            "v_mad_i32_i16 v88, v99, v112, v88\n"
            "ds_add_u32 v112, v98 offset:65533\n"
            "v_sub_u32 v100, v100, v86\n"
            "v_mad_i32_i16 v88, v101, v113, v88\n"
            "ds_add_u32 v113, v100 offset:65533\n"
            "v_sub_u32 v102, v102, v86\n"
            "v_mad_i32_i16 v88, v103, v114, v88\n"
            "ds_add_u32 v114, v102 offset:65533\n"
            "v_and_b32 v104, 0x1fffc, v24\n"
            "v_and_b32 v105, 0x1fffc, v25\n"
            "v_and_b32 v106, 0x1fffc, v26\n"
            "s_swappc_b64 s[80:81], s[72:73]\n"
            "ds_read_b32 v93, v104\n"
            "ds_read_b32 v95, v105\n"
            "ds_read_b32 v97, v106\n"
            "s_waitcnt lgkmcnt(0)\n"
            "s_branch Ls4_%=\n"
            "Le4_%=:\n"
            "s_cmp_lt_u32 s75, 8\n"
            "s_cbranch_scc1 1f\n"
            "s_swappc_b64 s[80:81], s[66:67]\n"
            "s_branch Lc4_%=\n"
            "1:\n"
            "s_cmp_lg_u32 s75, 7\n"
            "s_cbranch_scc1 1f\n"
            "s_setprio 2\n"
            "s_mov_b32 s79, 3\n"
            "s_branch Lc4_%=\n"
            "1:\n"
            "s_cmp_lg_u32 s75, 3\n"
            "s_cbranch_scc1 1f\n"
            "s_setprio 1\n"
            "s_mov_b32 s79, 1\n"
            "s_branch Lc4_%=\n"
            "1:\n"
            "s_setprio 0\n"
            "v_add_u32_dpp v111, v81, v82 row_newbcast:4 row_mask:0xf bank_mask:0x3\n"
            "v_add_u32_dpp v111, v81, v82 row_newbcast:12 row_mask:0xf bank_mask:0xc\n"
            "v_and_b32 v108, 0x7fe, v27\n"
            "v_and_b32 v109, 0x1ff800, v27\n"
            "v_lshrrev_b32 v110, 22, v27\n"
            "v_lshrrev_b32 v112, 15, v24\n"
            "v_lshrrev_b32 v113, 15, v25\n"
            "v_lshrrev_b32 v114, 15, v26\n"
            "v_and_b32 v111, " IDXMASK ", v111\n"
            "v_min_u32 v111, v111, v83\n"
            "buffer_load_dwordx4 v[24:27], v111, %[rs], 0 idxen " RAUX "\n"
            "v_fmamk_f32 v108, v108, 0x7e800000, v84\n"
            "v_fmamk_f32 v109, v109, 0x79800000, v84\n"
            "v_fmamk_f32 v110, v110, 0x7f000000, v84\n"
            "v_or_b32 v112, 3, v112\n"
            "v_or_b32 v113, 3, v113\n"
            "v_or_b32 v114, 3, v114\n"
            "s_waitcnt lgkmcnt(3)\n"
            "s_swappc_b64 s[80:81], s[70:71]\n"
            "v_lshlrev_b32 v92, 16, v93\n"
            "v_lshlrev_b32 v94, 16, v95\n"
            "v_lshlrev_b32 v96, 16, v97\n"
            "v_pk_fma_f32 v[92:93], v[92:93], v[108:109], v[86:87] op_sel:[0,0,0] op_sel_hi:[1,0,1]\n"
            "v_pk_fma_f32 v[94:95], v[94:95], v[108:109], v[86:87] op_sel:[0,1,0] op_sel_hi:[1,1,1]\n"
            "v_pk_fma_f32 v[96:97], v[96:97], v[110:111], v[86:87] op_sel:[0,0,0] op_sel_hi:[1,0,1]\n"
            "v_lshl_add_u32 v92, v93, 16, v92\n"
            "v_lshl_add_u32 v94, v95, 16, v94\n"
            "v_lshl_add_u32 v96, v97, 16, v96\n"
            "v_sub_u32 v92, v92, v86\n"
            "v_mad_i32_i16 v88, v93, v112, v88\n"
            "ds_add_u32 v112, v92 offset:65533\n"
            "v_sub_u32 v94, v94, v86\n"
            "v_mad_i32_i16 v88, v95, v113, v88\n"
            "ds_add_u32 v113, v94 offset:65533\n"
            "v_sub_u32 v96, v96, v86\n"
            "v_mad_i32_i16 v88, v97, v114, v88\n"
            "ds_add_u32 v114, v96 offset:65533\n"
            "v_and_b32 v104, 0x1fffc, v28\n"
            "v_and_b32 v105, 0x1fffc, v29\n"
            "v_and_b32 v106, 0x1fffc, v30\n"
            "s_swappc_b64 s[80:81], s[72:73]\n"
            "ds_read_b32 v99, v104\n"
            "ds_read_b32 v101, v105\n"
            "ds_read_b32 v103, v106\n"
            "s_waitcnt lgkmcnt(0)\n"
            "s_branch Ls5_%=\n"
            "Le5_%=:\n"
            "s_cmp_lt_u32 s75, 8\n"
            "s_cbranch_scc1 1f\n"
            "s_swappc_b64 s[80:81], s[66:67]\n"
            "s_branch Lc5_%=\n"
            "1:\n"
            "s_cmp_lg_u32 s75, 7\n"
            "s_cbranch_scc1 1f\n"
            "s_setprio 2\n"
            "s_mov_b32 s79, 3\n"
            "s_branch Lc5_%=\n"
            "1:\n"
            "s_cmp_lg_u32 s75, 3\n"
            "s_cbranch_scc1 1f\n"
            "s_setprio 1\n"
            "s_mov_b32 s79, 1\n"
            "s_branch Lc5_%=\n"
            "1:\n"
            "s_setprio 0\n"
            "v_add_u32_dpp v111, v81, v82 row_newbcast:5 row_mask:0xf bank_mask:0x3\n"
            "v_add_u32_dpp v111, v81, v82 row_newbcast:13 row_mask:0xf bank_mask:0xc\n"
            "v_and_b32 v108, 0x7fe, v31\n"
            "v_and_b32 v109, 0x1ff800, v31\n"
            "v_lshrrev_b32 v110, 22, v31\n"
            "v_lshrrev_b32 v112, 15, v28\n"
            "v_lshrrev_b32 v113, 15, v29\n"
            "v_lshrrev_b32 v114, 15, v30\n"
            "v_and_b32 v111, " IDXMASK ", v111\n"
            "v_min_u32 v111, v111, v83\n"
            "buffer_load_dwordx4 v[28:31], v111, %[rs], 0 idxen " RAUX "\n"
            "v_fmamk_f32 v108, v108, 0x7e800000, v84\n"
            "v_fmamk_f32 v109, v109, 0x79800000, v84\n"
            "v_fmamk_f32 v110, v110, 0x7f000000, v84\n"
            "v_or_b32 v112, 3, v112\n"
            "v_or_b32 v113, 3, v113\n"
            "v_or_b32 v114, 3, v114\n"
            "s_waitcnt lgkmcnt(3)\n"
            "s_swappc_b64 s[80:81], s[70:71]\n"
            "v_lshlrev_b32 v98, 16, v99\n"
            "v_lshlrev_b32 v100, 16, v101\n"
            "v_lshlrev_b32 v102, 16, v103\n"
            "v_pk_fma_f32 v[98:99], v[98:99], v[108:109], v[86:87] op_sel:[0,0,0] op_sel_hi:[1,0,1]\n"
            "v_pk_fma_f32 v[100:101], v[100:101], v[108:109], v[86:87] op_sel:[0,1,0] op_sel_hi:[1,1,1]\n"
            "v_pk_fma_f32 v[102:103], v[102:103], v[110:111], v[86:87] op_sel:[0,0,0] op_sel_hi:[1,0,1]\n"
            "v_lshl_add_u32 v98, v99, 16, v98\n"
            "v_lshl_add_u32 v100, v101, 16, v100\n"
            "v_lshl_add_u32 v102, v103, 16, v102\n"
            "v_sub_u32 v98, v98, v86\n"
            "v_mad_i32_i16 v88, v99, v112, v88\n"
            "ds_add_u32 v112, v98 offset:65533\n"
            "v_sub_u32 v100, v100, v86\n"
            "v_mad_i32_i16 v88, v101, v113, v88\n"
            "ds_add_u32 v113, v100 offset:65533\n"
            "v_sub_u32 v102, v102, v86\n"
            "v_mad_i32_i16 v88, v103, v114, v88\n"
            "ds_add_u32 v114, v102 offset:65533\n"
            "v_and_b32 v104, 0x1fffc, v32\n"
            "v_and_b32 v105, 0x1fffc, v33\n"
            "v_and_b32 v106, 0x1fffc, v34\n"
            "s_swappc_b64 s[80:81], s[72:73]\n"
            "ds_read_b32 v93, v104\n"
            "ds_read_b32 v95, v105\n"
            "ds_read_b32 v97, v106\n"
            "s_waitcnt lgkmcnt(0)\n"
            "s_branch Ls6_%=\n"
            "Le6_%=:\n"
            "s_cmp_lt_u32 s75, 8\n"
            "s_cbranch_scc1 1f\n"
            "s_swappc_b64 s[80:81], s[66:67]\n"
            "s_branch Lc6_%=\n"
            "1:\n"
            "s_cmp_lg_u32 s75, 7\n"
            "s_cbranch_scc1 1f\n"
            "s_setprio 2\n"
            "s_mov_b32 s79, 3\n"
            "s_branch Lc6_%=\n"
            "1:\n"
            "s_cmp_lg_u32 s75, 3\n"
            "s_cbranch_scc1 1f\n"
            "s_setprio 1\n"
            "s_mov_b32 s79, 1\n"
            "s_branch Lc6_%=\n"
            "1:\n"
            "s_setprio 0\n"
            "v_add_u32_dpp v111, v81, v82 row_newbcast:6 row_mask:0xf bank_mask:0x3\n"
            "v_add_u32_dpp v111, v81, v82 row_newbcast:14 row_mask:0xf bank_mask:0xc\n"
            "v_and_b32 v108, 0x7fe, v35\n"
            "v_and_b32 v109, 0x1ff800, v35\n"
            "v_lshrrev_b32 v110, 22, v35\n"
            "v_lshrrev_b32 v112, 15, v32\n"
            "v_lshrrev_b32 v113, 15, v33\n"
            "v_lshrrev_b32 v114, 15, v34\n"
            "v_and_b32 v111, " IDXMASK ", v111\n"
            "v_min_u32 v111, v111, v83\n"
            "buffer_load_dwordx4 v[32:35], v111, %[rs], 0 idxen " RAUX "\n"
            "v_fmamk_f32 v108, v108, 0x7e800000, v84\n"
            "v_fmamk_f32 v109, v109, 0x79800000, v84\n"
            "v_fmamk_f32 v110, v110, 0x7f000000, v84\n"
            "v_or_b32 v112, 3, v112\n"
            "v_or_b32 v113, 3, v113\n"
            "v_or_b32 v114, 3, v114\n"
            "s_waitcnt lgkmcnt(3)\n"
            "s_swappc_b64 s[80:81], s[70:71]\n"
            "v_lshlrev_b32 v92, 16, v93\n"
            "v_lshlrev_b32 v94, 16, v95\n"
            "v_lshlrev_b32 v96, 16, v97\n"
            "v_pk_fma_f32 v[92:93], v[92:93], v[108:109], v[86:87] op_sel:[0,0,0] op_sel_hi:[1,0,1]\n"
            "v_pk_fma_f32 v[94:95], v[94:95], v[108:109], v[86:87] op_sel:[0,1,0] op_sel_hi:[1,1,1]\n"
            "v_pk_fma_f32 v[96:97], v[96:97], v[110:111], v[86:87] op_sel:[0,0,0] op_sel_hi:[1,0,1]\n"
            "v_lshl_add_u32 v92, v93, 16, v92\n"
            "v_lshl_add_u32 v94, v95, 16, v94\n"
            "v_lshl_add_u32 v96, v97, 16, v96\n"
            "v_sub_u32 v92, v92, v86\n"
            "v_mad_i32_i16 v88, v93, v112, v88\n"
            "ds_add_u32 v112, v92 offset:65533\n"
            "v_sub_u32 v94, v94, v86\n"
            "v_mad_i32_i16 v88, v95, v113, v88\n"
            "ds_add_u32 v113, v94 offset:65533\n"
            "v_sub_u32 v96, v96, v86\n"
            "v_mad_i32_i16 v88, v97, v114, v88\n"
            "ds_add_u32 v114, v96 offset:65533\n"
            "v_and_b32 v104, 0x1fffc, v36\n"
            "v_and_b32 v105, 0x1fffc, v37\n"
            "v_and_b32 v106, 0x1fffc, v38\n"
            "s_swappc_b64 s[80:81], s[72:73]\n"
            "ds_read_b32 v99, v104\n"
            "ds_read_b32 v101, v105\n"
            "ds_read_b32 v103, v106\n"
            "s_waitcnt lgkmcnt(0)\n"
            "s_branch Ls7_%=\n"
            "Le7_%=:\n"
            "s_cmp_lt_u32 s75, 8\n"
            "s_cbranch_scc1 1f\n"
            "s_swappc_b64 s[80:81], s[66:67]\n"
            "s_branch Lc7_%=\n"
            "1:\n"
            "s_cmp_lg_u32 s75, 7\n"
            "s_cbranch_scc1 1f\n"
            "s_setprio 2\n"
            "s_mov_b32 s79, 3\n"
            "s_branch Lc7_%=\n"
            "1:\n"
            "s_cmp_lg_u32 s75, 3\n"
            "s_cbranch_scc1 1f\n"
            "s_setprio 1\n"
            "s_mov_b32 s79, 1\n"
            "s_branch Lc7_%=\n"
            "1:\n"
            "s_setprio 0\n"
            "v_add_u32_dpp v111, v81, v82 row_newbcast:7 row_mask:0xf bank_mask:0x3\n"
            "v_add_u32_dpp v111, v81, v82 row_newbcast:15 row_mask:0xf bank_mask:0xc\n"
            "v_and_b32 v108, 0x7fe, v39\n"
            "v_and_b32 v109, 0x1ff800, v39\n"
            "v_lshrrev_b32 v110, 22, v39\n"
            "v_lshrrev_b32 v112, 15, v36\n"
            "v_lshrrev_b32 v113, 15, v37\n"
            "v_lshrrev_b32 v114, 15, v38\n"
            "v_and_b32 v111, " IDXMASK ", v111\n"
            "v_min_u32 v111, v111, v83\n"
            "buffer_load_dwordx4 v[36:39], v111, %[rs], 0 idxen " RAUX "\n"
            "s_min_u32 s82, s82, 19\n"
            "s_add_u32 s82, s82, 1\n"
            "s_set_gpr_idx_on s82, gpr_idx(SRC0)\n"
            "s_nop 0\n"
            "v_mov_b32 v81, v60\n"
            "s_set_gpr_idx_off\n"
            "v_fmamk_f32 v108, v108, 0x7e800000, v84\n"
            "v_fmamk_f32 v109, v109, 0x79800000, v84\n"
            "v_fmamk_f32 v110, v110, 0x7f000000, v84\n"
            "v_or_b32 v112, 3, v112\n"
            "v_or_b32 v113, 3, v113\n"
            "v_or_b32 v114, 3, v114\n"
            "s_waitcnt lgkmcnt(3)\n"
            "s_swappc_b64 s[80:81], s[70:71]\n"
            "v_lshlrev_b32 v98, 16, v99\n"
            "v_lshlrev_b32 v100, 16, v101\n"
            "v_lshlrev_b32 v102, 16, v103\n"
            "v_pk_fma_f32 v[98:99], v[98:99], v[108:109], v[86:87] op_sel:[0,0,0] op_sel_hi:[1,0,1]\n"
            "v_pk_fma_f32 v[100:101], v[100:101], v[108:109], v[86:87] op_sel:[0,1,0] op_sel_hi:[1,1,1]\n"
            "v_pk_fma_f32 v[102:103], v[102:103], v[110:111], v[86:87] op_sel:[0,0,0] op_sel_hi:[1,0,1]\n"
            "v_lshl_add_u32 v98, v99, 16, v98\n"
            "v_lshl_add_u32 v100, v101, 16, v100\n"
            "v_lshl_add_u32 v102, v103, 16, v102\n"
            "v_sub_u32 v98, v98, v86\n"
            "v_mad_i32_i16 v88, v99, v112, v88\n"
            "ds_add_u32 v112, v98 offset:65533\n"
            "v_sub_u32 v100, v100, v86\n"
            "v_mad_i32_i16 v88, v101, v113, v88\n"
            "ds_add_u32 v113, v100 offset:65533\n"
            "v_sub_u32 v102, v102, v86\n"
            "v_mad_i32_i16 v88, v103, v114, v88\n"
            "ds_add_u32 v114, v102 offset:65533\n"
            "v_and_b32 v104, 0x1fffc, v8\n"
            "v_and_b32 v105, 0x1fffc, v9\n"
            "v_and_b32 v106, 0x1fffc, v10\n"
            "s_swappc_b64 s[80:81], s[72:73]\n"
            "ds_read_b32 v93, v104\n"
            "ds_read_b32 v95, v105\n"
            "ds_read_b32 v97, v106\n"
            "s_waitcnt lgkmcnt(0)\n"
            "s_branch Ls0_%=\n"
            "Lchg1%=:\n"
            "s_add_u32 s92, s92, 1\n"
            "s_cmp_eq_u32 s92, 8\n"
            "s_cbranch_scc1 1f\n"
            "s_barrier\n"
            "s_mov_b32 m0, %[wb]\n"
            "s_nop 0\n"
            "ds_write_addtid_b32 v40 offset:0\n"
            "ds_write_addtid_b32 v41 offset:256\n"
            "ds_write_addtid_b32 v42 offset:512\n"
            "ds_write_addtid_b32 v43 offset:768\n"
            "ds_write_addtid_b32 v44 offset:16384\n"
            "ds_write_addtid_b32 v45 offset:16640\n"
            "ds_write_addtid_b32 v46 offset:16896\n"
            "ds_write_addtid_b32 v47 offset:17152\n"
            "ds_write_addtid_b32 v48 offset:32768\n"
            "ds_write_addtid_b32 v49 offset:33024\n"
            "ds_write_addtid_b32 v50 offset:33280\n"
            "ds_write_addtid_b32 v51 offset:33536\n"
            "ds_write_addtid_b32 v52 offset:49152\n"
            "ds_write_addtid_b32 v53 offset:49408\n"
            "ds_write_addtid_b32 v54 offset:49664\n"
            "ds_write_addtid_b32 v55 offset:49920\n"
            "s_add_u32 m0, %[wb], 16384\n"
            "s_nop 0\n"
            "ds_write_addtid_b32 v56 offset:49152\n"
            "ds_write_addtid_b32 v57 offset:49408\n"
            "ds_write_addtid_b32 v58 offset:49664\n"
            "ds_write_addtid_b32 v59 offset:49920\n"
            "1:\n"
            "s_setpc_b64 s[80:81]\n"
            "Lchg2%=:\n"
            "s_cmp_eq_u32 s92, 8\n"
            "s_cbranch_scc1 Lfin%=\n"
            "s_lshr_b64 s[76:77], s[76:77], 8\n"
            "s_and_b32 s75, s76, 255\n"
            "s_movk_i32 s74, 1000\n"
            "s_sub_i32 s79, s75, %[j0]\n"
            "s_max_i32 s79, s79, 8\n"
            "s_cmp_eq_u32 s92, 7\n s_cselect_b32 s79, 8, s79\n"
            "s_setprio 3\n"
            "s_waitcnt lgkmcnt(3)\n"
            "s_barrier\n"
            "s_setpc_b64 s[80:81]\n"
            "Lchg3%=:\n"
            "s_sub_u32 s74, s75, 7\n"
            "s_mov_b32 s79, 7\n"
            "s_cmp_eq_u32 s92, 7\n"
            "s_cbranch_scc1 1f\n"
            "s_add_u32 s94, s94, %[i4]\n"
            "buffer_load_dwordx4 v[40:43], %[tid16], %[rm], s94 offen\n"
            "s_add_u32 s96, s94, 16384\n"
            "buffer_load_dwordx4 v[44:47], %[tid16], %[rm], s96 offen\n"
            "s_add_u32 s96, s94, 32768\n"
            "buffer_load_dwordx4 v[48:51], %[tid16], %[rm], s96 offen\n"
            "s_add_u32 s96, s94, 49152\n"
            "buffer_load_dwordx4 v[52:55], %[tid16], %[rm], s96 offen\n"
            "s_add_u32 s96, s94, 65536\n"
            "buffer_load_dwordx4 v[56:59], %[tid16], %[rm], s96 offen\n"
            "s_branch 2f\n"
            "1:\n"
            "s_mov_b32 s74, 0\n"
            "buffer_load_dword v50, v80, %[rd], 0 offen\n"
            "buffer_load_dword v48, %[ed], %[rd], 0 offen " NTH "\n"
            "buffer_load_dword v49, %[ed], %[rd], %[e1] offen " NTH "\n"
            "v_lshlrev_b32 v52, 2, %[ed]\n"
            "s_lshl_b32 s96, %[e1], 2\n"
            "buffer_load_dwordx4 v[40:43], v52, %[rmm], 0 offen " NTH "\n"
            "buffer_load_dwordx4 v[44:47], v52, %[rmm], s96 offen " NTH "\n"
            "2:\n"
            "s_setpc_b64 s[80:81]\n"
            "Lfin%=:\n"
            "s_waitcnt lgkmcnt(0)\n"
            "s_barrier\n"
            "ds_read_b128 v[60:63], v89 offset:0\n"
            "ds_read_b128 v[64:67], v89 offset:16384\n"
            "ds_read_b128 v[68:71], v89 offset:32768\n"
            "ds_read_b128 v[72:75], v89 offset:49152\n"
            "ds_read_b128 v[76:79], v90\n"
            "v_subrev_u32 v117, 65533, v89\n"
            "v_mov_b32 v118, 0\n"
            "v_bfe_u32 v116, %[tid16], 9, 3\n"
            "v_cmp_eq_u32 vcc, %[q], v116\n"
            "v_bfrev_b32 v115, 1\n"
            "s_nop 0\n"
            "v_cndmask_b32 v115, %[tid16], v115, vcc\n"
            "s_mov_b32 s96, 16384\n s_mov_b32 s97, 32768\n s_mov_b32 s92, 49152\n s_mov_b32 s93, 65536\n"
            "s_waitcnt lgkmcnt(4)\n"
            "buffer_store_dwordx4 v[60:63], v115, %[rp0], 0 offen sc1\n"
            "s_waitcnt lgkmcnt(3)\n"
            "buffer_store_dwordx4 v[64:67], v115, %[rp0], s96 offen sc1\n"
            "s_waitcnt lgkmcnt(2)\n"
            "buffer_store_dwordx4 v[68:71], v115, %[rp0], s97 offen sc1\n"
            "s_waitcnt lgkmcnt(1)\n"
            "buffer_store_dwordx4 v[72:75], v115, %[rp0], s92 offen sc1\n"
            "s_waitcnt lgkmcnt(0)\n"
            "buffer_store_dwordx4 v[76:79], v115, %[rp0], s93 offen sc1\n"
            "v_mad_i32_i16 v118, v60, v117, v118 op_sel:[1,0,0,0]\n"
            "v_add_u32 v116, 4, v117\n"
            "v_mad_i32_i16 v118, v61, v116, v118 op_sel:[1,0,0,0]\n"
            "v_add_u32 v116, 8, v117\n"
            "v_mad_i32_i16 v118, v62, v116, v118 op_sel:[1,0,0,0]\n"
            "v_add_u32 v116, 12, v117\n"
            "v_mad_i32_i16 v118, v63, v116, v118 op_sel:[1,0,0,0]\n"
            "v_add_u32 v117, 16384, v117\n"
            "v_mad_i32_i16 v118, v64, v117, v118 op_sel:[1,0,0,0]\n"
            "v_add_u32 v116, 4, v117\n"
            "v_mad_i32_i16 v118, v65, v116, v118 op_sel:[1,0,0,0]\n"
            "v_add_u32 v116, 8, v117\n"
            "v_mad_i32_i16 v118, v66, v116, v118 op_sel:[1,0,0,0]\n"
            "v_add_u32 v116, 12, v117\n"
            "v_mad_i32_i16 v118, v67, v116, v118 op_sel:[1,0,0,0]\n"
            "v_add_u32 v117, 16384, v117\n"
            "v_mad_i32_i16 v118, v68, v117, v118 op_sel:[1,0,0,0]\n"
            "v_add_u32 v116, 4, v117\n"
            "v_mad_i32_i16 v118, v69, v116, v118 op_sel:[1,0,0,0]\n"
            "v_add_u32 v116, 8, v117\n"
            "v_mad_i32_i16 v118, v70, v116, v118 op_sel:[1,0,0,0]\n"
            "v_add_u32 v116, 12, v117\n"
            "v_mad_i32_i16 v118, v71, v116, v118 op_sel:[1,0,0,0]\n"
            "v_add_u32 v117, 16384, v117\n"
            "v_mad_i32_i16 v118, v72, v117, v118 op_sel:[1,0,0,0]\n"
            "v_add_u32 v116, 4, v117\n"
            "v_mad_i32_i16 v118, v73, v116, v118 op_sel:[1,0,0,0]\n"
            "v_add_u32 v116, 8, v117\n"
            "v_mad_i32_i16 v118, v74, v116, v118 op_sel:[1,0,0,0]\n"
            "v_add_u32 v116, 12, v117\n"
            "v_mad_i32_i16 v118, v75, v116, v118 op_sel:[1,0,0,0]\n"
            "v_add_u32 v117, 16384, v117\n"
            "v_mad_i32_i16 v118, v76, v117, v118 op_sel:[1,0,0,0]\n"
            "v_add_u32 v116, 4, v117\n"
            "v_mad_i32_i16 v118, v77, v116, v118 op_sel:[1,0,0,0]\n"
            "v_add_u32 v116, 8, v117\n"
            "v_mad_i32_i16 v118, v78, v116, v118 op_sel:[1,0,0,0]\n"
            "v_add_u32 v116, 12, v117\n"
            "v_mad_i32_i16 v118, v79, v116, v118 op_sel:[1,0,0,0]\n"
            "v_add_u32 v117, 16384, v117\n"
            "v_sub_u32 v118, v88, v118\n"
            "v_mov_b32 v116, %[wb]\n"
            "v_mov_b32 v117, %[bad]\n"
            "v_add_u32_dpp v118, v118, v118 row_shr:1 row_mask:0xf bank_mask:0xf bound_ctrl:1\n"
            "s_nop 1\n"
            "v_add_u32_dpp v118, v118, v118 row_shr:2 row_mask:0xf bank_mask:0xf bound_ctrl:1\n"
            "s_nop 1\n"
            "v_add_u32_dpp v118, v118, v118 row_shr:4 row_mask:0xf bank_mask:0xf bound_ctrl:1\n"
            "s_nop 1\n"
            "v_add_u32_dpp v118, v118, v118 row_shr:8 row_mask:0xf bank_mask:0xf bound_ctrl:1\n"
            "s_nop 1\n"
            "v_add_u32_dpp v118, v118, v118 row_bcast:15 row_mask:0xa bank_mask:0xf\n"
            "s_nop 1\n"
            "v_add_u32_dpp v118, v118, v118 row_bcast:31 row_mask:0xc bank_mask:0xf\n"
            "s_nop 1\n"
            "v_lshrrev_b32 v116, 7, v116\n"
            "s_mov_b32 exec_lo, 0\n s_mov_b32 exec_hi, 0x80000000\n"
            "ds_write2_b32 v116, v118, v117 offset1:1\n"
            "s_mov_b64 exec, -1\n"
            "s_waitcnt vmcnt(5)\n"
            "s_branch Lend2%=\n"
            "Lend%=:\n"
            "s_waitcnt vmcnt(0)\n"
            "Lend2%=:\n"
            "s_setprio 0\n"
            "s_waitcnt lgkmcnt(0)\n"

            : [mode] "=&s"(mode), [bad] "=&s"(bad), [exs] "=&v"(exs), [tid16] "+v"(t16), "+v"(tc2), "=&{v[40:43]}"(mm[0]), "=&{v[44:47]}"(mm[1]), "=&{v[48:51]}"(dd)
            : [rs] "s"(rs), [rm] "s"(rm), [rp0] "s"(rp0), [rt] "s"(rt), [i4] "s"(LMAX * 4), [c4] "s"(a_.c4), [pb] "s"(81920 + wave * 5120), [wb] "s"(wave * 1024), [q] "s"(q), [j0] "s"(J0),
              [rmm] "s"(rmm), [rd] "s"(rd), [e1] "s"(128 * 32 * 128 * 2), [ed] "v"((((wave * 8 + q) * 32 + b) * 128 + lane * 2) * 2),
              [fof] "s"((NT * NWS * 3 + cp * (P1_GRID * 2)) * 4), [dumu] "s"(ly.dumu),
              [tof] "v"((w * NT + t0) * 12), [fb] "v"(frag_unit(w, ly)), [xb] "v"((blk_line(w) + grp_line(qt)) * 8), [hm] "v"(slot_line((lane >> 4) * 64 + b) * 8)
            : "memory", "vcc", "scc", "m0", "s66", "s67", "s70", "s71", "s72", "s73", "s74", "s75", "s76", "s77", "s79", "s80", "s81", "s82",
              "s84", "s85", "s86", "s87", "s88", "s89", "s90", "s91", "s92", "s93", "s94", "s96", "s97",
              "v8", "v9", "v10", "v11", "v12", "v13", "v14", "v15", "v16", "v17", "v18", "v19", "v20", "v21", "v22", "v23", "v24", "v25", "v26", "v27", "v28", "v29", "v30", "v31", "v32", "v33", "v34", "v35", "v36", "v37", "v38", "v39", "v52", "v53", "v54", "v55", "v56", "v57", "v58", "v59", "v60", "v61", "v62", "v63", "v64", "v65", "v66", "v67", "v68", "v69", "v70", "v71", "v72", "v73", "v74", "v75", "v76", "v77", "v78", "v79", "v80", "v81", "v82", "v83", "v84", "v85", "v86", "v87", "v88", "v89", "v90", "v91", "v92", "v93", "v94", "v95", "v96", "v97", "v98", "v99", "v100", "v101", "v102", "v103", "v104", "v105", "v106", "v107", "v108", "v109", "v110", "v111", "v112", "v113", "v114", "v115", "v116", "v117", "v118");
    }
    const int tid2 = (int)(t16 >> 4);
    kaptr ka = (kaptr)__builtin_amdgcn_kernarg_segment_ptr();
    asm volatile("" : "+s"(ka));
    unsigned long long* wm = (unsigned long long*)(ka->wgmode + b * 32);
    const int E = ka->E;
    const unsigned nblk = ka->O >> 7;
    const int ln = tid2 & 63, wv = __builtin_amdgcn_readfirstlane(tid2 >> 6);
    const bool two = (16 + wv) * 8 + q < nblk;
    typedef float v2f __attribute__((ext_vector_type(2)));
    typedef const __attribute__((address_space(4))) float* cfp;
    cfp wt = (cfp)(ka->flags + FL_SIZE + cp * 128);
    v2f g[2][2][2], h[2][2][2];
    auto gh = [&](int i0, int i1) {
#pragma unroll
        for (int p = 0; p < 2; p++) {
            if (p == 0 || two) {
#pragma unroll
                for (int i = i0; i < i1; i++) {
                    const float d = i ? hi_bf(dd[p]) : lo_bf(dd[p]);
#pragma unroll
                    for (int r = 0; r < 8; r++) {
                        const float df = d - wt[r], rb = __builtin_amdgcn_exp2f((df * -14.426950408889634f) * df);
                        const v2f r2 = {rb, rb};
#pragma unroll
                        for (int k = 0; k < 2; k++) {
                            const v2f u = {wt[8 + r * 8 + k * 2], wt[9 + r * 8 + k * 2]}, v = {wt[12 + r * 8 + k * 2], wt[13 + r * 8 + k * 2]};
                            g[p][i][k] = r ? __builtin_elementwise_fma(r2, u, g[p][i][k]) : r2 * u;
                            h[p][i][k] = r ? __builtin_elementwise_fma(r2, v, h[p][i][k]) : r2 * v;
                        }
                    }
                }
            }
        }
    };
    auto odd = [&]() {
        if (E & 1) {
            const int e = ((wv * 8 + q) * 32 + b) * 128 + ln * 2;
            unsigned dv = ka->dist[E - 1];
            asm volatile("" : "+v"(dv));
            dd.x = e == E - 1 ? dv : dd.x; dd.y = e + 128 * 32 * 128 == E - 1 ? dv : dd.y;
        }
    };
    v2u own[2] = {{0, 0}, {0, 0}};
    if (mode == 0) {
        asm volatile("" : "+s"(wm));
        odd();
        gh(0, 1);
        own[0] = *(const v2u*)(lds + OBASE + (wv * 8 + q) * 128 + ln * 2);
        if (two) own[1] = *(const v2u*)(lds + OBASE + ((16 + wv) * 8 + q) * 128 + ln * 2);
        asm volatile("s_waitcnt vmcnt(0)" : "+v"(g[0][0][0]), "+v"(h[0][0][1]) :: "memory");
        __syncthreads();
        const v2u cs = *(const v2u*)(lds + (ln & 15) * 2);
        if (__builtin_amdgcn_readlane(row_scan_add(cs.x), 15) != 0 || __ballot(cs.y != 0)) mode = 1;
    }
    if (mode != 0) {
        acc_fallback(lds, mode, *ka, tid2);
        asm volatile("s_waitcnt vmcnt(0)" ::: "memory");
        __syncthreads();
        if (tid2 == P2_THREADS - 64) { __builtin_amdgcn_fence(__ATOMIC_RELEASE, "agent"); asm volatile("s_waitcnt vmcnt(0)" ::: "memory"); }
        const __amdgpu_buffer_rsrc_t rmm = __builtin_amdgcn_make_buffer_rsrc((void*)ka->m, (short)0, E * 8, 0x00020000);
        const __amdgpu_buffer_rsrc_t rdd = __builtin_amdgcn_make_buffer_rsrc((void*)ka->dist, (short)0, E * 2, 0x00020000);
        const unsigned e = ((wv * 8 + q) * 32 + b) * 128 + ln * 2, e1 = e + 128 * 32 * 128;
        mm[0] = __builtin_amdgcn_raw_buffer_load_b128(rmm, e * 8, 0, 0); mm[1] = __builtin_amdgcn_raw_buffer_load_b128(rmm, e1 * 8, 0, 0);
        dd.x = __builtin_amdgcn_raw_buffer_load_b32(rdd, e * 2, 0, 0); dd.y = __builtin_amdgcn_raw_buffer_load_b32(rdd, e1 * 2, 0, 0);
        asm volatile("" : "+v"(mm[0]), "+v"(mm[1]), "+v"(dd));
        odd();
        gh(0, 1);
        own[0].x = own[0].y = own[1].x = own[1].y = 0x8000u;
        if (mode == 2) exs = 0;
    }
    v2u t0 = {0, 0}, t1 = t0, t2 = t0;
    const v2u add = {1u | (mode ? 256u << q : 0u), qt == 0 ? ((unsigned)__builtin_amdgcn_readfirstlane(exs) & 0xFFFF) << (16 * cp) : 0u};
    if (tid2 == P2_THREADS - 64) asm volatile("s_nop 4\n global_atomic_add_x2 %0, %1, %2, %3 sc0" : "+v"(t0) : "v"(0), "v"(add), "s"(wm) : "memory");
    if (wv == 15) __builtin_amdgcn_s_setprio(3);
    asm volatile("" : "+s"(wt));
    gh(1, 2);
    asm volatile("" : "+v"(g[0][1][0]), "+v"(h[0][1][1]), "+s"(wt) :: "memory");
    float wu[16];
#pragma unroll
    for (int i = 0; i < 16; i++) wu[i] = wt[72 + i];
    const unsigned* prb = ka->praw + (size_t)(b * 8) * ka->O; unsigned short* outp = ka->out;
    int EP = ka->EP;
    asm volatile("" : "+s"(prb), "+s"(outp), "+s"(EP));
#pragma unroll
    for (int i = 0; i < 16; i++) asm volatile("" : "+s"(wu[i]));
    const __amdgpu_buffer_rsrc_t rpr = __builtin_amdgcn_make_buffer_rsrc((void*)prb, (short)0, (int)(nblk << 12), 0x00020000);
    const __amdgpu_buffer_rsrc_t rou = __builtin_amdgcn_make_buffer_rsrc((void*)outp, (short)0, E * 8, 0x00020000);
    unsigned po[8], vo = ((wv * 8 + q) * 128 + ln * 2) * 4;
#pragma unroll
    for (int j = 1; j < 8; j++) { po[j] = (j ^ q) * (nblk << 9); asm volatile("" : "+s"(po[j])); }
    asm volatile("" : "+v"(vo));
    if (wv == 15) {
        unsigned lo, hi, x, n;
#define LOOK(nw, od) "s_and_b32 %[x], %[lo], 255\n s_cmp_eq_u32 %[x], 8\n s_cbranch_scc1 2f\n global_load_dwordx2 %[" nw "], %[z], %[wm] sc1\n s_waitcnt vmcnt(2)\n" \
                     "v_mov_b64 v[126:127], %[" od "]\n s_nop 1\n v_readfirstlane_b32 %[lo], v126\n v_readfirstlane_b32 %[hi], v127\n"
        asm volatile("s_nop 4\n global_load_dwordx2 %[t1], %[z], %[wm] sc1\n s_sleep 4\n global_load_dwordx2 %[t2], %[z], %[wm] sc1\n s_waitcnt vmcnt(2)\n"
                     "v_mov_b64 v[126:127], %[t0]\n s_mov_b32 %[n], 0x200000\n s_nop 0\n v_readfirstlane_b32 %[lo], v126\n v_readfirstlane_b32 %[hi], v127\n"
                     "s_add_u32 %[lo], %[lo], %[a0]\n s_addc_u32 %[hi], %[hi], %[a1]\n"
                     "1:\n" LOOK("t0", "t1") LOOK("t1", "t2") LOOK("t2", "t0")
                     "s_sub_u32 %[n], %[n], 1\n s_cbranch_scc0 1b\n 2:\n"
                     : [lo] "=&s"(lo), [hi] "=&s"(hi), [x] "=&s"(x), [n] "=&s"(n), [t0] "+v"(t0), [t1] "+v"(t1), [t2] "+v"(t2)
                     : [z] "v"(0), [wm] "s"(wm), [a0] "s"(add.x), [a1] "s"(add.y) : "memory", "scc", "v126", "v127");
        if (lo & 0xFF00) { __builtin_amdgcn_fence(__ATOMIC_ACQUIRE, "agent"); asm volatile("s_waitcnt vmcnt(0)" ::: "memory"); }
        if (ln == 0) { lds[64] = (lo >> 8) & 255; lds[65] = hi; }
    }
    __syncthreads();
    v2u w[2][8];
#pragma unroll
    for (int p = 0; p < 2; p++) {
        w[p][0] = own[p];
        if (p == 0 || two) {
#pragma unroll
            for (int j = 1; j < 8; j++)
                w[p][j] = __builtin_amdgcn_raw_buffer_load_b64(rpr, vo + p * 65536, po[j], PR_AUX);
        }
    }
    const unsigned fbm = __builtin_amdgcn_readfirstlane(lds[64]);
    unsigned exw = __builtin_amdgcn_readfirstlane(lds[65]);
    if (cp) exw = (exw >> 16) | (exw << 16);
    v2f inv[2], c[2][2][2], mf[2][2][2];
    auto up = [&](const v2f* a, int d, v2f hh, v2f m0) {
        v2f s;
#pragma unroll
        for (int k = 0; k < 4; k++) {
            const v2f ak = {a[k >> 1][k & 1], a[k >> 1][k & 1]}, u = {wu[k * 4 + d * 2], wu[k * 4 + d * 2 + 1]};
            s = k ? __builtin_elementwise_fma(ak, u, s) : ak * u;
        }
        return __builtin_elementwise_fma(s, hh, m0);
    };
#pragma unroll
    for (int k = 0; k < 4; k++) inv[k >> 1][k & 1] = __uint_as_float(((exw >> (8 * k)) & 255) << 23) * (1.0f / 511.0f);
#pragma unroll
    for (int p = 0; p < 2; p++) {
        const unsigned blk = (p * 16 + wv) * 8 + q;
        if (p == 0 || two) {
#pragma unroll
            for (int i = 0; i < 4; i++) { mf[p][i >> 1][i & 1].x = lo_bf(mm[p][i]); mf[p][i >> 1][i & 1].y = hi_bf(mm[p][i]); }
            if (fbm && blk < nblk) {
                v2f x[2][2] = {{{0.f, 0.f}, {0.f, 0.f}}, {{0.f, 0.f}, {0.f, 0.f}}};
                const float* part = ka->part + (size_t)b * ka->O + blk * 128 + ln * 2;
#pragma unroll 1
                for (int j = 0; j < 8; j++)
                    if ((fbm >> j) & 1) {
#pragma unroll
                        for (int i = 0; i < 2; i++) {
                            const v2f y = {part[(size_t)(j * 2) * EP + i], part[(size_t)(j * 2 + 1) * EP + i]};
                            if ((j ^ cp) & 1) x[i][1] += y; else x[i][0] += y;
                        }
                    }
#pragma unroll
                for (int i = 0; i < 4; i++) x[i >> 1][i & 1] *= g[p][i >> 1][i & 1];
#pragma unroll
                for (int i = 0; i < 4; i++) mf[p][i >> 1][i & 1] = up(x[i >> 1], i & 1, h[p][i >> 1][i & 1], mf[p][i >> 1][i & 1]);
            }
#pragma unroll
            for (int i = 0; i < 2; i++)
#pragma unroll
                for (int k = 0; k < 2; k++) {
                    g[p][i][k] *= inv[k];
                    c[p][i][k].x = g[p][i][k].x * -131072.f; c[p][i][k].y = 0.f;
                    asm volatile("" : "+v"(g[p][i][k]), "+v"(c[p][i][k]), "+v"(mf[p][i][k]));
                }
        }
    }
#pragma unroll
    for (int p = 0; p < 2; p++) {
        const unsigned blk = (p * 16 + wv) * 8 + q;
        if (p == 0 || two) {
            v4u r;
#pragma unroll
            for (int i = 0; i < 2; i++) {
                v2f a[2];
#pragma unroll
                for (int k = 0; k < 2; k++) {
                    unsigned lo; int hi;
                    asm("v_add_u32_sdwa %0, %2, %3 dst_sel:DWORD dst_unused:UNUSED_PAD src0_sel:WORD_0 src1_sel:WORD_0\n"
                        "v_add_u32_sdwa %1, sext(%2), sext(%3) dst_sel:DWORD dst_unused:UNUSED_PAD src0_sel:WORD_1 src1_sel:WORD_1\n" : "=&v"(lo), "=&v"(hi) : "v"(w[p][k][i]), "v"(w[p][k + 2][i]));
#pragma unroll
                    for (int j = 4; j < 8; j += 2)
                        asm("v_add_u32_sdwa %0, %0, %2 dst_sel:DWORD dst_unused:UNUSED_PAD src0_sel:DWORD src1_sel:WORD_0\n"
                            "v_add_u32_sdwa %1, %1, sext(%2) dst_sel:DWORD dst_unused:UNUSED_PAD src0_sel:DWORD src1_sel:WORD_1\n" : "+v"(lo), "+v"(hi) : "v"(w[p][k + j][i]));
                    const v2f f = {(float)lo, (float)hi};
                    a[k] = __builtin_elementwise_fma(f, g[p][i][k], c[p][i][k]);
                }
#pragma unroll
                for (int d = 0; d < 2; d++) {
                    const v2f s = up(a, d, h[p][i][d], mf[p][i][d]);
                    asm("v_cvt_pk_bf16_f32 %0, %1, %2" : "=v"(r[i * 2 + d]) : "v"(s.x), "v"(s.y));
                }
            }
            __builtin_amdgcn_raw_buffer_store_b128(r, rou, ((blk * 32 + b) * 128 + ln * 2) * 8, 0, OUT_AUX);
        }
    }
    asm volatile("" :: "v"(t0), "v"(t1), "v"(t2));
}

extern "C" __global__ __launch_bounds__(256) void k_naive(const int* __restrict__ idx_in, const int* __restrict__ idx_out,
                                               const unsigned short* __restrict__ angle, const unsigned short* __restrict__ ang_freq,
                                               const unsigned short* __restrict__ m, const unsigned short* __restrict__ w_down,
                                               float* __restrict__ part, int T, int EP) {
    const float freq = bf2f(ang_freq[0]);
    for (long long t = (long long)blockIdx.x * 256 + threadIdx.x; t < T; t += (long long)gridDim.x * 256) {
        v2u mv = ((const v2u*)m)[idx_in[t]];
        float a[4] = {lo_bf(mv.x), hi_bf(mv.x), lo_bf(mv.y), hi_bf(mv.y)};
        float c = __cosf(freq * bf2f(angle[t]));
        int eo = idx_out[t];
#pragma unroll
        for (int d = 0; d < 4; d++) {
            float s = 0.f;
#pragma unroll
            for (int k = 0; k < 4; k++) s += a[k] * bf2f(w_down[k * 4 + d]);
            atomicAdd(&part[(size_t)d * EP + eo], s * c);
        }
    }
}

__global__ __launch_bounds__(256) void k_final(const unsigned short* __restrict__ m, const unsigned short* __restrict__ dist,
                                               const unsigned short* __restrict__ centers, const unsigned short* __restrict__ w_rbf,
                                               const unsigned short* __restrict__ w_sbf, const unsigned short* __restrict__ w_up,
                                               const float* __restrict__ part, unsigned short* __restrict__ out, int E, int EP) {
    const int e = blockIdx.x * 256 + threadIdx.x;
    if (e >= E) return;
    float a[4], g[4], h[4];
#pragma unroll
    for (int k = 0; k < 4; k++) a[k] = part[(size_t)k * EP + e];
    edge_gh(bf2f(dist[e]), centers, w_sbf, w_rbf, g, h);
    const v2u mv = ((const v2u*)m)[e];
    unsigned wu[8];
#pragma unroll
    for (int i = 0; i < 8; i++) wu[i] = ((cptr)w_up)[i];
    ((v2u*)out)[e] = edge_out(a, g, h, mv.x, mv.y, wu);
}

extern "C" void kernel_launch(void* const* d_in, const int* in_sizes, int n_in,
                              void* d_out, int out_size, void* d_ws, size_t ws_size,
                              hipStream_t stream) {
    const unsigned short* m = (const unsigned short*)d_in[0];
    const int* idx_in = (const int*)d_in[1];
    const int* idx_out = (const int*)d_in[2];
    const unsigned short* dist = (const unsigned short*)d_in[3];
    const unsigned short* angle = (const unsigned short*)d_in[4];
    const unsigned short* centers = (const unsigned short*)d_in[5];
    const unsigned short* ang_freq = (const unsigned short*)d_in[6];
    const unsigned short* w_rbf = (const unsigned short*)d_in[7];
    const unsigned short* w_sbf = (const unsigned short*)d_in[8];
    const unsigned short* w_down = (const unsigned short*)d_in[9];
    const unsigned short* w_up = (const unsigned short*)d_in[10];
    const int E = in_sizes[0] / 4;
    const int T = in_sizes[1];
    const int NR = T / (P1_GRID * CH);
    const int per = ((T - NR * (P1_GRID * CH) + P1_GRID - 1) / P1_GRID + 3) / 4 * 4;
    const unsigned O = (unsigned)(((E + 127) / 128 + 31) / 32) * 128;
    const unsigned I = (unsigned)(E + 31) / 32;
    const int EP = (int)O * 32;
    Lay ly;
    const unsigned meanr = (unsigned)(((size_t)NR * CH + per) / NT);
    ly.K0 = meanr >= 30 ? (meanr - 30) / 24 : 0;
    ly.KX = ly.K0 + ly.K0 * 2 / 3 + 4;
#define FCAP 4928
    ly.fcap = meanr >= 100 ? FCAP : 8192;
    const size_t lines = (size_t)PLANE * (ly.KX + 1);
    char* ws = (char*)d_ws;
    size_t o = 0;
    unsigned* praw = (unsigned*)(ws + o); o += (size_t)8 * EP * 4; o = (o + 255) / 256 * 256;
    unsigned* wgmode = (unsigned*)(ws + o); o += 16384;
    unsigned* mdp = (unsigned*)(ws + o); o += (size_t)2 * 32 * LMAX * 4;
    unsigned* tab = (unsigned*)(ws + o); o += (size_t)NT * NWS * 12;
    unsigned* flags = (unsigned*)(ws + o); o += FL_SIZE * 4 + 1024; o = (o + 255) / 256 * 256;
    o += (size_t)NWS * ly.fcap * 16;
    char* rec = ws + o;
    ly.dumu = (unsigned)(lines * 8);
    o += 0x200000 + ((size_t)ly.dumu + 64) * 16 - (size_t)PLANE * 128;
    float* part = (float*)(ws + o); o += (size_t)16 * EP * 4;
    const bool ok = E >= 16384 && O <= LMAX && I <= LMAX && T > 0 && T < (1 << 29) && ly.KX <= 30 && o <= ws_size;
    if (!ok) {
        part = (float*)ws;
        const int EQ = (E + 3) / 4 * 4;
        (void)hipMemsetAsync(part, 0, (size_t)4 * EQ * 4, stream);
        if (T > 0) k_naive<<<2048, 256, 0, stream>>>(idx_in, idx_out, angle, ang_freq, m, w_down, part, T, EQ);
        k_final<<<(E + 255) / 256, 256, 0, stream>>>(m, dist, centers, w_rbf, w_sbf, w_up, part, (unsigned short*)d_out, E, EQ);
        return;
    }
    k_bin<<<P1_GRID, P1_THREADS, 0, stream>>>(idx_in, idx_out, angle, ang_freq, m, w_down, mdp, E, T, rec, tab, flags, NR, per, ly, wgmode, centers, w_sbf, w_rbf, w_up);
    AccArgs aa;
    aa.rec = rec; aa.tab = tab; aa.flags = flags; aa.mdp = mdp; aa.part = part; aa.praw = praw; aa.wgmode = wgmode;
    aa.idx_in = idx_in; aa.idx_out = idx_out; aa.angle = angle; aa.ang_freq = ang_freq; aa.m = m; aa.w_down = w_down;
    aa.EP = EP; aa.O = O; aa.T = T; aa.c4 = (float)((double)E / 144.0);
    aa.dist = dist; aa.centers = centers; aa.w_rbf = w_rbf; aa.w_sbf = w_sbf; aa.w_up = w_up; aa.out = (unsigned short*)d_out; aa.E = E; aa.unused = 0;
    aa.ly = ly;
    k_acc<<<256, P2_THREADS, 0, stream>>>(aa);
}
```
